```python
import torch

def _softmax(x, axis=-1):
    return torch.softmax(x, dim=axis)
def _gelu(x):
    return torch.nn.functional.gelu(x, approximate="tanh")
def _silu(x):
    return torch.nn.functional.silu(x)
def _sigmoid(x):
    return torch.sigmoid(x)
def _layernorm(x, axis=-1, eps=1e-5):
    m = x.mean(dim=axis, keepdim=True)
    v = x.var(dim=axis, keepdim=True, unbiased=False)
    return (x - m) / torch.sqrt(v + eps)
def _scatter_add(tgt, idx, src, axis=0):
    return tgt.clone().index_add_(axis, idx, src)
def _segment_sum(src, seg, n):
    out = torch.zeros((n,) + src.shape[1:], dtype=src.dtype)
    return out.index_add_(0, seg, src)
def _pad(x, pw):
    flat = tuple(v for pair in reversed(pw) for v in pair)
    return torch.nn.functional.pad(x, flat)

kernel_name = 'gated_mix__softmax_full_mha__dense_activation_mish__prompt'

def setup_inputs():

    _w = (lambda a: (lambda t: t.to(torch.bfloat16) if t.is_floating_point() else t)(torch.as_tensor(a)))
    def _A_setup():
        B, S, D = 1, 2048, 512
        return {
            "x": _w((lambda *s: torch.randn(*s, dtype=torch.bfloat16) * 0.05)(B, S, D)),
            "wqkv": _w((lambda *s: torch.randn(*s, dtype=torch.bfloat16) * 0.8)(D, 3 * D)),
            "wo": _w((lambda *s: torch.randn(*s, dtype=torch.bfloat16) * 0.05)(D, D)),
        }
    _A_in = _A_setup()

    def _B_setup():
        B, S, D, F = 1, 2048, 512, 1376
        return {
            "x": _w((lambda *s: torch.randn(*s, dtype=torch.bfloat16) * 0.05)(B, S, D)),
            "w1": _w((lambda *s: torch.randn(*s, dtype=torch.bfloat16) * 12.8)(D, F) * (D ** -0.5)),
            "b1": _w((lambda *s: torch.randn(*s, dtype=torch.bfloat16) * 0.2)(F)),
            "w2": _w((lambda *s: torch.randn(*s, dtype=torch.bfloat16) * 0.1)(F, D) * (F ** -0.5)),
        }
    _B_in = _B_setup()

    return {
        "A_x": _A_in["x"],
        "A_wqkv": _A_in["wqkv"],
        "A_wo": _A_in["wo"],
        "B_x": _B_in["x"],
        "B_w1": _B_in["w1"],
        "B_b1": _B_in["b1"],
        "B_w2": _B_in["w2"],
        "gate_w": _w((lambda *s: torch.randn(*s, dtype=torch.bfloat16) * 0.05)(512) * 16.0),
    }

def reference(A_x, A_wqkv, A_wo, B_x, B_w1, B_b1, B_w2, gate_w):
    def _A_ref(x, wqkv, wo):
        B, S, D = x.shape
        H = 32; Dh = D // H
        qkv = (lambda x, *d: x.permute(*d))((x @ wqkv).reshape(B, S, 3, H, Dh), 2, 0, 3, 1, 4)
        q, k, v = qkv[0], qkv[1], qkv[2]
        scale = Dh ** -0.5
        scores = torch.einsum("bhqd,bhkd->bhqk", q, k) * scale
        p = _softmax(scores, axis=-1)
        o = torch.einsum("bhqk,bhkd->bhqd", p, v)
        return (lambda x, *d: x.permute(*d))(o, 0, 2, 1, 3).reshape(B, S, D) @ wo

    def _B_ref(x, w1, b1, w2):
        h = torch.einsum("bsd,df->bsf", x, w1) + b1
        sp = torch.log1p(torch.exp(-torch.abs(h))) + torch.where(h > 0, h, torch.zeros_like(h))
        act = h * torch.tanh(sp)
        return torch.einsum("bsf,fd->bsd", act, w2)

    x = A_x
    g = _sigmoid((x * gate_w).sum(-1, keepdims=True))
    a = _A_ref(x, A_wqkv, A_wo)
    b = _B_ref(x, B_w1, B_b1, B_w2)
    return g * a + (1 - g) * b
```

```cpp
#include <hip/hip_runtime.h>
#include <hip/hip_bf16.h>
#include <stdint.h>

typedef short bf16x8 __attribute__((ext_vector_type(8)));
typedef short s16x4 __attribute__((ext_vector_type(4)));
typedef float f32x16 __attribute__((ext_vector_type(16)));
typedef float f32x2 __attribute__((ext_vector_type(2)));
typedef __bf16 bf16x2_t __attribute__((ext_vector_type(2)));
typedef unsigned u32x4 __attribute__((ext_vector_type(4)));
typedef unsigned u32x2 __attribute__((ext_vector_type(2)));
typedef unsigned short u16;
#define DI __device__ __forceinline__
#define MFMA(a, b, c) __builtin_amdgcn_mfma_f32_32x32x16_bf16((a), (b), (c), 0, 0, 0)
#define LAS __attribute__((address_space(3)))

constexpr int S_ = 2048, D_ = 512, H_ = 32, F_ = 1376;
constexpr size_t WS_Q = 0, WS_K = 2u << 20, WS_V = 4u << 20, WS_ACT = 6u << 20, WS_O = 12u << 20, WS_W2P = 14u << 20,
                 WS_WOP = 16u << 20, WS_GATE = 17u << 20, WS_KMAX = (17u << 20) + 16384;

DI unsigned pk2(float a, float b) {
  f32x2 v = {a, b};
  bf16x2_t r = __builtin_convertvector(v, bf16x2_t);
  return __builtin_bit_cast(unsigned, r);
}
DI float bf_lo(unsigned u) { return __uint_as_float(u << 16); }
DI float bf_hi(unsigned u) { return __uint_as_float(u & 0xffff0000u); }
DI float rbf(float x) { return bf_lo(pk2(x, 0.f)); }
DI void glds16(const void* g, char* l) {
  __builtin_amdgcn_global_load_lds((const unsigned*)g, (LAS unsigned*)l, 16, 0, 0);
}
DI void glds4(const void* g, char* l) {
  __builtin_amdgcn_global_load_lds((const unsigned*)g, (LAS unsigned*)l, 4, 0, 0);
}
DI s16x4 ldtr(const char* l) {
  typedef short v4i16_t __attribute__((ext_vector_type(4)));
  return __builtin_amdgcn_ds_read_tr16_b64_v4i16((LAS v4i16_t*)l);
}
#define WAIT_V(n) asm volatile("s_waitcnt vmcnt(" #n ")" ::: "memory")
#define BAR() __builtin_amdgcn_s_barrier()

DI float mish_f(float h) {
  float t = fminf(h * 1.4426950408889634f, 60.f);
  float w = __builtin_amdgcn_exp2f(t);
  float a = w + 2.f;
  float u = w * a;
  float d = __builtin_fmaf(w, a, 2.f);
  return h * u * __builtin_amdgcn_rcpf(d);
}

__global__ __launch_bounds__(256) void k1(const u16* __restrict__ x, const u16* __restrict__ wqkv,
                                          const u16* __restrict__ wo, const u16* __restrict__ w1,
                                          const u16* __restrict__ b1, const u16* __restrict__ w2,
                                          const u16* __restrict__ gw, char* __restrict__ ws) {
  __shared__ __attribute__((aligned(16))) char lds[3 * 40960];
  const int bid = blockIdx.x, xcd = bid & 7, idx = bid >> 3;
  const int rb = (xcd & 1) * 8 + (idx & 7), j = (xcd >> 1) * 4 + (idx >> 3);
  const int tid = threadIdx.x, lane = tid & 63;
  const int w = __builtin_amdgcn_readfirstlane(tid >> 6);
  const int r = lane & 31, h = lane >> 5;

  const u16* xsrc0 = x + (size_t)(rb * 128 + 32 * w + (lane >> 3)) * D_ + (((lane & 7) ^ ((lane >> 4) & 7)) * 8);
  const u16* xsrc1 = x + (size_t)(rb * 128 + 32 * w + 8 + (lane >> 3)) * D_ + (((lane & 7) ^ ((4 + (lane >> 4)) & 7)) * 8);
  const u16* wq_src = wqkv + (size_t)(16 * w + (lane >> 2)) * 1536 + (lane & 3) * 8;
  const u16* w1_src = w1 + (size_t)(16 * w + (lane >> 2)) * F_ + (lane & 3) * 8;
  int fb0 = 3 * j, fb1 = 3 * j + 1, fb2 = 3 * j + 2;
  const bool fv0 = fb0 < 43, fv1 = fb1 < 43, fv2 = fb2 < 43;
  fb0 = fv0 ? fb0 : 42; fb1 = fv1 ? fb1 : 42; fb2 = fv2 ? fb2 : 42;

#define STAGE(kt, sb)                                                                             \
  do {                                                                                            \
    char* xl = lds + (sb) * 16384 + w * 4096;                                                     \
    glds16(xsrc0 + (kt) * 64, xl);                                                                \
    glds16(xsrc1 + (kt) * 64, xl + 1024);                                                         \
    glds16(xsrc0 + (kt) * 64 + 16 * D_, xl + 2048);                                               \
    glds16(xsrc1 + (kt) * 64 + 16 * D_, xl + 3072);                                               \
    char* wl = lds + 49152 + (sb) * 24576 + w * 1024;                                             \
    const u16* a_ = wq_src + (size_t)(kt) * 64 * 1536;                                            \
    const u16* b_ = w1_src + (size_t)(kt) * 64 * F_;                                              \
    glds16(a_ + 64 * j, wl);                                                                      \
    glds16(a_ + 64 * j + 32, wl + 4096);                                                          \
    glds16(a_ + 1024 + 32 * j, wl + 8192);                                                        \
    glds16(b_ + 32 * fb0, wl + 12288);                                                            \
    glds16(b_ + 32 * fb1, wl + 16384);                                                            \
    glds16(b_ + 32 * fb2, wl + 20480);                                                            \
  } while (0)

  f32x16 acc[6];
#pragma unroll
  for (int b = 0; b < 6; ++b)
#pragma unroll
    for (int i = 0; i < 16; ++i) acc[b][i] = 0.f;

  const int sw = (r >> 1) & 7;
  const int xrd = w * 4096 + r * 128;
  const int q4 = (lane & 15) >> 2, p4 = lane & 3, grp = (lane >> 4) & 1;
  const int wrd = 49152 + (8 * h + q4) * 64 + grp * 32 + p4 * 8;

  STAGE(0, 0);
  STAGE(1, 1);

  u32x2 bia[3][4];
  u32x4 gx[2], gg;
  u16 pw[2][8];

#pragma unroll
  for (int kt = 0; kt < 8; ++kt) {
    const int sb = kt % 3;
    if (kt < 7) WAIT_V(10); else WAIT_V(0);
    BAR();
    if (kt + 2 < 8) STAGE(kt + 2, (kt + 2) % 3);
    if (kt == 7) {
#pragma unroll
      for (int g = 0; g < 4; ++g) {
        bia[0][g] = *(const u32x2*)(b1 + 32 * fb0 + 8 * g + 4 * h);
        bia[1][g] = *(const u32x2*)(b1 + 32 * fb1 + 8 * g + 4 * h);
        bia[2][g] = *(const u32x2*)(b1 + 32 * fb2 + 8 * g + 4 * h);
      }
      const int tok = rb * 128 + j * 8 + 2 * w;
      gx[0] = *(const u32x4*)(x + (size_t)tok * D_ + lane * 8);
      gx[1] = *(const u32x4*)(x + (size_t)(tok + 1) * D_ + lane * 8);
      gg = *(const u32x4*)(gw + lane * 8);
#pragma unroll
      for (int c = 0; c < 2; ++c) {
        int loc = tid + c * 256;
        loc = loc < 472 ? loc : 471;
        int ch = bid * 472 + loc;
        const u16* src = w2;
        if (ch >= 88064) { ch -= 88064; src = wo; }
        const int kb = ch >> 10, nb = (ch >> 6) & 15, l2 = ch & 63;
        const u16* p = src + (size_t)(16 * kb + 4 * (l2 >> 5)) * D_ + 32 * nb + (l2 & 31);
#pragma unroll
        for (int e = 0; e < 8; ++e) pw[c][e] = p[(8 * (e >> 2) + (e & 3)) * D_];
      }
    }
#pragma unroll
    for (int ks = 0; ks < 4; ++ks) {
      const bf16x8 xf = *(const bf16x8*)(lds + sb * 16384 + xrd + (((2 * ks + h) ^ sw) * 16));
#pragma unroll
      for (int b = 0; b < 6; ++b) {
        const char* wp = lds + wrd + sb * 24576 + b * 4096 + ks * 1024;
        const s16x4 lo = ldtr(wp), hi = ldtr(wp + 256);
        const bf16x8 wf = __builtin_shufflevector(lo, hi, 0, 1, 2, 3, 4, 5, 6, 7);
        if (b == 2) acc[b] = MFMA(xf, wf, acc[b]);
        else acc[b] = MFMA(wf, xf, acc[b]);
      }
    }
  }
#undef STAGE

  const int tile = rb * 4 + w;
#pragma unroll
  for (int b = 0; b < 2; ++b) {
    const int c0 = 64 * j + 32 * b;
    const bool isq = c0 < 512;
    const float sc = isq ? 0.36067376022224085f : 1.0f;
    const int head0 = (c0 & 511) >> 4;
    char* dst = ws + (isq ? WS_Q : WS_K);
#pragma unroll
    for (int hh = 0; hh < 2; ++hh) {
      u32x4 o;
#pragma unroll
      for (int e = 0; e < 4; ++e) o[e] = pk2(acc[b][8 * hh + 2 * e] * sc, acc[b][8 * hh + 2 * e + 1] * sc);
      *(u32x4*)(dst + ((size_t)((head0 + hh) * 64 + tile) * 64 + lane) * 16) = o;
      if (!isq) {
        float ss = 0.f;
#pragma unroll
        for (int e = 0; e < 4; ++e) {
          const float a = bf_lo(o[e]), c = bf_hi(o[e]);
          ss = __builtin_fmaf(a, a, ss);
          ss = __builtin_fmaf(c, c, ss);
        }
        ss += __shfl_xor(ss, 32);
#pragma unroll
        for (int m = 16; m >= 1; m >>= 1) ss = fmaxf(ss, __shfl_xor(ss, m));
        if (lane == 0) ((float*)(ws + WS_KMAX))[(head0 + hh) * 64 + tile] = ss;
      }
    }
  }
  {
    const int head = 2 * j + (r >> 4), d = r & 15;
#pragma unroll
    for (int s = 0; s < 2; ++s) {
      u32x4 o;
#pragma unroll
      for (int e = 0; e < 4; ++e) o[e] = pk2(acc[2][8 * s + 2 * e], acc[2][8 * s + 2 * e + 1]);
      *(u32x4*)(ws + WS_V + (size_t)(head * 64 + tile) * 1024 + s * 512 + h * 256 + d * 16) = o;
    }
  }
#pragma unroll
  for (int b = 0; b < 3; ++b) {
    const bool fv = b == 0 ? fv0 : (b == 1 ? fv1 : fv2);
    const int fb = b == 0 ? fb0 : (b == 1 ? fb1 : fb2);
    if (fv) {
#pragma unroll
      for (int hh = 0; hh < 2; ++hh) {
        u32x4 o;
#pragma unroll
        for (int gq = 0; gq < 2; ++gq) {
          const int g = 2 * hh + gq;
          const u32x2 bb = bia[b][g];
          const float h0 = acc[3 + b][4 * g + 0] + bf_lo(bb[0]);
          const float h1 = acc[3 + b][4 * g + 1] + bf_hi(bb[0]);
          const float h2 = acc[3 + b][4 * g + 2] + bf_lo(bb[1]);
          const float h3 = acc[3 + b][4 * g + 3] + bf_hi(bb[1]);
          o[2 * gq] = pk2(mish_f(h0), mish_f(h1));
          o[2 * gq + 1] = pk2(mish_f(h2), mish_f(h3));
        }
        *(u32x4*)(ws + WS_ACT + ((size_t)((2 * fb + hh) * 64 + tile) * 64 + lane) * 16) = o;
      }
    }
  }
#pragma unroll
  for (int t = 0; t < 2; ++t) {
    float s = 0.f;
#pragma unroll
    for (int e = 0; e < 4; ++e) {
      s += rbf(bf_lo(gx[t][e]) * bf_lo(gg[e]));
      s += rbf(bf_hi(gx[t][e]) * bf_hi(gg[e]));
    }
#pragma unroll
    for (int m = 32; m >= 1; m >>= 1) s += __shfl_xor(s, m);
    s = rbf(s);
    const float g = rbf(__builtin_amdgcn_rcpf(1.f + __builtin_amdgcn_exp2f(-s * 1.4426950408889634f)));
    if (lane == 0) ((float*)(ws + WS_GATE))[rb * 128 + j * 8 + 2 * w + t] = g;
  }
#pragma unroll
  for (int c = 0; c < 2; ++c) {
    const int loc = tid + c * 256;
    if (loc < 472) {
      const int ch = bid * 472 + loc;
      u32x4 o;
#pragma unroll
      for (int e = 0; e < 4; ++e) o[e] = (unsigned)pw[c][2 * e] | ((unsigned)pw[c][2 * e + 1] << 16);
      char* dst = ch >= 88064 ? ws + WS_WOP + (size_t)(ch - 88064) * 16 : ws + WS_W2P + (size_t)ch * 16;
      *(u32x4*)dst = o;
    }
  }
}

__global__ __launch_bounds__(512) void k2(char* __restrict__ ws) {
  __shared__ __attribute__((aligned(16))) char lds[131072 + 8192 + 2048];
  const int bid = blockIdx.x, xcd = bid & 7, idx = bid >> 3;
  const int head = xcd * 4 + (idx & 3), qb = idx >> 2;
  const int tid = threadIdx.x, lane = tid & 63;
  const int w = __builtin_amdgcn_readfirstlane(tid >> 6);
  const int h = lane >> 5;
  const int qt = qb * 8 + w;

  glds16(ws + WS_Q + ((size_t)(head * 64 + qt) * 64 + lane) * 16, lds + 131072 + w * 1024);
  glds4(ws + WS_KMAX + (size_t)(head * 64 + lane) * 4, lds + 131072 + 8192 + w * 256);
  {
    const char* src = ws + (w < 4 ? WS_K : WS_V) + (size_t)(head * 64 + (w & 3)) * 1024 + lane * 16;
    char* dst = lds + (w < 4 ? 0 : 65536) + (w & 3) * 1024;
#pragma unroll
    for (int c = 0; c < 16; ++c) glds16(src + c * 4096, dst + c * 4096);
  }
  WAIT_V(16);
  const bf16x8 qf = *(const bf16x8*)(lds + 131072 + w * 1024 + lane * 16);
  float km = *(const float*)(lds + 131072 + 8192 + w * 256 + lane * 4);
#pragma unroll
  for (int m = 32; m >= 1; m >>= 1) km = fmaxf(km, __shfl_xor(km, m));
  float qs = 0.f;
  {
    const u32x4 qu = __builtin_bit_cast(u32x4, qf);
#pragma unroll
    for (int e = 0; e < 4; ++e) {
      const float a = bf_lo(qu[e]), c = bf_hi(qu[e]);
      qs = __builtin_fmaf(a, a, qs);
      qs = __builtin_fmaf(c, c, qs);
    }
    qs += __shfl_xor(qs, 32);
  }
  const float mb = __builtin_amdgcn_sqrtf(qs * km) * 1.0001f;
  f32x16 negm, O;
#pragma unroll
  for (int i = 0; i < 16; ++i) { negm[i] = -mb; O[i] = 0.f; }

  const char* kp = lds + lane * 16;
  const char* vp = lds + 65536 + h * 256 + (lane & 15) * 16;
  const bool vact = (lane & 16) == 0;
  bf16x8 va0, va1, vb0, vb1;
#pragma unroll
  for (int i = 0; i < 8; ++i) { va0[i] = 0x3F80; va1[i] = 0x3F80; vb0[i] = 0x3F80; vb1[i] = 0x3F80; }

#define SOFTMAX_PV(Sx, v0, v1)                                                                     \
  do {                                                                                             \
    u32x4 p0, p1;                                                                                  \
    _Pragma("unroll") for (int e = 0; e < 4; ++e) {                                                \
      p0[e] = pk2(__builtin_amdgcn_exp2f(Sx[2 * e]), __builtin_amdgcn_exp2f(Sx[2 * e + 1]));       \
      p1[e] = pk2(__builtin_amdgcn_exp2f(Sx[8 + 2 * e]), __builtin_amdgcn_exp2f(Sx[8 + 2 * e + 1])); \
    }                                                                                              \
    O = MFMA(v0, __builtin_bit_cast(bf16x8, p0), O);                                               \
    O = MFMA(v1, __builtin_bit_cast(bf16x8, p1), O);                                               \
  } while (0)

#define SEGMENT(t0, t1)                                                                            \
  do {                                                                                             \
    f32x16 Sa = MFMA(*(const bf16x8*)(kp + (t0) * 1024), qf, negm), Sb;                            \
    for (int t = (t0); t < (t1); t += 2) {                                                         \
      const bf16x8 k1f = *(const bf16x8*)(kp + (t + 1) * 1024);                                    \
      if (vact) { va0 = *(const bf16x8*)(vp + t * 1024); va1 = *(const bf16x8*)(vp + t * 1024 + 512); } \
      Sb = MFMA(k1f, qf, negm);                                                                    \
      SOFTMAX_PV(Sa, va0, va1);                                                                    \
      const int tn = (t + 2 < (t1)) ? t + 2 : t + 1;                                               \
      const bf16x8 k2f = *(const bf16x8*)(kp + tn * 1024);                                         \
      if (vact) { vb0 = *(const bf16x8*)(vp + (t + 1) * 1024); vb1 = *(const bf16x8*)(vp + (t + 1) * 1024 + 512); } \
      Sa = MFMA(k2f, qf, negm);                                                                    \
      SOFTMAX_PV(Sb, vb0, vb1);                                                                    \
    }                                                                                              \
  } while (0)

  WAIT_V(15); BAR();
  SEGMENT(0, 4);
  WAIT_V(12); BAR();
  SEGMENT(4, 16);
  WAIT_V(0); BAR();
  SEGMENT(16, 64);

  float l = O[8];
  if (__any(!(l >= 8.271806125530277e-25f))) {
    float m = -INFINITY;
    l = 0.f;
    f32x16 zero;
#pragma unroll
    for (int i = 0; i < 16; ++i) { zero[i] = 0.f; O[i] = 0.f; }
    for (int t = 0; t < 64; ++t) {
      f32x16 Sx = MFMA(*(const bf16x8*)(kp + t * 1024), qf, zero);
      float mx = Sx[0];
#pragma unroll
      for (int i = 1; i < 16; ++i) mx = fmaxf(mx, Sx[i]);
      mx = fmaxf(mx, __shfl_xor(mx, 32));
      const float mn = fmaxf(m, mx);
      const float alpha = __builtin_amdgcn_exp2f(m - mn);
      m = mn;
#pragma unroll
      for (int i = 0; i < 16; ++i) { O[i] *= alpha; Sx[i] -= mn; }
      if (vact) { va0 = *(const bf16x8*)(vp + t * 1024); va1 = *(const bf16x8*)(vp + t * 1024 + 512); }
      SOFTMAX_PV(Sx, va0, va1);
    }
    l = O[8];
  }
  const float rl = 1.0f / l;
  u32x4 o;
#pragma unroll
  for (int e = 0; e < 4; ++e) o[e] = pk2(O[2 * e] * rl, O[2 * e + 1] * rl);
  *(u32x4*)(ws + WS_O + ((size_t)(head * 64 + qt) * 64 + lane) * 16) = o;
}

__global__ __launch_bounds__(256) void k3(const char* __restrict__ ws, u16* __restrict__ out) {
  const int bid = blockIdx.x, xcd = bid & 7, idx = bid >> 3;
  const int rb = (xcd & 3) * 8 + (idx & 7), cb = (xcd >> 2) * 4 + (idx >> 3);
  const int tid = threadIdx.x, lane = tid & 63;
  const int w = __builtin_amdgcn_readfirstlane(tid >> 6);
  const int r = lane & 31, h = lane >> 5;
  const int tile = rb * 2 + (w & 1), nb = cb * 2 + (w >> 1);
  f32x16 a, b;
#pragma unroll
  for (int i = 0; i < 16; ++i) { a[i] = 0.f; b[i] = 0.f; }
  {
    const char* op = ws + WS_O + (size_t)tile * 1024 + lane * 16;
    const char* wp = ws + WS_WOP + (size_t)nb * 1024 + lane * 16;
#pragma unroll 8
    for (int kb = 0; kb < 32; ++kb) {
      const bf16x8 of = *(const bf16x8*)(op + (size_t)kb * 65536);
      const bf16x8 wf = *(const bf16x8*)(wp + (size_t)kb * 16384);
      a = MFMA(wf, of, a);
    }
  }
  {
    const char* ap = ws + WS_ACT + (size_t)tile * 1024 + lane * 16;
    const char* wp = ws + WS_W2P + (size_t)nb * 1024 + lane * 16;
#pragma unroll 2
    for (int kb0 = 0; kb0 < 86; kb0 += 2) {
#pragma unroll
      for (int u = 0; u < 2; ++u) {
        const int kb = kb0 + u;
        const bf16x8 af = *(const bf16x8*)(ap + (size_t)kb * 65536);
        const bf16x8 wf = *(const bf16x8*)(wp + (size_t)kb * 16384);
        b = MFMA(wf, af, b);
      }
    }
  }
  const int tok = tile * 32 + r;
  const float g = ((const float*)(ws + WS_GATE))[tok];
  const float g1 = rbf(1.f - g);
#pragma unroll
  for (int q = 0; q < 4; ++q) {
    u32x2 o;
    o[0] = pk2(g * a[4 * q] + g1 * b[4 * q], g * a[4 * q + 1] + g1 * b[4 * q + 1]);
    o[1] = pk2(g * a[4 * q + 2] + g1 * b[4 * q + 2], g * a[4 * q + 3] + g1 * b[4 * q + 3]);
    *(u32x2*)(out + (size_t)tok * D_ + nb * 32 + 8 * q + 4 * h) = o;
  }
}

extern "C" void kernel_launch(void* const* d_in, const int* in_sizes, int n_in, void* d_out, int out_size,
                              void* d_ws, size_t ws_size, hipStream_t stream) {
  const u16* x = (const u16*)d_in[0];
  const u16* wqkv = (const u16*)d_in[1];
  const u16* wo = (const u16*)d_in[2];
  const u16* w1 = (const u16*)d_in[4];
  const u16* b1 = (const u16*)d_in[5];
  const u16* w2 = (const u16*)d_in[6];
  const u16* gw = (const u16*)d_in[7];
  char* ws = (char*)d_ws;
  k1<<<256, 256, 0, stream>>>(x, wqkv, wo, w1, b1, w2, gw, ws);
  k2<<<256, 512, 0, stream>>>(ws);
  k3<<<256, 256, 0, stream>>>(ws, (u16*)d_out);
}
```

```cpp
#include <hip/hip_runtime.h>
#include <hip/hip_bf16.h>
#include <stdint.h>

typedef short bf16x8 __attribute__((ext_vector_type(8)));
typedef short s16x4 __attribute__((ext_vector_type(4)));
typedef float f32x16 __attribute__((ext_vector_type(16)));
typedef float f32x4 __attribute__((ext_vector_type(4)));
typedef float f32x2 __attribute__((ext_vector_type(2)));
typedef __bf16 bf16x2_t __attribute__((ext_vector_type(2)));
typedef unsigned u32x4 __attribute__((ext_vector_type(4)));
typedef unsigned u32x2 __attribute__((ext_vector_type(2)));
typedef unsigned short u16;
#define DI __device__ __forceinline__
#define MFMA(a, b, c) __builtin_amdgcn_mfma_f32_32x32x16_bf16((a), (b), (c), 0, 0, 0)
#define LAS __attribute__((address_space(3)))

#define KSTAG ((idx + (idx >> 3)) & 7)
#define PACE 5
#define LD_PRIO 0
#define FFN_PRIO 0
constexpr int S_ = 2048, D_ = 512, H_ = 32, F_ = 1376;
constexpr size_t WS_Q = 0, WS_K = 2u << 20, WS_V = 4u << 20, WS_ACT = 6u << 20, WS_O = 12u << 20, WS_W2P = 14u << 20,
                 WS_WOP = 16u << 20, WS_GATE = 17u << 20, WS_KMAX = (17u << 20) + 16384, WS_B = 18u << 20;

DI unsigned pk2(float a, float b) {
  f32x2 v = {a, b};
  bf16x2_t r = __builtin_convertvector(v, bf16x2_t);
  return __builtin_bit_cast(unsigned, r);
}
DI float bf_lo(unsigned u) { return __uint_as_float(u << 16); }
DI float bf_hi(unsigned u) { return __uint_as_float(u & 0xffff0000u); }
DI float rbf(float x) { return bf_lo(pk2(x, 0.f)); }
DI void gldsa16(const void* sbase, unsigned voff, unsigned dst) {
  unsigned keep;
  asm volatile("s_mov_b32 %0, m0\n\ts_mov_b32 m0, %3\n\ts_nop 0\n\tglobal_load_lds_dwordx4 %1, %2\n\ts_mov_b32 m0, %0"
               : "=&s"(keep) : "v"(voff), "s"(sbase), "s"(dst) : "memory");
}
DI void gldsa4(const void* sbase, unsigned voff, unsigned dst) {
  unsigned keep;
  asm volatile("s_mov_b32 %0, m0\n\ts_mov_b32 m0, %3\n\ts_nop 0\n\tglobal_load_lds_dword %1, %2\n\ts_mov_b32 m0, %0"
               : "=&s"(keep) : "v"(voff), "s"(sbase), "s"(dst) : "memory");
}
DI s16x4 ldtr(const char* l) {
  typedef short v4i16_t __attribute__((ext_vector_type(4)));
  return __builtin_amdgcn_ds_read_tr16_b64_v4i16((LAS v4i16_t*)l);
}
#define WAIT_V(n) asm volatile("s_waitcnt vmcnt(" #n ") lgkmcnt(0)" ::: "memory")
#define BAR() __builtin_amdgcn_s_barrier()
template <int CTRL> DI float dppf(float v) {
  return __int_as_float(__builtin_amdgcn_update_dpp(0, __float_as_int(v), CTRL, 0xF, 0xF, true));
}
DI float rdl(float v, int l) { return __int_as_float(__builtin_amdgcn_readlane(__float_as_int(v), l)); }
DI float row16_max(float v) {
  v = fmaxf(v, dppf<0xB1>(v)); v = fmaxf(v, dppf<0x4E>(v)); v = fmaxf(v, dppf<0x141>(v)); v = fmaxf(v, dppf<0x140>(v));
  return v;
}
DI float row16_sum(float v) {
  v += dppf<0xB1>(v); v += dppf<0x4E>(v); v += dppf<0x141>(v); v += dppf<0x140>(v);
  return v;
}
DI float add_half(float v) {
  auto rr = __builtin_amdgcn_permlane32_swap(__float_as_uint(v), __float_as_uint(v), false, false);
  return __uint_as_float(rr[0]) + __uint_as_float(rr[1]);
}
DI float max_half(float v) {
  auto rr = __builtin_amdgcn_permlane32_swap(__float_as_uint(v), __float_as_uint(v), false, false);
  return fmaxf(__uint_as_float(rr[0]), __uint_as_float(rr[1]));
}

DI float mish_f(float h) {
  float t = fminf(h * 1.4426950408889634f, 60.f);
  float w = __builtin_amdgcn_exp2f(t);
  float a = w + 2.f;
  float u = w * a;
  float d = __builtin_fmaf(w, a, 2.f);
  return h * u * __builtin_amdgcn_rcpf(d);
}

#define K1_LOOP_ASM0 \
  "s_mov_b32 %[keep], m0\n\t" \
  "s_waitcnt vmcnt(5)\n\t" \
  "s_barrier\n\t" \
  "ds_read_b128 v[48:51], v80\n\t" \
  "ds_read_b64_tr_b16 v[52:53], v84 offset:0\n\t" \
  "ds_read_b64_tr_b16 v[54:55], v84 offset:256\n\t" \
  "ds_read_b64_tr_b16 v[56:57], v84 offset:4096\n\t" \
  "ds_read_b64_tr_b16 v[58:59], v84 offset:4352\n\t" \
  "ds_read_b64_tr_b16 v[60:61], v84 offset:8192\n\t" \
  "ds_read_b64_tr_b16 v[62:63], v84 offset:8448\n\t" \
  "s_add_u32 m0, %[fx], 0\n\t" \
  "s_nop 0\n\t" \
  "global_load_lds_dwordx4 v85, s[60:61]\n\t" \
  "s_add_u32 m0, %[fx], 1024\n\t" \
  "s_nop 0\n\t" \
  "global_load_lds_dwordx4 v86, s[60:61]\n\t" \
  "s_add_u32 m0, %[fw], 0\n\t" \
  "s_nop 0\n\t" \
  "global_load_lds_dwordx4 v87, s[62:63]\n\t" \
  "s_add_u32 m0, %[fw], 4096\n\t" \
  "s_nop 0\n\t" \
  "global_load_lds_dwordx4 v88, s[62:63]\n\t" \
  "s_add_u32 m0, %[fw], 8192\n\t" \
  "s_nop 0\n\t" \
  "global_load_lds_dwordx4 v89, s[62:63]\n\t" \
  "1:\n\t" \
  "ds_read_b128 v[64:67], v81\n\t" \
  "ds_read_b64_tr_b16 v[68:69], v84 offset:1024\n\t" \
  "ds_read_b64_tr_b16 v[70:71], v84 offset:1280\n\t" \
  "ds_read_b64_tr_b16 v[72:73], v84 offset:5120\n\t" \
  "ds_read_b64_tr_b16 v[74:75], v84 offset:5376\n\t" \
  "ds_read_b64_tr_b16 v[76:77], v84 offset:9216\n\t" \
  "ds_read_b64_tr_b16 v[78:79], v84 offset:9472\n\t" \
  "s_waitcnt lgkmcnt(7)\n\t" \
  "v_mfma_f32_32x32x16_bf16 v[0:15], v[52:55], v[48:51], v[0:15]\n\t" \
  "v_mfma_f32_32x32x16_bf16 v[16:31], v[56:59], v[48:51], v[16:31]\n\t" \
  "v_mfma_f32_32x32x16_bf16 v[32:47], v[48:51], v[60:63], v[32:47]\n\t" \
  "ds_read_b128 v[48:51], v82\n\t" \
  "ds_read_b64_tr_b16 v[52:53], v84 offset:2048\n\t" \
  "ds_read_b64_tr_b16 v[54:55], v84 offset:2304\n\t" \
  "ds_read_b64_tr_b16 v[56:57], v84 offset:6144\n\t" \
  "ds_read_b64_tr_b16 v[58:59], v84 offset:6400\n\t" \
  "ds_read_b64_tr_b16 v[60:61], v84 offset:10240\n\t" \
  "ds_read_b64_tr_b16 v[62:63], v84 offset:10496\n\t" \
  "s_waitcnt lgkmcnt(7)\n\t" \
  "v_mfma_f32_32x32x16_bf16 v[0:15], v[68:71], v[64:67], v[0:15]\n\t" \
  "v_mfma_f32_32x32x16_bf16 v[16:31], v[72:75], v[64:67], v[16:31]\n\t" \
  "v_mfma_f32_32x32x16_bf16 v[32:47], v[64:67], v[76:79], v[32:47]\n\t" \
  "ds_read_b128 v[64:67], v83\n\t" \
  "ds_read_b64_tr_b16 v[68:69], v84 offset:3072\n\t" \
  "ds_read_b64_tr_b16 v[70:71], v84 offset:3328\n\t" \
  "ds_read_b64_tr_b16 v[72:73], v84 offset:7168\n\t" \
  "ds_read_b64_tr_b16 v[74:75], v84 offset:7424\n\t" \
  "ds_read_b64_tr_b16 v[76:77], v84 offset:11264\n\t" \
  "ds_read_b64_tr_b16 v[78:79], v84 offset:11520\n\t" \
  "s_waitcnt lgkmcnt(7)\n\t" \
  "v_mfma_f32_32x32x16_bf16 v[0:15], v[52:55], v[48:51], v[0:15]\n\t" \
  "v_mfma_f32_32x32x16_bf16 v[16:31], v[56:59], v[48:51], v[16:31]\n\t" \
  "v_mfma_f32_32x32x16_bf16 v[32:47], v[48:51], v[60:63], v[32:47]\n\t" \
  "s_cmp_eq_u32 %[kt], 7\n\t" \
  "s_cbranch_scc1 9f\n\t" \
  "s_add_u32 %[rd], %[rd], 1\n\t" \
  "s_cmp_eq_u32 %[rd], 3\n\t" \
  "s_cselect_b32 %[t0], %[cm80], %[c40]\n\t" \
  "s_cselect_b32 %[rd], 0, %[rd]\n\t" \
  "v_add_u32 v80, %[t0], v80\n\t" \
  "v_add_u32 v81, %[t0], v81\n\t" \
  "v_add_u32 v82, %[t0], v82\n\t" \
  "v_add_u32 v83, %[t0], v83\n\t" \
  "v_add_u32 v84, %[t0], v84\n\t" \
  "s_cmp_lt_u32 %[kt], 6\n\t" \
  "s_cbranch_scc1 3f\n\t" \
  "s_waitcnt vmcnt(0)\n\t" \
  "3:\n\t" \
  "s_waitcnt vmcnt(5) lgkmcnt(0)\n\t" \
  "s_barrier\n\t" \
  "ds_read_b128 v[48:51], v80\n\t" \
  "ds_read_b64_tr_b16 v[52:53], v84 offset:0\n\t" \
  "ds_read_b64_tr_b16 v[54:55], v84 offset:256\n\t" \
  "ds_read_b64_tr_b16 v[56:57], v84 offset:4096\n\t" \
  "ds_read_b64_tr_b16 v[58:59], v84 offset:4352\n\t" \
  "ds_read_b64_tr_b16 v[60:61], v84 offset:8192\n\t" \
  "ds_read_b64_tr_b16 v[62:63], v84 offset:8448\n\t" \
  "v_mfma_f32_32x32x16_bf16 v[0:15], v[68:71], v[64:67], v[0:15]\n\t" \
  "s_cmp_lt_u32 %[kt], 5\n\t" \
  "s_cbranch_scc0 4f\n\t" \
  "s_add_u32 s60, s60, 128\n\t" \
  "s_addc_u32 s61, s61, 0\n\t" \
  "s_add_u32 s62, s62, %[wstep]\n\t" \
  "s_addc_u32 s63, s63, 0\n\t" \
  "s_add_u32 %[fx], %[fx], %[c40]\n\t" \
  "s_add_u32 %[fw], %[fw], %[c40]\n\t" \
  "s_cmp_ge_u32 %[fx], %[lim]\n\t" \
  "s_cselect_b32 %[t0], %[cm120], 0\n\t" \
  "s_add_u32 %[fx], %[fx], %[t0]\n\t" \
  "s_add_u32 %[fw], %[fw], %[t0]\n\t" \
  "s_add_u32 m0, %[fx], 0\n\t" \
  "s_nop 0\n\t" \
  "global_load_lds_dwordx4 v85, s[60:61]\n\t" \
  "s_add_u32 m0, %[fx], 1024\n\t" \
  "s_nop 0\n\t" \
  "global_load_lds_dwordx4 v86, s[60:61]\n\t" \
  "4:\n\t" \
  "v_mfma_f32_32x32x16_bf16 v[16:31], v[72:75], v[64:67], v[16:31]\n\t" \
  "s_cmp_lt_u32 %[kt], 5\n\t" \
  "s_cbranch_scc0 5f\n\t" \
  "s_add_u32 m0, %[fw], 0\n\t" \
  "s_nop 0\n\t" \
  "global_load_lds_dwordx4 v87, s[62:63]\n\t" \
  "s_add_u32 m0, %[fw], 4096\n\t" \
  "s_nop 0\n\t" \
  "global_load_lds_dwordx4 v88, s[62:63]\n\t" \
  "s_add_u32 m0, %[fw], 8192\n\t" \
  "s_nop 0\n\t" \
  "global_load_lds_dwordx4 v89, s[62:63]\n\t" \
  "5:\n\t" \
  "v_mfma_f32_32x32x16_bf16 v[32:47], v[64:67], v[76:79], v[32:47]\n\t" \
  "s_add_u32 %[kt], %[kt], 1\n\t" \
  "s_branch 1b\n\t" \
  "9:\n\t" \
  "s_waitcnt lgkmcnt(0)\n\t" \
  "v_mfma_f32_32x32x16_bf16 v[0:15], v[68:71], v[64:67], v[0:15]\n\t" \
  "v_mfma_f32_32x32x16_bf16 v[16:31], v[72:75], v[64:67], v[16:31]\n\t" \
  "v_mfma_f32_32x32x16_bf16 v[32:47], v[64:67], v[76:79], v[32:47]\n\t" \
  "s_mov_b32 m0, %[keep]\n\t" \
  "s_nop 15\n\t" \
  ""
#define K1_LOOP_ASM1 \
  "s_mov_b32 %[keep], m0\n\t" \
  "s_waitcnt vmcnt(5)\n\t" \
  "s_barrier\n\t" \
  "ds_read_b128 v[48:51], v80\n\t" \
  "ds_read_b64_tr_b16 v[52:53], v84 offset:0\n\t" \
  "ds_read_b64_tr_b16 v[54:55], v84 offset:256\n\t" \
  "ds_read_b64_tr_b16 v[56:57], v84 offset:4096\n\t" \
  "ds_read_b64_tr_b16 v[58:59], v84 offset:4352\n\t" \
  "ds_read_b64_tr_b16 v[60:61], v84 offset:8192\n\t" \
  "ds_read_b64_tr_b16 v[62:63], v84 offset:8448\n\t" \
  "s_add_u32 m0, %[fx], 0\n\t" \
  "s_nop 0\n\t" \
  "global_load_lds_dwordx4 v85, s[60:61]\n\t" \
  "s_add_u32 m0, %[fx], 1024\n\t" \
  "s_nop 0\n\t" \
  "global_load_lds_dwordx4 v86, s[60:61]\n\t" \
  "s_add_u32 m0, %[fw], 0\n\t" \
  "s_nop 0\n\t" \
  "global_load_lds_dwordx4 v87, s[62:63]\n\t" \
  "s_add_u32 m0, %[fw], 4096\n\t" \
  "s_nop 0\n\t" \
  "global_load_lds_dwordx4 v88, s[62:63]\n\t" \
  "s_add_u32 m0, %[fw], 8192\n\t" \
  "s_nop 0\n\t" \
  "global_load_lds_dwordx4 v89, s[62:63]\n\t" \
  "1:\n\t" \
  "ds_read_b128 v[64:67], v81\n\t" \
  "ds_read_b64_tr_b16 v[68:69], v84 offset:1024\n\t" \
  "ds_read_b64_tr_b16 v[70:71], v84 offset:1280\n\t" \
  "ds_read_b64_tr_b16 v[72:73], v84 offset:5120\n\t" \
  "ds_read_b64_tr_b16 v[74:75], v84 offset:5376\n\t" \
  "ds_read_b64_tr_b16 v[76:77], v84 offset:9216\n\t" \
  "ds_read_b64_tr_b16 v[78:79], v84 offset:9472\n\t" \
  "s_waitcnt lgkmcnt(7)\n\t" \
  "v_mfma_f32_32x32x16_bf16 v[0:15], v[52:55], v[48:51], v[0:15]\n\t" \
  "v_mfma_f32_32x32x16_bf16 v[16:31], v[56:59], v[48:51], v[16:31]\n\t" \
  "v_mfma_f32_32x32x16_bf16 v[32:47], v[60:63], v[48:51], v[32:47]\n\t" \
  "ds_read_b128 v[48:51], v82\n\t" \
  "ds_read_b64_tr_b16 v[52:53], v84 offset:2048\n\t" \
  "ds_read_b64_tr_b16 v[54:55], v84 offset:2304\n\t" \
  "ds_read_b64_tr_b16 v[56:57], v84 offset:6144\n\t" \
  "ds_read_b64_tr_b16 v[58:59], v84 offset:6400\n\t" \
  "ds_read_b64_tr_b16 v[60:61], v84 offset:10240\n\t" \
  "ds_read_b64_tr_b16 v[62:63], v84 offset:10496\n\t" \
  "s_waitcnt lgkmcnt(7)\n\t" \
  "v_mfma_f32_32x32x16_bf16 v[0:15], v[68:71], v[64:67], v[0:15]\n\t" \
  "v_mfma_f32_32x32x16_bf16 v[16:31], v[72:75], v[64:67], v[16:31]\n\t" \
  "v_mfma_f32_32x32x16_bf16 v[32:47], v[76:79], v[64:67], v[32:47]\n\t" \
  "ds_read_b128 v[64:67], v83\n\t" \
  "ds_read_b64_tr_b16 v[68:69], v84 offset:3072\n\t" \
  "ds_read_b64_tr_b16 v[70:71], v84 offset:3328\n\t" \
  "ds_read_b64_tr_b16 v[72:73], v84 offset:7168\n\t" \
  "ds_read_b64_tr_b16 v[74:75], v84 offset:7424\n\t" \
  "ds_read_b64_tr_b16 v[76:77], v84 offset:11264\n\t" \
  "ds_read_b64_tr_b16 v[78:79], v84 offset:11520\n\t" \
  "s_waitcnt lgkmcnt(7)\n\t" \
  "v_mfma_f32_32x32x16_bf16 v[0:15], v[52:55], v[48:51], v[0:15]\n\t" \
  "v_mfma_f32_32x32x16_bf16 v[16:31], v[56:59], v[48:51], v[16:31]\n\t" \
  "v_mfma_f32_32x32x16_bf16 v[32:47], v[60:63], v[48:51], v[32:47]\n\t" \
  "s_cmp_eq_u32 %[kt], 7\n\t" \
  "s_cbranch_scc1 9f\n\t" \
  "s_add_u32 %[rd], %[rd], 1\n\t" \
  "s_cmp_eq_u32 %[rd], 3\n\t" \
  "s_cselect_b32 %[t0], %[cm80], %[c40]\n\t" \
  "s_cselect_b32 %[rd], 0, %[rd]\n\t" \
  "v_add_u32 v80, %[t0], v80\n\t" \
  "v_add_u32 v81, %[t0], v81\n\t" \
  "v_add_u32 v82, %[t0], v82\n\t" \
  "v_add_u32 v83, %[t0], v83\n\t" \
  "v_add_u32 v84, %[t0], v84\n\t" \
  "s_cmp_lt_u32 %[kt], 6\n\t" \
  "s_cbranch_scc1 3f\n\t" \
  "s_waitcnt vmcnt(0)\n\t" \
  "3:\n\t" \
  "s_waitcnt vmcnt(5) lgkmcnt(0)\n\t" \
  "s_barrier\n\t" \
  "ds_read_b128 v[48:51], v80\n\t" \
  "ds_read_b64_tr_b16 v[52:53], v84 offset:0\n\t" \
  "ds_read_b64_tr_b16 v[54:55], v84 offset:256\n\t" \
  "ds_read_b64_tr_b16 v[56:57], v84 offset:4096\n\t" \
  "ds_read_b64_tr_b16 v[58:59], v84 offset:4352\n\t" \
  "ds_read_b64_tr_b16 v[60:61], v84 offset:8192\n\t" \
  "ds_read_b64_tr_b16 v[62:63], v84 offset:8448\n\t" \
  "v_mfma_f32_32x32x16_bf16 v[0:15], v[68:71], v[64:67], v[0:15]\n\t" \
  "s_cmp_lt_u32 %[kt], 5\n\t" \
  "s_cbranch_scc0 4f\n\t" \
  "s_add_u32 s60, s60, 128\n\t" \
  "s_addc_u32 s61, s61, 0\n\t" \
  "s_add_u32 s62, s62, %[wstep]\n\t" \
  "s_addc_u32 s63, s63, 0\n\t" \
  "s_add_u32 %[fx], %[fx], %[c40]\n\t" \
  "s_add_u32 %[fw], %[fw], %[c40]\n\t" \
  "s_cmp_ge_u32 %[fx], %[lim]\n\t" \
  "s_cselect_b32 %[t0], %[cm120], 0\n\t" \
  "s_add_u32 %[fx], %[fx], %[t0]\n\t" \
  "s_add_u32 %[fw], %[fw], %[t0]\n\t" \
  "s_add_u32 m0, %[fx], 0\n\t" \
  "s_nop 0\n\t" \
  "global_load_lds_dwordx4 v85, s[60:61]\n\t" \
  "s_add_u32 m0, %[fx], 1024\n\t" \
  "s_nop 0\n\t" \
  "global_load_lds_dwordx4 v86, s[60:61]\n\t" \
  "4:\n\t" \
  "v_mfma_f32_32x32x16_bf16 v[16:31], v[72:75], v[64:67], v[16:31]\n\t" \
  "s_cmp_lt_u32 %[kt], 5\n\t" \
  "s_cbranch_scc0 5f\n\t" \
  "s_add_u32 m0, %[fw], 0\n\t" \
  "s_nop 0\n\t" \
  "global_load_lds_dwordx4 v87, s[62:63]\n\t" \
  "s_add_u32 m0, %[fw], 4096\n\t" \
  "s_nop 0\n\t" \
  "global_load_lds_dwordx4 v88, s[62:63]\n\t" \
  "s_add_u32 m0, %[fw], 8192\n\t" \
  "s_nop 0\n\t" \
  "global_load_lds_dwordx4 v89, s[62:63]\n\t" \
  "5:\n\t" \
  "v_mfma_f32_32x32x16_bf16 v[32:47], v[76:79], v[64:67], v[32:47]\n\t" \
  "s_add_u32 %[kt], %[kt], 1\n\t" \
  "s_branch 1b\n\t" \
  "9:\n\t" \
  "s_waitcnt lgkmcnt(0)\n\t" \
  "v_mfma_f32_32x32x16_bf16 v[0:15], v[68:71], v[64:67], v[0:15]\n\t" \
  "v_mfma_f32_32x32x16_bf16 v[16:31], v[72:75], v[64:67], v[16:31]\n\t" \
  "v_mfma_f32_32x32x16_bf16 v[32:47], v[76:79], v[64:67], v[32:47]\n\t" \
  "s_mov_b32 m0, %[keep]\n\t" \
  "s_nop 15\n\t" \
  ""

constexpr int K1_SIDE = 3 * 40960;
__global__ __launch_bounds__(512) void k1(const u16* __restrict__ x, const u16* __restrict__ wqkv,
                                          const u16* __restrict__ wo, const u16* __restrict__ w1,
                                          const u16* __restrict__ b1, const u16* __restrict__ w2,
                                          const u16* __restrict__ gw, char* __restrict__ ws) {
  __shared__ __attribute__((aligned(16))) char lds[K1_SIDE + 8 * 3328];
  const int bid = blockIdx.x, xcd = bid & 7, idx = bid >> 3;
  const int rb = (xcd & 1) * 8 + (idx & 7), j = (xcd >> 1) * 4 + (idx >> 3);
  const int tid = threadIdx.x, lane = tid & 63;
  const int w = __builtin_amdgcn_readfirstlane(tid >> 6);
  const int r = lane & 31, h = lane >> 5, m = w & 3, nh = w >> 2;
  const unsigned lb = (unsigned)(size_t)(LAS char*)lds;
  const int tile = rb * 4 + m;
  const int ldw = nh ? F_ : 1536;
  unsigned xo0 = (unsigned)((lane >> 3) * D_ + (((lane & 7) ^ ((lane >> 4) & 7)) * 8)) * 2u;
  unsigned xo1 = (unsigned)((8 + (lane >> 3)) * D_ + (((lane & 7) ^ ((4 + (lane >> 4)) & 7)) * 8)) * 2u;
  const unsigned wo_ = (unsigned)((lane >> 2) * ldw + (lane & 3) * 8) * 2u;
  const int fb0 = min(3 * j, 42), fb1 = min(3 * j + 1, 42), fb2 = min(3 * j + 2, 42);
  unsigned wo0 = wo_ + (nh ? 64 * fb0 : 128 * j), wo1 = wo_ + (nh ? 64 * fb1 : 128 * j + 64),
           wo2 = wo_ + (nh ? 64 * fb2 : 2048 + 64 * j);
  const char* xb = (const char*)(x + (size_t)(rb * 128 + 16 * w) * D_);
  const char* wb = (const char*)((nh ? w1 : wqkv) + (size_t)(16 * m) * ldw);
  const unsigned wstep = 128u * ldw;
  const unsigned xl0 = lb + w * 2048, wl0 = lb + 16384 + nh * 12288 + m * 1024;
#define STAGE(sb)                                                                                 \
  do {                                                                                            \
    gldsa16(xb, xo0, xl0 + (sb) * 40960);                                                         \
    gldsa16(xb, xo1, xl0 + (sb) * 40960 + 1024);                                                  \
    gldsa16(wb, wo0, wl0 + (sb) * 40960);                                                         \
    gldsa16(wb, wo1, wl0 + (sb) * 40960 + 4096);                                                  \
    gldsa16(wb, wo2, wl0 + (sb) * 40960 + 8192);                                                  \
    xb += 128; wb += wstep;                                                                       \
  } while (0)
  STAGE(0);
  const int fid = bid * 8 + w;
  const int fidc = min(fid, 1887);
  const int tok = rb * 128 + j * 8 + w;
  const unsigned sl = lb + K1_SIDE + w * 3328;
  {
    const u16* psrc = fidc < 1376 ? w2 + (size_t)(fidc >> 4) * 16 * D_ + (fidc & 15) * 32
                                  : wo + (size_t)((fidc - 1376) >> 4) * 16 * D_ + (fidc & 15) * 32;
    gldsa16(psrc, (unsigned)((lane >> 2) * D_ + (lane & 3) * 8) * 2u, sl);
    gldsa16(x + (size_t)tok * D_, lane * 16, sl + 1024);
    gldsa16(gw, lane * 16, sl + 2048);
    gldsa4(b1, (unsigned)min(96 * j + 2 * lane, F_ - 2) * 2u, sl + 3072);
  }
  STAGE(1);
#undef STAGE

  f32x16 acc[3];
#pragma unroll
  for (int b = 0; b < 3; ++b)
#pragma unroll
    for (int i = 0; i < 16; ++i) acc[b][i] = 0.f;
  const int sw = (r >> 1) & 7;
  const int q4 = (lane & 15) >> 2, p4 = lane & 3, grp = (lane >> 4) & 1;
  {
    const unsigned xr0 = lb + m * 4096 + r * 128;
    unsigned xa0 = xr0 + ((0 + h) ^ sw) * 16, xa1 = xr0 + ((2 + h) ^ sw) * 16, xa2 = xr0 + ((4 + h) ^ sw) * 16,
             xa3 = xr0 + ((6 + h) ^ sw) * 16;
    unsigned wa = lb + 16384 + nh * 12288 + (8 * h + q4) * 64 + grp * 32 + p4 * 8;
    unsigned fx = xl0 + 2 * 40960, fw = wl0 + 2 * 40960, kt = 0, rd = 0, t0, keep;
    const unsigned lim = lb + 3 * 40960;
#define K1_RUN(ASM)                                                                                            \
    asm volatile(ASM                                                                                           \
                 : "+{v[0:15]}"(acc[0]), "+{v[16:31]}"(acc[1]), "+{v[32:47]}"(acc[2]), "+{v80}"(xa0),          \
                   "+{v81}"(xa1), "+{v82}"(xa2), "+{v83}"(xa3), "+{v84}"(wa), "+{s[60:61]}"(xb),               \
                   "+{s[62:63]}"(wb), [fx] "+s"(fx), [fw] "+s"(fw), [kt] "+s"(kt), [rd] "+s"(rd),              \
                   [t0] "=&s"(t0), [keep] "=&s"(keep)                                                          \
                 : "{v85}"(xo0), "{v86}"(xo1), "{v87}"(wo0), "{v88}"(wo1), "{v89}"(wo2), [wstep] "s"(wstep),   \
                   [lim] "s"(lim), [c40] "s"(40960u), [cm80] "s"(0u - 81920u), [cm120] "s"(0u - 122880u)       \
                 : "memory", "scc", "v48", "v49", "v50", "v51", "v52", "v53", "v54", "v55", "v56", "v57",      \
                   "v58", "v59", "v60", "v61", "v62", "v63", "v64", "v65", "v66", "v67", "v68", "v69", "v70",  \
                   "v71", "v72", "v73", "v74", "v75", "v76", "v77", "v78", "v79")
    if (nh) K1_RUN(K1_LOOP_ASM1); else K1_RUN(K1_LOOP_ASM0);
  }
  const char* sp = lds + K1_SIDE + w * 3328;
  if (nh == 0) {
#pragma unroll
    for (int b = 0; b < 2; ++b) {
      const int c0 = 64 * j + 32 * b;
      const bool isq = c0 < 512;
      const float sc = isq ? 0.36067376022224085f : 1.0f;
      const int head0 = (c0 & 511) >> 4;
      char* dst = ws + (isq ? WS_Q : WS_K);
#pragma unroll
      for (int hh = 0; hh < 2; ++hh) {
        u32x4 o;
#pragma unroll
        for (int e = 0; e < 4; ++e) o[e] = pk2(acc[b][8 * hh + 2 * e] * sc, acc[b][8 * hh + 2 * e + 1] * sc);
        *(u32x4*)(dst + ((size_t)((head0 + hh) * 64 + tile) * 64 + lane) * 16) = o;
        if (!isq) {
          float ss = 0.f;
#pragma unroll
          for (int e = 0; e < 4; ++e) {
            const float a = bf_lo(o[e]), c = bf_hi(o[e]);
            ss = __builtin_fmaf(a, a, ss);
            ss = __builtin_fmaf(c, c, ss);
          }
          ss = row16_max(add_half(ss));
          ss = fmaxf(rdl(ss, 0), rdl(ss, 16));
          if (lane == 0) ((float*)(ws + WS_KMAX))[(head0 + hh) * 64 + tile] = ss;
        }
      }
    }
    {
      const int head = 2 * j + (r >> 4), d = r & 15;
#pragma unroll
      for (int s = 0; s < 2; ++s) {
        u32x4 o;
#pragma unroll
        for (int e = 0; e < 4; ++e) o[e] = pk2(acc[2][8 * s + 2 * e], acc[2][8 * s + 2 * e + 1]);
        *(u32x4*)(ws + WS_V + (size_t)(head * 64 + tile) * 1024 + s * 512 + h * 256 + d * 16) = o;
      }
    }
  } else {
#pragma unroll
    for (int b = 0; b < 3; ++b) {
      if (3 * j + b < 43) {
#pragma unroll
        for (int hh = 0; hh < 2; ++hh) {
          u32x4 o;
#pragma unroll
          for (int gq = 0; gq < 2; ++gq) {
            const int g = 2 * hh + gq;
            const u32x2 bb = *(const u32x2*)(sp + 3072 + (32 * b + 8 * g + 4 * h) * 2);
            const float h0 = acc[b][4 * g + 0] + bf_lo(bb[0]);
            const float h1 = acc[b][4 * g + 1] + bf_hi(bb[0]);
            const float h2 = acc[b][4 * g + 2] + bf_lo(bb[1]);
            const float h3 = acc[b][4 * g + 3] + bf_hi(bb[1]);
            o[2 * gq] = pk2(mish_f(h0), mish_f(h1));
            o[2 * gq + 1] = pk2(mish_f(h2), mish_f(h3));
          }
          *(u32x4*)(ws + WS_ACT + ((size_t)((2 * (3 * j + b) + hh) * 64 + tile) * 64 + lane) * 16) = o;
        }
      }
    }
  }
  {
    const char* pp = sp + (4 * h + q4) * 64 + grp * 32 + p4 * 8;
    const s16x4 lo = ldtr(pp), hi = ldtr(pp + 512);
    const bf16x8 f = __builtin_shufflevector(lo, hi, 0, 1, 2, 3, 4, 5, 6, 7);
    if (fid < 1888) *(bf16x8*)(ws + (fid < 1376 ? WS_W2P + (size_t)fid * 1024 : WS_WOP + (size_t)(fid - 1376) * 1024) + lane * 16) = f;
  }
  {
    const u32x4 gx = *(const u32x4*)(sp + 1024 + lane * 16), gg = *(const u32x4*)(sp + 2048 + lane * 16);
    float s = 0.f;
#pragma unroll
    for (int e = 0; e < 4; ++e) {
      s += rbf(bf_lo(gx[e]) * bf_lo(gg[e]));
      s += rbf(bf_hi(gx[e]) * bf_hi(gg[e]));
    }
    s = row16_sum(s);
    s = (rdl(s, 0) + rdl(s, 16)) + (rdl(s, 32) + rdl(s, 48));
    s = rbf(s);
    const float g = rbf(__builtin_amdgcn_rcpf(1.f + __builtin_amdgcn_exp2f(-s * 1.4426950408889634f)));
    if (lane == 0) ((float*)(ws + WS_GATE))[tok] = g;
  }
}

#define ATTN_LOOP_ASM \
  "1:\n\t" \
  "v_exp_f32 v32, v32\n\t" \
  "v_exp_f32 v33, v33\n\t" \
  "v_exp_f32 v34, v34\n\t" \
  "v_cvt_pk_bf16_f32 v88, v32, v33\n\t" \
  "v_mfma_f32_32x32x16_bf16 v[0:15], v[84:87], v[92:95], v[0:15]\n\t" \
  "s_mov_b64 exec, %[mask]\n\t" \
  "ds_read_b128 v[80:83], v97 offset:1024\n\t" \
  "ds_read_b128 v[84:87], v97 offset:1536\n\t" \
  "s_mov_b64 exec, -1\n\t" \
  "v_exp_f32 v35, v35\n\t" \
  "v_exp_f32 v36, v36\n\t" \
  "v_cvt_pk_bf16_f32 v89, v34, v35\n\t" \
  "v_exp_f32 v37, v37\n\t" \
  "v_exp_f32 v38, v38\n\t" \
  "v_cvt_pk_bf16_f32 v90, v36, v37\n\t" \
  "v_exp_f32 v39, v39\n\t" \
  "v_exp_f32 v40, v40\n\t" \
  "v_cvt_pk_bf16_f32 v91, v38, v39\n\t" \
  "v_exp_f32 v41, v41\n\t" \
  "v_exp_f32 v42, v42\n\t" \
  "v_mfma_f32_32x32x16_bf16 v[0:15], v[72:75], v[88:91], v[0:15]\n\t" \
  "v_cvt_pk_bf16_f32 v92, v40, v41\n\t" \
  "v_exp_f32 v43, v43\n\t" \
  "v_exp_f32 v44, v44\n\t" \
  "v_cvt_pk_bf16_f32 v93, v42, v43\n\t" \
  "v_exp_f32 v45, v45\n\t" \
  "v_exp_f32 v46, v46\n\t" \
  "v_cvt_pk_bf16_f32 v94, v44, v45\n\t" \
  "v_exp_f32 v47, v47\n\t" \
  "s_waitcnt lgkmcnt(0)\n\t" \
  "v_cvt_pk_bf16_f32 v95, v46, v47\n\t" \
  "v_mfma_f32_32x32x16_bf16 v[32:47], v[68:71], v[64:67], v[16:31]\n\t" \
  "ds_read_b128 v[68:71], v96 offset:3072\n\t" \
  "v_exp_f32 v48, v48\n\t" \
  "v_exp_f32 v49, v49\n\t" \
  "v_exp_f32 v50, v50\n\t" \
  "v_cvt_pk_bf16_f32 v88, v48, v49\n\t" \
  "v_mfma_f32_32x32x16_bf16 v[0:15], v[76:79], v[92:95], v[0:15]\n\t" \
  "s_mov_b64 exec, %[mask]\n\t" \
  "ds_read_b128 v[72:75], v97 offset:2048\n\t" \
  "ds_read_b128 v[76:79], v97 offset:2560\n\t" \
  "s_mov_b64 exec, -1\n\t" \
  "v_exp_f32 v51, v51\n\t" \
  "v_exp_f32 v52, v52\n\t" \
  "v_cvt_pk_bf16_f32 v89, v50, v51\n\t" \
  "v_exp_f32 v53, v53\n\t" \
  "v_exp_f32 v54, v54\n\t" \
  "v_cvt_pk_bf16_f32 v90, v52, v53\n\t" \
  "v_exp_f32 v55, v55\n\t" \
  "v_exp_f32 v56, v56\n\t" \
  "v_cvt_pk_bf16_f32 v91, v54, v55\n\t" \
  "v_exp_f32 v57, v57\n\t" \
  "v_exp_f32 v58, v58\n\t" \
  "v_mfma_f32_32x32x16_bf16 v[0:15], v[80:83], v[88:91], v[0:15]\n\t" \
  "v_cvt_pk_bf16_f32 v92, v56, v57\n\t" \
  "v_exp_f32 v59, v59\n\t" \
  "v_exp_f32 v60, v60\n\t" \
  "v_cvt_pk_bf16_f32 v93, v58, v59\n\t" \
  "v_exp_f32 v61, v61\n\t" \
  "v_exp_f32 v62, v62\n\t" \
  "v_cvt_pk_bf16_f32 v94, v60, v61\n\t" \
  "v_exp_f32 v63, v63\n\t" \
  "s_waitcnt lgkmcnt(0)\n\t" \
  "v_cvt_pk_bf16_f32 v95, v62, v63\n\t" \
  "v_mfma_f32_32x32x16_bf16 v[48:63], v[68:71], v[64:67], v[16:31]\n\t" \
  "ds_read_b128 v[68:71], v96 offset:4096\n\t" \
  "v_exp_f32 v32, v32\n\t" \
  "v_exp_f32 v33, v33\n\t" \
  "v_exp_f32 v34, v34\n\t" \
  "v_cvt_pk_bf16_f32 v88, v32, v33\n\t" \
  "v_mfma_f32_32x32x16_bf16 v[0:15], v[84:87], v[92:95], v[0:15]\n\t" \
  "s_mov_b64 exec, %[mask]\n\t" \
  "ds_read_b128 v[80:83], v97 offset:3072\n\t" \
  "ds_read_b128 v[84:87], v97 offset:3584\n\t" \
  "s_mov_b64 exec, -1\n\t" \
  "v_exp_f32 v35, v35\n\t" \
  "v_exp_f32 v36, v36\n\t" \
  "v_cvt_pk_bf16_f32 v89, v34, v35\n\t" \
  "v_exp_f32 v37, v37\n\t" \
  "v_exp_f32 v38, v38\n\t" \
  "v_cvt_pk_bf16_f32 v90, v36, v37\n\t" \
  "v_exp_f32 v39, v39\n\t" \
  "v_exp_f32 v40, v40\n\t" \
  "v_cvt_pk_bf16_f32 v91, v38, v39\n\t" \
  "v_exp_f32 v41, v41\n\t" \
  "v_exp_f32 v42, v42\n\t" \
  "v_mfma_f32_32x32x16_bf16 v[0:15], v[72:75], v[88:91], v[0:15]\n\t" \
  "v_cvt_pk_bf16_f32 v92, v40, v41\n\t" \
  "v_exp_f32 v43, v43\n\t" \
  "v_exp_f32 v44, v44\n\t" \
  "v_cvt_pk_bf16_f32 v93, v42, v43\n\t" \
  "v_exp_f32 v45, v45\n\t" \
  "v_exp_f32 v46, v46\n\t" \
  "v_cvt_pk_bf16_f32 v94, v44, v45\n\t" \
  "v_exp_f32 v47, v47\n\t" \
  "s_waitcnt lgkmcnt(0)\n\t" \
  "v_cvt_pk_bf16_f32 v95, v46, v47\n\t" \
  "v_mfma_f32_32x32x16_bf16 v[32:47], v[68:71], v[64:67], v[16:31]\n\t" \
  "ds_read_b128 v[68:71], v96 offset:5120\n\t" \
  "v_exp_f32 v48, v48\n\t" \
  "v_exp_f32 v49, v49\n\t" \
  "v_exp_f32 v50, v50\n\t" \
  "v_cvt_pk_bf16_f32 v88, v48, v49\n\t" \
  "v_mfma_f32_32x32x16_bf16 v[0:15], v[76:79], v[92:95], v[0:15]\n\t" \
  "s_mov_b64 exec, %[mask]\n\t" \
  "ds_read_b128 v[72:75], v97 offset:4096\n\t" \
  "ds_read_b128 v[76:79], v97 offset:4608\n\t" \
  "s_mov_b64 exec, -1\n\t" \
  "v_exp_f32 v51, v51\n\t" \
  "v_exp_f32 v52, v52\n\t" \
  "v_cvt_pk_bf16_f32 v89, v50, v51\n\t" \
  "v_exp_f32 v53, v53\n\t" \
  "v_exp_f32 v54, v54\n\t" \
  "v_cvt_pk_bf16_f32 v90, v52, v53\n\t" \
  "v_exp_f32 v55, v55\n\t" \
  "v_exp_f32 v56, v56\n\t" \
  "v_cvt_pk_bf16_f32 v91, v54, v55\n\t" \
  "v_exp_f32 v57, v57\n\t" \
  "v_exp_f32 v58, v58\n\t" \
  "v_mfma_f32_32x32x16_bf16 v[0:15], v[80:83], v[88:91], v[0:15]\n\t" \
  "v_cvt_pk_bf16_f32 v92, v56, v57\n\t" \
  "v_exp_f32 v59, v59\n\t" \
  "v_exp_f32 v60, v60\n\t" \
  "v_cvt_pk_bf16_f32 v93, v58, v59\n\t" \
  "v_exp_f32 v61, v61\n\t" \
  "v_exp_f32 v62, v62\n\t" \
  "v_cvt_pk_bf16_f32 v94, v60, v61\n\t" \
  "v_exp_f32 v63, v63\n\t" \
  "s_waitcnt lgkmcnt(0)\n\t" \
  "v_cvt_pk_bf16_f32 v95, v62, v63\n\t" \
  "v_mfma_f32_32x32x16_bf16 v[48:63], v[68:71], v[64:67], v[16:31]\n\t" \
  "ds_read_b128 v[68:71], v96 offset:6144\n\t" \
  "v_add_u32 v96, 0x1000, v96\n\t" \
  "v_add_u32 v97, 0x1000, v97\n\t" \
  "s_sub_u32 %[cnt], %[cnt], 1\n\t" \
  "s_and_b32 %[tmp], %[cnt], 13\n\t" \
  "s_cmp_lg_u32 %[tmp], 12\n\t" \
  "s_cbranch_scc1 2f\n\t" \
  "s_barrier\n\t" \
  "2:\n\t" \
  "s_cmp_lg_u32 %[cnt], 0\n\t" \
  "s_cbranch_scc1 1b\n\t" \
  "s_waitcnt lgkmcnt(0)\n\t" \
  "s_nop 15\n\t" \
  ""

__global__ __launch_bounds__(768) void k2(char* __restrict__ ws) {
  __shared__ __attribute__((aligned(16))) char lds[131072 + 4096];
  const int bid = blockIdx.x, xcd = bid & 7, idx = bid >> 3;
  const int head = xcd * 4 + (idx & 3), qb = idx >> 2;
  const int tid = threadIdx.x, lane = tid & 63;
  const int w = __builtin_amdgcn_readfirstlane(tid >> 6);
  const int h = lane >> 5;
  const unsigned lb = (unsigned)(size_t)(LAS char*)lds;
  const unsigned l16 = lane * 16;

  if (w >= 8) {
    __builtin_amdgcn_s_setprio(LD_PRIO);
    const int L = w - 8;
    const char* ksrc = ws + WS_K + (size_t)(head * 64 + L) * 1024;
    const char* vsrc = ws + WS_V + (size_t)(head * 64 + L) * 1024;
    const unsigned kdst = lb + L * 1024, vdst = lb + 65536 + L * 1024;
#pragma unroll
    for (int c = 0; c < 3; ++c) { gldsa16(ksrc + c * 4096, l16, kdst + c * 4096); gldsa16(vsrc + c * 4096, l16, vdst + c * 4096); }
    WAIT_V(0); BAR();
#pragma unroll 1
    for (int c = 3; c < 11; ++c) {
      gldsa16(ksrc + c * 4096, l16, kdst + c * 4096); gldsa16(vsrc + c * 4096, l16, vdst + c * 4096);
      __builtin_amdgcn_s_sleep(PACE);
    }
    WAIT_V(4); BAR();
#pragma unroll 1
    for (int c = 11; c < 16; ++c) {
      gldsa16(ksrc + c * 4096, l16, kdst + c * 4096); gldsa16(vsrc + c * 4096, l16, vdst + c * 4096);
      __builtin_amdgcn_s_sleep(PACE);
    }
    WAIT_V(0);
    BAR();
    __builtin_amdgcn_s_setprio(FFN_PRIO);
    const int rb = (xcd & 3) * 8 + (idx & 7), cb = (xcd >> 2) * 4 + (idx >> 3);
    const int tile = rb * 2 + (L & 1), nb = cb * 2 + (L >> 1);
    const auto ra = __builtin_amdgcn_make_buffer_rsrc((void*)(ws + WS_ACT + (size_t)tile * 1024), 0, 85 * 65536 + 1024, 0x00020000);
    const auto rw = __builtin_amdgcn_make_buffer_rsrc((void*)(ws + WS_W2P + (size_t)nb * 1024), 0, 85 * 16384 + 1024, 0x00020000);
    f32x16 b;
#pragma unroll
    for (int i = 0; i < 16; ++i) b[i] = 0.f;
    constexpr int PD = 8;
    u32x4 fa[PD], fw[PD];
#pragma unroll
    for (int i = 0; i < PD; ++i) {
      fa[i] = __builtin_amdgcn_raw_buffer_load_b128(ra, l16, i * 65536, 0);
      fw[i] = __builtin_amdgcn_raw_buffer_load_b128(rw, l16, i * 16384, 0);
    }
    __builtin_amdgcn_sched_barrier(0);
#pragma unroll 1
    for (int k0 = PD; k0 < 88 + PD; k0 += PD) {
#pragma unroll
      for (int i = 0; i < PD; ++i) {
        b = MFMA(__builtin_bit_cast(bf16x8, fw[i]), __builtin_bit_cast(bf16x8, fa[i]), b);
        fa[i] = __builtin_amdgcn_raw_buffer_load_b128(ra, l16, (k0 + i) * 65536, 0);
        fw[i] = __builtin_amdgcn_raw_buffer_load_b128(rw, l16, (k0 + i) * 16384, 0);
        __builtin_amdgcn_sched_barrier(0);
      }
    }
    float* bp = (float*)(ws + WS_B) + ((size_t)(tile * 16 + nb) * 4 * 64 + lane) * 4;
#pragma unroll
    for (int q = 0; q < 4; ++q) {
      f32x4 o = {b[4 * q], b[4 * q + 1], b[4 * q + 2], b[4 * q + 3]};
      *(f32x4*)(bp + q * 256) = o;
    }
    return;
  }

  const int qt = qb * 8 + w;
  bf16x8 qf = *(const bf16x8*)(ws + WS_Q + (size_t)(head * 64 + qt) * 1024 + l16);
  float km = ((const float*)(ws + WS_KMAX))[head * 64 + lane];
  km = row16_max(km);
  km = fmaxf(fmaxf(rdl(km, 0), rdl(km, 16)), fmaxf(rdl(km, 32), rdl(km, 48)));
  float qs = 0.f;
  {
    const u32x4 qu = __builtin_bit_cast(u32x4, qf);
#pragma unroll
    for (int e = 0; e < 4; ++e) {
      const float a = bf_lo(qu[e]), c = bf_hi(qu[e]);
      qs = __builtin_fmaf(a, a, qs);
      qs = __builtin_fmaf(c, c, qs);
    }
    qs = add_half(qs);
  }
  const float mb = __builtin_amdgcn_sqrtf(qs * km) * 1.0001f;
  f32x16 negm, O;
#pragma unroll
  for (int i = 0; i < 16; ++i) { negm[i] = -mb; O[i] = 0.f; }

  const char* kp = lds + lane * 16;
  const char* vp = lds + 65536 + h * 256 + (lane & 15) * 16;
  const bool vact = (lane & 16) == 0;
  const short one = ((lane & 27) == 16) ? (short)0x3F80 : (short)0;
  bf16x8 va0, va1, vb0, vb1;
#pragma unroll
  for (int i = 0; i < 8; ++i) { va0[i] = one; va1[i] = one; vb0[i] = one; vb1[i] = one; }
  u32x4 p0 = {0, 0, 0, 0}, p1 = {0, 0, 0, 0};
  unsigned kaddr = lb + l16, vaddr = lb + 65536 + h * 256 + (lane & 15) * 16;
  const unsigned long long vmask = 0x0000FFFF0000FFFFull;

  BAR();
  asm volatile("" ::: "memory");
  f32x16 Sa = MFMA(*(const bf16x8*)(kp), qf, negm);
  f32x16 Sb = MFMA(*(const bf16x8*)(kp + 1024), qf, negm);
  bf16x8 kf = *(const bf16x8*)(kp + 2048);
  if (vact) { va0 = *(const bf16x8*)(vp); va1 = *(const bf16x8*)(vp + 512); }

#define ATTN_GROUPS(n)                                                                                       \
  do {                                                                                                       \
    unsigned cnt = (n), tmp;                                                                                 \
    asm volatile("s_nop 15\n\t" ATTN_LOOP_ASM                                                                \
                 : "+{v[0:15]}"(O), "+{v[32:47]}"(Sa), "+{v[48:63]}"(Sb), "+{v[68:71]}"(kf),                 \
                   "+{v[72:75]}"(va0), "+{v[76:79]}"(va1), "+{v[80:83]}"(vb0), "+{v[84:87]}"(vb1),           \
                   "+{v[88:91]}"(p0), "+{v[92:95]}"(p1), "+{v96}"(kaddr), "+{v97}"(vaddr), [cnt] "+s"(cnt), [tmp] "=&s"(tmp) \
                 : "{v[16:31]}"(negm), "{v[64:67]}"(qf), [mask] "s"(vmask)                                   \
                 : "memory", "scc");                                                                         \
  } while (0)

  ATTN_GROUPS(16);
  O = MFMA(vb1, __builtin_bit_cast(bf16x8, p1), O);

#define SOFTMAX_PV(Sx, v0, v1)                                                                     \
  do {                                                                                             \
    _Pragma("unroll") for (int e = 0; e < 4; ++e) {                                                \
      p0[e] = pk2(__builtin_amdgcn_exp2f(Sx[2 * e]), __builtin_amdgcn_exp2f(Sx[2 * e + 1]));       \
      p1[e] = pk2(__builtin_amdgcn_exp2f(Sx[8 + 2 * e]), __builtin_amdgcn_exp2f(Sx[8 + 2 * e + 1])); \
    }                                                                                              \
    O = MFMA(v0, __builtin_bit_cast(bf16x8, p0), O);                                               \
    O = MFMA(v1, __builtin_bit_cast(bf16x8, p1), O);                                               \
  } while (0)

  float l = O[8];
  if (__any(!(l >= 8.271806125530277e-25f))) {
    float m = -INFINITY;
    f32x16 zero;
#pragma unroll
    for (int i = 0; i < 16; ++i) { zero[i] = 0.f; O[i] = 0.f; }
    for (int t = 0; t < 64; ++t) {
      f32x16 Sx = MFMA(*(const bf16x8*)(kp + t * 1024), qf, zero);
      float mx = Sx[0];
#pragma unroll
      for (int i = 1; i < 16; ++i) mx = fmaxf(mx, Sx[i]);
      mx = max_half(mx);
      const float mn = fmaxf(m, mx);
      const float alpha = __builtin_amdgcn_exp2f(m - mn);
      m = mn;
#pragma unroll
      for (int i = 0; i < 16; ++i) { O[i] *= alpha; Sx[i] -= mn; }
      if (vact) { va0 = *(const bf16x8*)(vp + t * 1024); va1 = *(const bf16x8*)(vp + t * 1024 + 512); }
      SOFTMAX_PV(Sx, va0, va1);
    }
    l = O[8];
  }
  const float rl = 1.0f / l;
  u32x4 o;
#pragma unroll
  for (int e = 0; e < 4; ++e) o[e] = pk2(O[2 * e] * rl, O[2 * e + 1] * rl);
  *(u32x4*)(ws + WS_O + (size_t)(head * 64 + qt) * 1024 + l16) = o;
}

__global__ __launch_bounds__(256) void k3(const char* __restrict__ ws, u16* __restrict__ out) {
  __shared__ __attribute__((aligned(16))) float red[4][4][4][64][4];
  const int bid = blockIdx.x, xcd = bid & 7, idx = bid >> 3;
  const int rb = (xcd & 3) * 8 + (idx & 7), cb = (xcd >> 2) * 4 + (idx >> 3);
  const int tid = threadIdx.x, lane = tid & 63;
  const int w = __builtin_amdgcn_readfirstlane(tid >> 6);
  const int r = lane & 31, h = lane >> 5;
  const char* op = ws + WS_O + (size_t)(w * 8) * 65536 + (size_t)(rb * 2) * 1024 + lane * 16;
  const char* wp = ws + WS_WOP + (size_t)(w * 8) * 16384 + (size_t)(cb * 2) * 1024 + lane * 16;
  bf16x8 fo[8][2], fw[8][2];
#pragma unroll
  for (int i = 0; i < 8; ++i) {
    fo[i][0] = *(const bf16x8*)(op + (size_t)i * 65536);
    fw[i][0] = *(const bf16x8*)(wp + (size_t)i * 16384);
    fo[i][1] = *(const bf16x8*)(op + (size_t)i * 65536 + 1024);
    fw[i][1] = *(const bf16x8*)(wp + (size_t)i * 16384 + 1024);
  }
  const int tile = rb * 2 + (w & 1), nb = cb * 2 + (w >> 1);
  const int tok = tile * 32 + r;
  const float g = ((const float*)(ws + WS_GATE))[tok];
  f32x4 bq[4];
  {
    const float* bp = (const float*)(ws + WS_B) + ((size_t)(tile * 16 + nb) * 4 * 64 + lane) * 4;
#pragma unroll
    for (int q = 0; q < 4; ++q) bq[q] = *(const f32x4*)(bp + q * 256);
  }
  __builtin_amdgcn_sched_barrier(0);
  f32x16 a[4];
#pragma unroll
  for (int t = 0; t < 4; ++t)
#pragma unroll
    for (int i = 0; i < 16; ++i) a[t][i] = 0.f;
#pragma unroll
  for (int i = 0; i < 8; ++i) {
#pragma unroll
    for (int t = 0; t < 4; ++t) a[t] = MFMA(fw[i][t >> 1], fo[i][t & 1], a[t]);
  }
#pragma unroll
  for (int t = 0; t < 4; ++t) {
#pragma unroll
    for (int q = 0; q < 4; ++q) {
      f32x4 v = {a[t][4 * q], a[t][4 * q + 1], a[t][4 * q + 2], a[t][4 * q + 3]};
      *(f32x4*)&red[t][w][q][lane][0] = v;
    }
  }
  __syncthreads();
  const float g1 = rbf(1.f - g);
#pragma unroll
  for (int q = 0; q < 4; ++q) {
    f32x4 s = *(const f32x4*)&red[w][0][q][lane][0];
#pragma unroll
    for (int u = 1; u < 4; ++u) s += *(const f32x4*)&red[w][u][q][lane][0];
    u32x2 o;
    o[0] = pk2(g * s[0] + g1 * bq[q][0], g * s[1] + g1 * bq[q][1]);
    o[1] = pk2(g * s[2] + g1 * bq[q][2], g * s[3] + g1 * bq[q][3]);
    *(u32x2*)(out + (size_t)tok * D_ + nb * 32 + 8 * q + 4 * h) = o;
  }
}

extern "C" void kernel_launch(void* const* d_in, const int* in_sizes, int n_in, void* d_out, int out_size,
                              void* d_ws, size_t ws_size, hipStream_t stream) {
  const u16* x = (const u16*)d_in[0];
  const u16* wqkv = (const u16*)d_in[1];
  const u16* wo = (const u16*)d_in[2];
  const u16* w1 = (const u16*)d_in[4];
  const u16* b1 = (const u16*)d_in[5];
  const u16* w2 = (const u16*)d_in[6];
  const u16* gw = (const u16*)d_in[7];
  char* ws = (char*)d_ws;
  k1<<<256, 512, 0, stream>>>(x, wqkv, wo, w1, b1, w2, gw, ws);
  k2<<<256, 768, 0, stream>>>(ws);
  k3<<<256, 256, 0, stream>>>(ws, (u16*)d_out);
}
```

```cpp
#include <hip/hip_runtime.h>
#include <hip/hip_bf16.h>
#include <stdint.h>

typedef short bf16x8 __attribute__((ext_vector_type(8)));
typedef short s16x4 __attribute__((ext_vector_type(4)));
typedef float f32x16 __attribute__((ext_vector_type(16)));
typedef float f32x4 __attribute__((ext_vector_type(4)));
typedef float f32x2 __attribute__((ext_vector_type(2)));
typedef __bf16 bf16x2_t __attribute__((ext_vector_type(2)));
typedef unsigned u32x4 __attribute__((ext_vector_type(4)));
typedef unsigned u32x2 __attribute__((ext_vector_type(2)));
typedef unsigned short u16;
#define DI __device__ __forceinline__
#define MFMA(a, b, c) __builtin_amdgcn_mfma_f32_32x32x16_bf16((a), (b), (c), 0, 0, 0)
#define LAS __attribute__((address_space(3)))

#define KSTAG 0
#define PACE 5
#define LD_PRIO 0
#define FFN_PRIO 0
constexpr int S_ = 2048, D_ = 512, H_ = 32, F_ = 1376;
constexpr unsigned WS_Q = 0, WS_K = 2u << 20, WS_V = 4u << 20, WS_ACT = 6u << 20, WS_O = 12u << 20, WS_W2P = 14u << 20,
                 WS_WOP = 16u << 20, WS_GATE = 17u << 20, WS_KMAX = (17u << 20) + 16384, WS_B = 18u << 20;

DI unsigned pk2(float a, float b) {
  f32x2 v = {a, b};
  bf16x2_t r = __builtin_convertvector(v, bf16x2_t);
  return __builtin_bit_cast(unsigned, r);
}
DI float bf_lo(unsigned u) { return __uint_as_float(u << 16); }
DI float bf_hi(unsigned u) { return __uint_as_float(u & 0xffff0000u); }
DI float rbf(float x) { return bf_lo(pk2(x, 0.f)); }
DI void gldsa16(const void* sbase, unsigned voff, unsigned dst) {
  unsigned keep;
  asm volatile("s_mov_b32 %0, m0\n\ts_mov_b32 m0, %3\n\ts_nop 0\n\tglobal_load_lds_dwordx4 %1, %2\n\ts_mov_b32 m0, %0"
               : "=&s"(keep) : "v"(voff), "s"(sbase), "s"(dst) : "memory");
}
DI void gldsa4(const void* sbase, unsigned voff, unsigned dst) {
  unsigned keep;
  asm volatile("s_mov_b32 %0, m0\n\ts_mov_b32 m0, %3\n\ts_nop 0\n\tglobal_load_lds_dword %1, %2\n\ts_mov_b32 m0, %0"
               : "=&s"(keep) : "v"(voff), "s"(sbase), "s"(dst) : "memory");
}
DI s16x4 ldtr(const char* l) {
  typedef short v4i16_t __attribute__((ext_vector_type(4)));
  return __builtin_amdgcn_ds_read_tr16_b64_v4i16((LAS v4i16_t*)l);
}
#define WAIT_V(n) asm volatile("s_waitcnt vmcnt(" #n ") lgkmcnt(0)" ::: "memory")
#define BAR() __builtin_amdgcn_s_barrier()
template <int CTRL> DI float dppf(float v) {
  return __int_as_float(__builtin_amdgcn_update_dpp(0, __float_as_int(v), CTRL, 0xF, 0xF, true));
}
DI float rdl(float v, int l) { return __int_as_float(__builtin_amdgcn_readlane(__float_as_int(v), l)); }
DI float row16_max(float v) {
  v = fmaxf(v, dppf<0xB1>(v)); v = fmaxf(v, dppf<0x4E>(v)); v = fmaxf(v, dppf<0x141>(v)); v = fmaxf(v, dppf<0x140>(v));
  return v;
}
DI float row16_sum(float v) {
  v += dppf<0xB1>(v); v += dppf<0x4E>(v); v += dppf<0x141>(v); v += dppf<0x140>(v);
  return v;
}
DI float add_half(float v) {
  auto rr = __builtin_amdgcn_permlane32_swap(__float_as_uint(v), __float_as_uint(v), false, false);
  return __uint_as_float(rr[0]) + __uint_as_float(rr[1]);
}
DI float max_half(float v) {
  auto rr = __builtin_amdgcn_permlane32_swap(__float_as_uint(v), __float_as_uint(v), false, false);
  return fmaxf(__uint_as_float(rr[0]), __uint_as_float(rr[1]));
}

#define ST_AUX 16
#define WS_RSRC(ws) __builtin_amdgcn_make_buffer_rsrc((void*)(ws), 0, 32u << 20, 0x00020000)
#define WST16(rs, off, v) __builtin_amdgcn_raw_buffer_store_b128(__builtin_bit_cast(u32x4, v), rs, (unsigned)(off), 0, ST_AUX)

DI float mish_f(float h) {
  float t = fminf(h * 1.4426950408889634f, 60.f);
  float w = __builtin_amdgcn_exp2f(t);
  float a = w + 2.f;
  float u = w * a;
  float d = __builtin_fmaf(w, a, 2.f);
  return h * u * __builtin_amdgcn_rcpf(d);
}

#define K1_LOOP_ASM0 \
  "s_barrier\n\t" \
  "ds_read_b128 v[48:51], v80\n\t" \
  "ds_read_b64_tr_b16 v[52:53], v84 offset:0\n\t" \
  "ds_read_b64_tr_b16 v[54:55], v84 offset:256\n\t" \
  "ds_read_b64_tr_b16 v[56:57], v84 offset:4096\n\t" \
  "ds_read_b64_tr_b16 v[58:59], v84 offset:4352\n\t" \
  "ds_read_b64_tr_b16 v[60:61], v84 offset:8192\n\t" \
  "ds_read_b64_tr_b16 v[62:63], v84 offset:8448\n\t" \
  "1:\n\t" \
  "ds_read_b128 v[64:67], v81\n\t" \
  "ds_read_b64_tr_b16 v[68:69], v84 offset:1024\n\t" \
  "ds_read_b64_tr_b16 v[70:71], v84 offset:1280\n\t" \
  "ds_read_b64_tr_b16 v[72:73], v84 offset:5120\n\t" \
  "ds_read_b64_tr_b16 v[74:75], v84 offset:5376\n\t" \
  "ds_read_b64_tr_b16 v[76:77], v84 offset:9216\n\t" \
  "ds_read_b64_tr_b16 v[78:79], v84 offset:9472\n\t" \
  "s_waitcnt lgkmcnt(7)\n\t" \
  "v_mfma_f32_32x32x16_bf16 v[0:15], v[52:55], v[48:51], v[0:15]\n\t" \
  "v_mfma_f32_32x32x16_bf16 v[16:31], v[56:59], v[48:51], v[16:31]\n\t" \
  "v_mfma_f32_32x32x16_bf16 v[32:47], v[48:51], v[60:63], v[32:47]\n\t" \
  "ds_read_b128 v[48:51], v82\n\t" \
  "ds_read_b64_tr_b16 v[52:53], v84 offset:2048\n\t" \
  "ds_read_b64_tr_b16 v[54:55], v84 offset:2304\n\t" \
  "ds_read_b64_tr_b16 v[56:57], v84 offset:6144\n\t" \
  "ds_read_b64_tr_b16 v[58:59], v84 offset:6400\n\t" \
  "ds_read_b64_tr_b16 v[60:61], v84 offset:10240\n\t" \
  "ds_read_b64_tr_b16 v[62:63], v84 offset:10496\n\t" \
  "s_waitcnt lgkmcnt(7)\n\t" \
  "v_mfma_f32_32x32x16_bf16 v[0:15], v[68:71], v[64:67], v[0:15]\n\t" \
  "v_mfma_f32_32x32x16_bf16 v[16:31], v[72:75], v[64:67], v[16:31]\n\t" \
  "v_mfma_f32_32x32x16_bf16 v[32:47], v[64:67], v[76:79], v[32:47]\n\t" \
  "ds_read_b128 v[64:67], v83\n\t" \
  "ds_read_b64_tr_b16 v[68:69], v84 offset:3072\n\t" \
  "ds_read_b64_tr_b16 v[70:71], v84 offset:3328\n\t" \
  "ds_read_b64_tr_b16 v[72:73], v84 offset:7168\n\t" \
  "ds_read_b64_tr_b16 v[74:75], v84 offset:7424\n\t" \
  "ds_read_b64_tr_b16 v[76:77], v84 offset:11264\n\t" \
  "ds_read_b64_tr_b16 v[78:79], v84 offset:11520\n\t" \
  "s_waitcnt lgkmcnt(7)\n\t" \
  "v_mfma_f32_32x32x16_bf16 v[0:15], v[52:55], v[48:51], v[0:15]\n\t" \
  "v_mfma_f32_32x32x16_bf16 v[16:31], v[56:59], v[48:51], v[16:31]\n\t" \
  "v_mfma_f32_32x32x16_bf16 v[32:47], v[48:51], v[60:63], v[32:47]\n\t" \
  "s_cmp_eq_u32 %[kt], 7\n\t" \
  "s_cbranch_scc1 9f\n\t" \
  "s_add_u32 %[rd], %[rd], 1\n\t" \
  "s_cmp_eq_u32 %[rd], 3\n\t" \
  "s_cselect_b32 %[t0], %[cm80], %[c40]\n\t" \
  "s_cselect_b32 %[rd], 0, %[rd]\n\t" \
  "v_add_u32 v80, %[t0], v80\n\t" \
  "v_add_u32 v81, %[t0], v81\n\t" \
  "v_add_u32 v82, %[t0], v82\n\t" \
  "v_add_u32 v83, %[t0], v83\n\t" \
  "v_add_u32 v84, %[t0], v84\n\t" \
  "s_add_u32 %[kt], %[kt], 1\n\t" \
  "s_waitcnt lgkmcnt(0)\n\t" \
  "s_barrier\n\t" \
  "ds_read_b128 v[48:51], v80\n\t" \
  "ds_read_b64_tr_b16 v[52:53], v84 offset:0\n\t" \
  "ds_read_b64_tr_b16 v[54:55], v84 offset:256\n\t" \
  "ds_read_b64_tr_b16 v[56:57], v84 offset:4096\n\t" \
  "ds_read_b64_tr_b16 v[58:59], v84 offset:4352\n\t" \
  "ds_read_b64_tr_b16 v[60:61], v84 offset:8192\n\t" \
  "ds_read_b64_tr_b16 v[62:63], v84 offset:8448\n\t" \
  "v_mfma_f32_32x32x16_bf16 v[0:15], v[68:71], v[64:67], v[0:15]\n\t" \
  "v_mfma_f32_32x32x16_bf16 v[16:31], v[72:75], v[64:67], v[16:31]\n\t" \
  "v_mfma_f32_32x32x16_bf16 v[32:47], v[64:67], v[76:79], v[32:47]\n\t" \
  "s_branch 1b\n\t" \
  "9:\n\t" \
  "s_waitcnt lgkmcnt(0)\n\t" \
  "v_mfma_f32_32x32x16_bf16 v[0:15], v[68:71], v[64:67], v[0:15]\n\t" \
  "v_mfma_f32_32x32x16_bf16 v[16:31], v[72:75], v[64:67], v[16:31]\n\t" \
  "v_mfma_f32_32x32x16_bf16 v[32:47], v[64:67], v[76:79], v[32:47]\n\t" \
  "s_nop 15\n\t" \
  ""
#define K1_LOOP_ASM1 \
  "s_barrier\n\t" \
  "ds_read_b128 v[48:51], v80\n\t" \
  "ds_read_b64_tr_b16 v[52:53], v84 offset:0\n\t" \
  "ds_read_b64_tr_b16 v[54:55], v84 offset:256\n\t" \
  "ds_read_b64_tr_b16 v[56:57], v84 offset:4096\n\t" \
  "ds_read_b64_tr_b16 v[58:59], v84 offset:4352\n\t" \
  "ds_read_b64_tr_b16 v[60:61], v84 offset:8192\n\t" \
  "ds_read_b64_tr_b16 v[62:63], v84 offset:8448\n\t" \
  "1:\n\t" \
  "ds_read_b128 v[64:67], v81\n\t" \
  "ds_read_b64_tr_b16 v[68:69], v84 offset:1024\n\t" \
  "ds_read_b64_tr_b16 v[70:71], v84 offset:1280\n\t" \
  "ds_read_b64_tr_b16 v[72:73], v84 offset:5120\n\t" \
  "ds_read_b64_tr_b16 v[74:75], v84 offset:5376\n\t" \
  "ds_read_b64_tr_b16 v[76:77], v84 offset:9216\n\t" \
  "ds_read_b64_tr_b16 v[78:79], v84 offset:9472\n\t" \
  "s_waitcnt lgkmcnt(7)\n\t" \
  "v_mfma_f32_32x32x16_bf16 v[0:15], v[52:55], v[48:51], v[0:15]\n\t" \
  "v_mfma_f32_32x32x16_bf16 v[16:31], v[56:59], v[48:51], v[16:31]\n\t" \
  "v_mfma_f32_32x32x16_bf16 v[32:47], v[60:63], v[48:51], v[32:47]\n\t" \
  "ds_read_b128 v[48:51], v82\n\t" \
  "ds_read_b64_tr_b16 v[52:53], v84 offset:2048\n\t" \
  "ds_read_b64_tr_b16 v[54:55], v84 offset:2304\n\t" \
  "ds_read_b64_tr_b16 v[56:57], v84 offset:6144\n\t" \
  "ds_read_b64_tr_b16 v[58:59], v84 offset:6400\n\t" \
  "ds_read_b64_tr_b16 v[60:61], v84 offset:10240\n\t" \
  "ds_read_b64_tr_b16 v[62:63], v84 offset:10496\n\t" \
  "s_waitcnt lgkmcnt(7)\n\t" \
  "v_mfma_f32_32x32x16_bf16 v[0:15], v[68:71], v[64:67], v[0:15]\n\t" \
  "v_mfma_f32_32x32x16_bf16 v[16:31], v[72:75], v[64:67], v[16:31]\n\t" \
  "v_mfma_f32_32x32x16_bf16 v[32:47], v[76:79], v[64:67], v[32:47]\n\t" \
  "ds_read_b128 v[64:67], v83\n\t" \
  "ds_read_b64_tr_b16 v[68:69], v84 offset:3072\n\t" \
  "ds_read_b64_tr_b16 v[70:71], v84 offset:3328\n\t" \
  "ds_read_b64_tr_b16 v[72:73], v84 offset:7168\n\t" \
  "ds_read_b64_tr_b16 v[74:75], v84 offset:7424\n\t" \
  "ds_read_b64_tr_b16 v[76:77], v84 offset:11264\n\t" \
  "ds_read_b64_tr_b16 v[78:79], v84 offset:11520\n\t" \
  "s_waitcnt lgkmcnt(7)\n\t" \
  "v_mfma_f32_32x32x16_bf16 v[0:15], v[52:55], v[48:51], v[0:15]\n\t" \
  "v_mfma_f32_32x32x16_bf16 v[16:31], v[56:59], v[48:51], v[16:31]\n\t" \
  "v_mfma_f32_32x32x16_bf16 v[32:47], v[60:63], v[48:51], v[32:47]\n\t" \
  "s_cmp_eq_u32 %[kt], 7\n\t" \
  "s_cbranch_scc1 9f\n\t" \
  "s_add_u32 %[rd], %[rd], 1\n\t" \
  "s_cmp_eq_u32 %[rd], 3\n\t" \
  "s_cselect_b32 %[t0], %[cm80], %[c40]\n\t" \
  "s_cselect_b32 %[rd], 0, %[rd]\n\t" \
  "v_add_u32 v80, %[t0], v80\n\t" \
  "v_add_u32 v81, %[t0], v81\n\t" \
  "v_add_u32 v82, %[t0], v82\n\t" \
  "v_add_u32 v83, %[t0], v83\n\t" \
  "v_add_u32 v84, %[t0], v84\n\t" \
  "s_add_u32 %[kt], %[kt], 1\n\t" \
  "s_waitcnt lgkmcnt(0)\n\t" \
  "s_barrier\n\t" \
  "ds_read_b128 v[48:51], v80\n\t" \
  "ds_read_b64_tr_b16 v[52:53], v84 offset:0\n\t" \
  "ds_read_b64_tr_b16 v[54:55], v84 offset:256\n\t" \
  "ds_read_b64_tr_b16 v[56:57], v84 offset:4096\n\t" \
  "ds_read_b64_tr_b16 v[58:59], v84 offset:4352\n\t" \
  "ds_read_b64_tr_b16 v[60:61], v84 offset:8192\n\t" \
  "ds_read_b64_tr_b16 v[62:63], v84 offset:8448\n\t" \
  "v_mfma_f32_32x32x16_bf16 v[0:15], v[68:71], v[64:67], v[0:15]\n\t" \
  "v_mfma_f32_32x32x16_bf16 v[16:31], v[72:75], v[64:67], v[16:31]\n\t" \
  "v_mfma_f32_32x32x16_bf16 v[32:47], v[76:79], v[64:67], v[32:47]\n\t" \
  "s_branch 1b\n\t" \
  "9:\n\t" \
  "s_waitcnt lgkmcnt(0)\n\t" \
  "v_mfma_f32_32x32x16_bf16 v[0:15], v[68:71], v[64:67], v[0:15]\n\t" \
  "v_mfma_f32_32x32x16_bf16 v[16:31], v[72:75], v[64:67], v[16:31]\n\t" \
  "v_mfma_f32_32x32x16_bf16 v[32:47], v[76:79], v[64:67], v[32:47]\n\t" \
  "s_nop 15\n\t" \
  ""

constexpr int K1_SIDE = 3 * 40960;
__global__ __launch_bounds__(768) void k1(const u16* __restrict__ x, const u16* __restrict__ wqkv,
                                          const u16* __restrict__ wo, const u16* __restrict__ w1,
                                          const u16* __restrict__ b1, const u16* __restrict__ w2,
                                          const u16* __restrict__ gw, char* __restrict__ ws) {
  __shared__ __attribute__((aligned(16))) char lds[K1_SIDE + 4 * 5120 + 256];
  const int bid = blockIdx.x, xcd = bid & 7, idx = bid >> 3;
  const int rb = (xcd & 1) * 8 + (idx & 7), j = (xcd >> 1) * 4 + (idx >> 3);
  const int tid = threadIdx.x, lane = tid & 63;
  const int w = __builtin_amdgcn_readfirstlane(tid >> 6);
  const int r = lane & 31, h = lane >> 5, m = w & 3, nh = w >> 2;
  const unsigned lb = (unsigned)(size_t)(LAS char*)lds;
  const int tile = rb * 4 + m;
  const int q4 = (lane & 15) >> 2, p4 = lane & 3, grp = (lane >> 4) & 1;
  const auto rs = WS_RSRC(ws);
  if (w >= 8) {
    const unsigned xo0 = (unsigned)((lane >> 3) * D_ + (((lane & 7) ^ ((lane >> 4) & 7)) * 8)) * 2u;
    const unsigned xo1 = (unsigned)((8 + (lane >> 3)) * D_ + (((lane & 7) ^ ((4 + (lane >> 4)) & 7)) * 8)) * 2u;
    const unsigned wqo = (unsigned)((lane >> 2) * 1536 + (lane & 3) * 8) * 2u;
    const unsigned w1o = (unsigned)((lane >> 2) * F_ + (lane & 3) * 8) * 2u;
    const int fb0 = min(3 * j, 42), fb1 = min(3 * j + 1, 42), fb2 = min(3 * j + 2, 42);
    const char* xb = (const char*)(x + (size_t)(rb * 128 + 32 * m) * D_);
    const char* wqb = (const char*)(wqkv + (size_t)(16 * m) * 1536 + 64 * j);
    const char* w1b = (const char*)(w1 + (size_t)(16 * m) * F_);
    const unsigned vo_ = wqo + 2048 - 64 * j;
    const unsigned f0o = w1o + 64 * fb0, f1o = w1o + 64 * fb1, f2o = w1o + 64 * fb2;
    unsigned xl = lb + m * 4096, wl = lb + 16384 + m * 1024;
#define STAGE()                                                                                   \
  do {                                                                                            \
    gldsa16(xb, xo0, xl);                                                                         \
    gldsa16(wqb, wqo, wl);                                                                        \
    gldsa16(w1b, f0o, wl + 12288);                                                                \
    gldsa16(xb, xo1, xl + 1024);                                                                  \
    gldsa16(wqb, wqo + 64, wl + 4096);                                                            \
    gldsa16(w1b, f1o, wl + 16384);                                                                \
    gldsa16(xb + 16 * D_ * 2, xo0, xl + 2048);                                                    \
    gldsa16(wqb, vo_, wl + 8192);                                                                 \
    gldsa16(w1b, f2o, wl + 20480);                                                                \
    gldsa16(xb + 16 * D_ * 2, xo1, xl + 3072);                                                    \
    xb += 128; wqb += 128 * 1536; w1b += 128 * F_;                                                \
    const bool wrap_ = xl + 40960 >= lb + 3 * 40960;                                              \
    xl += wrap_ ? 0u - 81920u : 40960u; wl += wrap_ ? 0u - 81920u : 40960u;                       \
  } while (0)
    STAGE();
    STAGE();
    const int fid = bid * 8 + 2 * m;
    const int tok = rb * 128 + j * 8 + 2 * m;
    const unsigned sl = lb + K1_SIDE + m * 5120;
    {
      const int fc = min(fid, 1886);
      const u16* psrc = fc < 1376 ? w2 + (size_t)(fc >> 4) * 16 * D_ + (fc & 15) * 32
                                  : wo + (size_t)((fc - 1376) >> 4) * 16 * D_ + (fc & 15) * 32;
      const unsigned po = (unsigned)((lane >> 2) * D_ + (lane & 3) * 8) * 2u;
      gldsa16(psrc, po, sl);
      gldsa16(psrc, po + 64, sl + 1024);
      gldsa16(x + (size_t)tok * D_, lane * 16, sl + 2048);
      gldsa16(x + (size_t)tok * D_, lane * 16 + 1024, sl + 3072);
      gldsa16(gw, lane * 16, sl + 4096);
      gldsa4(b1, (unsigned)min(96 * j + 2 * lane, F_ - 2) * 2u, lb + K1_SIDE + 20480);
    }
    WAIT_V(16); BAR();
    STAGE();
    WAIT_V(16); BAR();
#pragma unroll 1
    for (int kt = 1; kt < 6; ++kt) {
      STAGE();
      WAIT_V(10); BAR();
    }
#undef STAGE
    WAIT_V(0); BAR();
    const char* sp = lds + K1_SIDE + m * 5120;
#pragma unroll
    for (int c = 0; c < 2; ++c) {
      const char* pp = sp + c * 1024 + (4 * h + q4) * 64 + grp * 32 + p4 * 8;
      const s16x4 lo = ldtr(pp), hi = ldtr(pp + 512);
      const bf16x8 f = __builtin_shufflevector(lo, hi, 0, 1, 2, 3, 4, 5, 6, 7);
      const int fi = fid + c;
      if (fi < 1888) WST16(rs, (fi < 1376 ? WS_W2P + fi * 1024 : WS_WOP + (fi - 1376) * 1024) + lane * 16, f);
    }
    const u32x4 gg = *(const u32x4*)(sp + 4096 + lane * 16);
#pragma unroll
    for (int t = 0; t < 2; ++t) {
      const u32x4 gx = *(const u32x4*)(sp + 2048 + t * 1024 + lane * 16);
      float s = 0.f;
#pragma unroll
      for (int e = 0; e < 4; ++e) {
        s += rbf(bf_lo(gx[e]) * bf_lo(gg[e]));
        s += rbf(bf_hi(gx[e]) * bf_hi(gg[e]));
      }
      s = row16_sum(s);
      s = (rdl(s, 0) + rdl(s, 16)) + (rdl(s, 32) + rdl(s, 48));
      s = rbf(s);
      const float g = rbf(__builtin_amdgcn_rcpf(1.f + __builtin_amdgcn_exp2f(-s * 1.4426950408889634f)));
      if (lane == 0) ((float*)(ws + WS_GATE))[tok + t] = g;
    }
    return;
  }

  f32x16 acc[3];
#pragma unroll
  for (int b = 0; b < 3; ++b)
#pragma unroll
    for (int i = 0; i < 16; ++i) acc[b][i] = 0.f;
  {
    const int sw = (r >> 1) & 7;
    const unsigned xr0 = lb + m * 4096 + r * 128;
    unsigned xa0 = xr0 + ((0 + h) ^ sw) * 16, xa1 = xr0 + ((2 + h) ^ sw) * 16, xa2 = xr0 + ((4 + h) ^ sw) * 16,
             xa3 = xr0 + ((6 + h) ^ sw) * 16;
    unsigned wa = lb + 16384 + nh * 12288 + (8 * h + q4) * 64 + grp * 32 + p4 * 8;
    unsigned kt = 0, rd = 0, t0;
#define K1_RUN(ASM)                                                                                            \
    asm volatile(ASM                                                                                           \
                 : "+{v[0:15]}"(acc[0]), "+{v[16:31]}"(acc[1]), "+{v[32:47]}"(acc[2]), "+{v80}"(xa0),          \
                   "+{v81}"(xa1), "+{v82}"(xa2), "+{v83}"(xa3), "+{v84}"(wa), [kt] "+s"(kt), [rd] "+s"(rd),    \
                   [t0] "=&s"(t0)                                                                              \
                 : [c40] "s"(40960u), [cm80] "s"(0u - 81920u)                                                  \
                 : "memory", "scc", "v48", "v49", "v50", "v51", "v52", "v53", "v54", "v55", "v56", "v57",      \
                   "v58", "v59", "v60", "v61", "v62", "v63", "v64", "v65", "v66", "v67", "v68", "v69", "v70",  \
                   "v71", "v72", "v73", "v74", "v75", "v76", "v77", "v78", "v79")
    if (nh) K1_RUN(K1_LOOP_ASM1); else K1_RUN(K1_LOOP_ASM0);
  }
  const char* bsp = lds + K1_SIDE + 20480;
  if (nh == 0) {
#pragma unroll
    for (int b = 0; b < 2; ++b) {
      const int c0 = 64 * j + 32 * b;
      const bool isq = c0 < 512;
      const float sc = isq ? 0.36067376022224085f : 1.0f;
      const int head0 = (c0 & 511) >> 4;
      const unsigned dst = isq ? WS_Q : WS_K;
#pragma unroll
      for (int hh = 0; hh < 2; ++hh) {
        u32x4 o;
#pragma unroll
        for (int e = 0; e < 4; ++e) o[e] = pk2(acc[b][8 * hh + 2 * e] * sc, acc[b][8 * hh + 2 * e + 1] * sc);
        WST16(rs, dst + (((head0 + hh) * 64 + tile) * 64 + lane) * 16, o);
        if (!isq) {
          float ss = 0.f;
#pragma unroll
          for (int e = 0; e < 4; ++e) {
            const float a = bf_lo(o[e]), c = bf_hi(o[e]);
            ss = __builtin_fmaf(a, a, ss);
            ss = __builtin_fmaf(c, c, ss);
          }
          ss = row16_max(add_half(ss));
          ss = fmaxf(rdl(ss, 0), rdl(ss, 16));
          if (lane == 0) ((float*)(ws + WS_KMAX))[(head0 + hh) * 64 + tile] = ss;
        }
      }
    }
    {
      const int head = 2 * j + (r >> 4), d = r & 15;
#pragma unroll
      for (int s = 0; s < 2; ++s) {
        u32x4 o;
#pragma unroll
        for (int e = 0; e < 4; ++e) o[e] = pk2(acc[2][8 * s + 2 * e], acc[2][8 * s + 2 * e + 1]);
        WST16(rs, WS_V + (head * 64 + tile) * 1024 + s * 512 + h * 256 + d * 16, o);
      }
    }
  } else {
#pragma unroll
    for (int b = 0; b < 3; ++b) {
      if (3 * j + b < 43) {
#pragma unroll
        for (int hh = 0; hh < 2; ++hh) {
          u32x4 o;
#pragma unroll
          for (int gq = 0; gq < 2; ++gq) {
            const int g = 2 * hh + gq;
            const u32x2 bb = *(const u32x2*)(bsp + (32 * b + 8 * g + 4 * h) * 2);
            const float h0 = acc[b][4 * g + 0] + bf_lo(bb[0]);
            const float h1 = acc[b][4 * g + 1] + bf_hi(bb[0]);
            const float h2 = acc[b][4 * g + 2] + bf_lo(bb[1]);
            const float h3 = acc[b][4 * g + 3] + bf_hi(bb[1]);
            o[2 * gq] = pk2(mish_f(h0), mish_f(h1));
            o[2 * gq + 1] = pk2(mish_f(h2), mish_f(h3));
          }
          WST16(rs, WS_ACT + (((2 * (3 * j + b) + hh) * 64 + tile) * 64 + lane) * 16, o);
        }
      }
    }
  }
}

#define ATTN_LOOP_ASM \
  "1:\n\t" \
  "v_exp_f32 v32, v32\n\t" \
  "v_exp_f32 v33, v33\n\t" \
  "v_exp_f32 v34, v34\n\t" \
  "v_cvt_pk_bf16_f32 v88, v32, v33\n\t" \
  "v_mfma_f32_32x32x16_bf16 v[0:15], v[84:87], v[92:95], v[0:15]\n\t" \
  "s_mov_b64 exec, %[mask]\n\t" \
  "ds_read_b128 v[80:83], v97 offset:1024\n\t" \
  "ds_read_b128 v[84:87], v97 offset:1536\n\t" \
  "s_mov_b64 exec, -1\n\t" \
  "v_exp_f32 v35, v35\n\t" \
  "v_exp_f32 v36, v36\n\t" \
  "v_cvt_pk_bf16_f32 v89, v34, v35\n\t" \
  "v_exp_f32 v37, v37\n\t" \
  "v_exp_f32 v38, v38\n\t" \
  "v_cvt_pk_bf16_f32 v90, v36, v37\n\t" \
  "v_exp_f32 v39, v39\n\t" \
  "v_exp_f32 v40, v40\n\t" \
  "v_cvt_pk_bf16_f32 v91, v38, v39\n\t" \
  "v_exp_f32 v41, v41\n\t" \
  "v_exp_f32 v42, v42\n\t" \
  "v_mfma_f32_32x32x16_bf16 v[0:15], v[72:75], v[88:91], v[0:15]\n\t" \
  "v_cvt_pk_bf16_f32 v92, v40, v41\n\t" \
  "v_exp_f32 v43, v43\n\t" \
  "v_exp_f32 v44, v44\n\t" \
  "v_cvt_pk_bf16_f32 v93, v42, v43\n\t" \
  "v_exp_f32 v45, v45\n\t" \
  "v_exp_f32 v46, v46\n\t" \
  "v_cvt_pk_bf16_f32 v94, v44, v45\n\t" \
  "v_exp_f32 v47, v47\n\t" \
  "s_waitcnt lgkmcnt(0)\n\t" \
  "v_cvt_pk_bf16_f32 v95, v46, v47\n\t" \
  "v_mfma_f32_32x32x16_bf16 v[32:47], v[68:71], v[64:67], v[16:31]\n\t" \
  "ds_read_b128 v[68:71], v96 offset:3072\n\t" \
  "v_exp_f32 v48, v48\n\t" \
  "v_exp_f32 v49, v49\n\t" \
  "v_exp_f32 v50, v50\n\t" \
  "v_cvt_pk_bf16_f32 v88, v48, v49\n\t" \
  "v_mfma_f32_32x32x16_bf16 v[0:15], v[76:79], v[92:95], v[0:15]\n\t" \
  "s_mov_b64 exec, %[mask]\n\t" \
  "ds_read_b128 v[72:75], v97 offset:2048\n\t" \
  "ds_read_b128 v[76:79], v97 offset:2560\n\t" \
  "s_mov_b64 exec, -1\n\t" \
  "v_exp_f32 v51, v51\n\t" \
  "v_exp_f32 v52, v52\n\t" \
  "v_cvt_pk_bf16_f32 v89, v50, v51\n\t" \
  "v_exp_f32 v53, v53\n\t" \
  "v_exp_f32 v54, v54\n\t" \
  "v_cvt_pk_bf16_f32 v90, v52, v53\n\t" \
  "v_exp_f32 v55, v55\n\t" \
  "v_exp_f32 v56, v56\n\t" \
  "v_cvt_pk_bf16_f32 v91, v54, v55\n\t" \
  "v_exp_f32 v57, v57\n\t" \
  "v_exp_f32 v58, v58\n\t" \
  "v_mfma_f32_32x32x16_bf16 v[0:15], v[80:83], v[88:91], v[0:15]\n\t" \
  "v_cvt_pk_bf16_f32 v92, v56, v57\n\t" \
  "v_exp_f32 v59, v59\n\t" \
  "v_exp_f32 v60, v60\n\t" \
  "v_cvt_pk_bf16_f32 v93, v58, v59\n\t" \
  "v_exp_f32 v61, v61\n\t" \
  "v_exp_f32 v62, v62\n\t" \
  "v_cvt_pk_bf16_f32 v94, v60, v61\n\t" \
  "v_exp_f32 v63, v63\n\t" \
  "s_waitcnt lgkmcnt(0)\n\t" \
  "v_cvt_pk_bf16_f32 v95, v62, v63\n\t" \
  "v_mfma_f32_32x32x16_bf16 v[48:63], v[68:71], v[64:67], v[16:31]\n\t" \
  "ds_read_b128 v[68:71], v96 offset:4096\n\t" \
  "v_exp_f32 v32, v32\n\t" \
  "v_exp_f32 v33, v33\n\t" \
  "v_exp_f32 v34, v34\n\t" \
  "v_cvt_pk_bf16_f32 v88, v32, v33\n\t" \
  "v_mfma_f32_32x32x16_bf16 v[0:15], v[84:87], v[92:95], v[0:15]\n\t" \
  "s_mov_b64 exec, %[mask]\n\t" \
  "ds_read_b128 v[80:83], v97 offset:3072\n\t" \
  "ds_read_b128 v[84:87], v97 offset:3584\n\t" \
  "s_mov_b64 exec, -1\n\t" \
  "v_exp_f32 v35, v35\n\t" \
  "v_exp_f32 v36, v36\n\t" \
  "v_cvt_pk_bf16_f32 v89, v34, v35\n\t" \
  "v_exp_f32 v37, v37\n\t" \
  "v_exp_f32 v38, v38\n\t" \
  "v_cvt_pk_bf16_f32 v90, v36, v37\n\t" \
  "v_exp_f32 v39, v39\n\t" \
  "v_exp_f32 v40, v40\n\t" \
  "v_cvt_pk_bf16_f32 v91, v38, v39\n\t" \
  "v_exp_f32 v41, v41\n\t" \
  "v_exp_f32 v42, v42\n\t" \
  "v_mfma_f32_32x32x16_bf16 v[0:15], v[72:75], v[88:91], v[0:15]\n\t" \
  "v_cvt_pk_bf16_f32 v92, v40, v41\n\t" \
  "v_exp_f32 v43, v43\n\t" \
  "v_exp_f32 v44, v44\n\t" \
  "v_cvt_pk_bf16_f32 v93, v42, v43\n\t" \
  "v_exp_f32 v45, v45\n\t" \
  "v_exp_f32 v46, v46\n\t" \
  "v_cvt_pk_bf16_f32 v94, v44, v45\n\t" \
  "v_exp_f32 v47, v47\n\t" \
  "s_waitcnt lgkmcnt(0)\n\t" \
  "v_cvt_pk_bf16_f32 v95, v46, v47\n\t" \
  "v_mfma_f32_32x32x16_bf16 v[32:47], v[68:71], v[64:67], v[16:31]\n\t" \
  "ds_read_b128 v[68:71], v96 offset:5120\n\t" \
  "v_exp_f32 v48, v48\n\t" \
  "v_exp_f32 v49, v49\n\t" \
  "v_exp_f32 v50, v50\n\t" \
  "v_cvt_pk_bf16_f32 v88, v48, v49\n\t" \
  "v_mfma_f32_32x32x16_bf16 v[0:15], v[76:79], v[92:95], v[0:15]\n\t" \
  "s_mov_b64 exec, %[mask]\n\t" \
  "ds_read_b128 v[72:75], v97 offset:4096\n\t" \
  "ds_read_b128 v[76:79], v97 offset:4608\n\t" \
  "s_mov_b64 exec, -1\n\t" \
  "v_exp_f32 v51, v51\n\t" \
  "v_exp_f32 v52, v52\n\t" \
  "v_cvt_pk_bf16_f32 v89, v50, v51\n\t" \
  "v_exp_f32 v53, v53\n\t" \
  "v_exp_f32 v54, v54\n\t" \
  "v_cvt_pk_bf16_f32 v90, v52, v53\n\t" \
  "v_exp_f32 v55, v55\n\t" \
  "v_exp_f32 v56, v56\n\t" \
  "v_cvt_pk_bf16_f32 v91, v54, v55\n\t" \
  "v_exp_f32 v57, v57\n\t" \
  "v_exp_f32 v58, v58\n\t" \
  "v_mfma_f32_32x32x16_bf16 v[0:15], v[80:83], v[88:91], v[0:15]\n\t" \
  "v_cvt_pk_bf16_f32 v92, v56, v57\n\t" \
  "v_exp_f32 v59, v59\n\t" \
  "v_exp_f32 v60, v60\n\t" \
  "v_cvt_pk_bf16_f32 v93, v58, v59\n\t" \
  "v_exp_f32 v61, v61\n\t" \
  "v_exp_f32 v62, v62\n\t" \
  "v_cvt_pk_bf16_f32 v94, v60, v61\n\t" \
  "v_exp_f32 v63, v63\n\t" \
  "s_waitcnt lgkmcnt(0)\n\t" \
  "v_cvt_pk_bf16_f32 v95, v62, v63\n\t" \
  "v_mfma_f32_32x32x16_bf16 v[48:63], v[68:71], v[64:67], v[16:31]\n\t" \
  "ds_read_b128 v[68:71], v96 offset:6144\n\t" \
  "v_add_u32 v96, 0x1000, v96\n\t" \
  "v_add_u32 v97, 0x1000, v97\n\t" \
  "s_sub_u32 %[cnt], %[cnt], 1\n\t" \
  "s_and_b32 %[tmp], %[cnt], 13\n\t" \
  "s_cmp_lg_u32 %[tmp], 12\n\t" \
  "s_cbranch_scc1 2f\n\t" \
  "s_barrier\n\t" \
  "2:\n\t" \
  "s_cmp_lg_u32 %[cnt], 0\n\t" \
  "s_cbranch_scc1 1b\n\t" \
  "s_waitcnt lgkmcnt(0)\n\t" \
  "s_nop 15\n\t" \
  ""

__global__ __launch_bounds__(768) void k2(char* __restrict__ ws) {
  __shared__ __attribute__((aligned(16))) char lds[131072 + 4096];
  const int bid = blockIdx.x, xcd = bid & 7, idx = bid >> 3;
  const int head = xcd * 4 + (idx & 3), qb = idx >> 2;
  const int tid = threadIdx.x, lane = tid & 63;
  const int w = __builtin_amdgcn_readfirstlane(tid >> 6);
  const int h = lane >> 5;
  const unsigned lb = (unsigned)(size_t)(LAS char*)lds;
  const unsigned l16 = lane * 16;

  if (w >= 8) {
    __builtin_amdgcn_s_setprio(LD_PRIO);
    const int L = w - 8;
    const char* ksrc = ws + WS_K + (size_t)(head * 64 + L) * 1024;
    const char* vsrc = ws + WS_V + (size_t)(head * 64 + L) * 1024;
    const unsigned kdst = lb + L * 1024, vdst = lb + 65536 + L * 1024;
#pragma unroll
    for (int c = 0; c < 3; ++c) { gldsa16(ksrc + c * 4096, l16, kdst + c * 4096); gldsa16(vsrc + c * 4096, l16, vdst + c * 4096); }
    WAIT_V(0); BAR();
#pragma unroll 1
    for (int c = 3; c < 11; ++c) {
      gldsa16(ksrc + c * 4096, l16, kdst + c * 4096); gldsa16(vsrc + c * 4096, l16, vdst + c * 4096);
      __builtin_amdgcn_s_sleep(PACE);
    }
    WAIT_V(4); BAR();
#pragma unroll 1
    for (int c = 11; c < 16; ++c) {
      gldsa16(ksrc + c * 4096, l16, kdst + c * 4096); gldsa16(vsrc + c * 4096, l16, vdst + c * 4096);
      __builtin_amdgcn_s_sleep(PACE);
    }
    WAIT_V(0);
    BAR();
    __builtin_amdgcn_s_setprio(FFN_PRIO);
    const int rb = (xcd & 3) * 8 + (idx & 7), cb = (xcd >> 2) * 4 + (idx >> 3);
    const int tile = rb * 2 + (L & 1), nb = cb * 2 + (L >> 1);
    const auto ra = __builtin_amdgcn_make_buffer_rsrc((void*)(ws + WS_ACT + (size_t)tile * 1024), 0, 85 * 65536 + 1024, 0x00020000);
    const auto rw = __builtin_amdgcn_make_buffer_rsrc((void*)(ws + WS_W2P + (size_t)nb * 1024), 0, 85 * 16384 + 1024, 0x00020000);
    f32x16 b;
#pragma unroll
    for (int i = 0; i < 16; ++i) b[i] = 0.f;
    constexpr int PD = 8;
    u32x4 fa[PD], fw[PD];
#pragma unroll
    for (int i = 0; i < PD; ++i) {
      fa[i] = __builtin_amdgcn_raw_buffer_load_b128(ra, l16, i * 65536, 0);
      fw[i] = __builtin_amdgcn_raw_buffer_load_b128(rw, l16, i * 16384, 0);
    }
    __builtin_amdgcn_sched_barrier(0);
#pragma unroll 1
    for (int k0 = PD; k0 < 88 + PD; k0 += PD) {
#pragma unroll
      for (int i = 0; i < PD; ++i) {
        b = MFMA(__builtin_bit_cast(bf16x8, fw[i]), __builtin_bit_cast(bf16x8, fa[i]), b);
        fa[i] = __builtin_amdgcn_raw_buffer_load_b128(ra, l16, (k0 + i) * 65536, 0);
        fw[i] = __builtin_amdgcn_raw_buffer_load_b128(rw, l16, (k0 + i) * 16384, 0);
        __builtin_amdgcn_sched_barrier(0);
      }
    }
    const auto rs = WS_RSRC(ws);
#pragma unroll
    for (int q = 0; q < 4; ++q) {
      f32x4 o = {b[4 * q], b[4 * q + 1], b[4 * q + 2], b[4 * q + 3]};
      WST16(rs, WS_B + (tile * 16 + nb) * 4096 + q * 1024 + l16, o);
    }
    return;
  }

  const int qt = qb * 8 + w;
  bf16x8 qf = *(const bf16x8*)(ws + WS_Q + (size_t)(head * 64 + qt) * 1024 + l16);
  float km = ((const float*)(ws + WS_KMAX))[head * 64 + lane];
  km = row16_max(km);
  km = fmaxf(fmaxf(rdl(km, 0), rdl(km, 16)), fmaxf(rdl(km, 32), rdl(km, 48)));
  float qs = 0.f;
  {
    const u32x4 qu = __builtin_bit_cast(u32x4, qf);
#pragma unroll
    for (int e = 0; e < 4; ++e) {
      const float a = bf_lo(qu[e]), c = bf_hi(qu[e]);
      qs = __builtin_fmaf(a, a, qs);
      qs = __builtin_fmaf(c, c, qs);
    }
    qs = add_half(qs);
  }
  const float mb = __builtin_amdgcn_sqrtf(qs * km) * 1.0001f;
  f32x16 negm, O;
#pragma unroll
  for (int i = 0; i < 16; ++i) { negm[i] = -mb; O[i] = 0.f; }

  const char* kp = lds + lane * 16;
  const char* vp = lds + 65536 + h * 256 + (lane & 15) * 16;
  const bool vact = (lane & 16) == 0;
  const short one = ((lane & 27) == 16) ? (short)0x3F80 : (short)0;
  bf16x8 va0, va1, vb0, vb1;
#pragma unroll
  for (int i = 0; i < 8; ++i) { va0[i] = one; va1[i] = one; vb0[i] = one; vb1[i] = one; }
  u32x4 p0 = {0, 0, 0, 0}, p1 = {0, 0, 0, 0};
  unsigned kaddr = lb + l16, vaddr = lb + 65536 + h * 256 + (lane & 15) * 16;
  const unsigned long long vmask = 0x0000FFFF0000FFFFull;

  BAR();
  asm volatile("" ::: "memory");
  f32x16 Sa = MFMA(*(const bf16x8*)(kp), qf, negm);
  f32x16 Sb = MFMA(*(const bf16x8*)(kp + 1024), qf, negm);
  bf16x8 kf = *(const bf16x8*)(kp + 2048);
  if (vact) { va0 = *(const bf16x8*)(vp); va1 = *(const bf16x8*)(vp + 512); }

#define ATTN_GROUPS(n)                                                                                       \
  do {                                                                                                       \
    unsigned cnt = (n), tmp;                                                                                 \
    asm volatile("s_nop 15\n\t" ATTN_LOOP_ASM                                                                \
                 : "+{v[0:15]}"(O), "+{v[32:47]}"(Sa), "+{v[48:63]}"(Sb), "+{v[68:71]}"(kf),                 \
                   "+{v[72:75]}"(va0), "+{v[76:79]}"(va1), "+{v[80:83]}"(vb0), "+{v[84:87]}"(vb1),           \
                   "+{v[88:91]}"(p0), "+{v[92:95]}"(p1), "+{v96}"(kaddr), "+{v97}"(vaddr), [cnt] "+s"(cnt), [tmp] "=&s"(tmp) \
                 : "{v[16:31]}"(negm), "{v[64:67]}"(qf), [mask] "s"(vmask)                                   \
                 : "memory", "scc");                                                                         \
  } while (0)

  ATTN_GROUPS(16);
  O = MFMA(vb1, __builtin_bit_cast(bf16x8, p1), O);

#define SOFTMAX_PV(Sx, v0, v1)                                                                     \
  do {                                                                                             \
    _Pragma("unroll") for (int e = 0; e < 4; ++e) {                                                \
      p0[e] = pk2(__builtin_amdgcn_exp2f(Sx[2 * e]), __builtin_amdgcn_exp2f(Sx[2 * e + 1]));       \
      p1[e] = pk2(__builtin_amdgcn_exp2f(Sx[8 + 2 * e]), __builtin_amdgcn_exp2f(Sx[8 + 2 * e + 1])); \
    }                                                                                              \
    O = MFMA(v0, __builtin_bit_cast(bf16x8, p0), O);                                               \
    O = MFMA(v1, __builtin_bit_cast(bf16x8, p1), O);                                               \
  } while (0)

  float l = O[8];
  if (__any(!(l >= 8.271806125530277e-25f))) {
    float m = -INFINITY;
    f32x16 zero;
#pragma unroll
    for (int i = 0; i < 16; ++i) { zero[i] = 0.f; O[i] = 0.f; }
    for (int t = 0; t < 64; ++t) {
      f32x16 Sx = MFMA(*(const bf16x8*)(kp + t * 1024), qf, zero);
      float mx = Sx[0];
#pragma unroll
      for (int i = 1; i < 16; ++i) mx = fmaxf(mx, Sx[i]);
      mx = max_half(mx);
      const float mn = fmaxf(m, mx);
      const float alpha = __builtin_amdgcn_exp2f(m - mn);
      m = mn;
#pragma unroll
      for (int i = 0; i < 16; ++i) { O[i] *= alpha; Sx[i] -= mn; }
      if (vact) { va0 = *(const bf16x8*)(vp + t * 1024); va1 = *(const bf16x8*)(vp + t * 1024 + 512); }
      SOFTMAX_PV(Sx, va0, va1);
    }
    l = O[8];
  }
  const float rl = 1.0f / l;
  u32x4 o;
#pragma unroll
  for (int e = 0; e < 4; ++e) o[e] = pk2(O[2 * e] * rl, O[2 * e + 1] * rl);
  WST16(WS_RSRC(ws), WS_O + (head * 64 + qt) * 1024 + l16, o);
}

__global__ __launch_bounds__(256) void k3(const char* __restrict__ ws, u16* __restrict__ out) {
  __shared__ __attribute__((aligned(16))) float red[4][4][4][64][4];
  const int bid = blockIdx.x, xcd = bid & 7, idx = bid >> 3;
  const int rb = (xcd & 3) * 8 + (idx & 7), cb = (xcd >> 2) * 4 + (idx >> 3);
  const int tid = threadIdx.x, lane = tid & 63;
  const int w = __builtin_amdgcn_readfirstlane(tid >> 6);
  const int r = lane & 31, h = lane >> 5;
  const char* op = ws + WS_O + (size_t)(w * 8) * 65536 + (size_t)(rb * 2) * 1024 + lane * 16;
  const char* wp = ws + WS_WOP + (size_t)(w * 8) * 16384 + (size_t)(cb * 2) * 1024 + lane * 16;
  bf16x8 fo[8][2], fw[8][2];
#pragma unroll
  for (int i = 0; i < 8; ++i) {
    fo[i][0] = *(const bf16x8*)(op + (size_t)i * 65536);
    fw[i][0] = *(const bf16x8*)(wp + (size_t)i * 16384);
    fo[i][1] = *(const bf16x8*)(op + (size_t)i * 65536 + 1024);
    fw[i][1] = *(const bf16x8*)(wp + (size_t)i * 16384 + 1024);
  }
  const int tile = rb * 2 + (w & 1), nb = cb * 2 + (w >> 1);
  const int tok = tile * 32 + r;
  const float g = ((const float*)(ws + WS_GATE))[tok];
  f32x4 bq[4];
  {
    const float* bp = (const float*)(ws + WS_B) + ((size_t)(tile * 16 + nb) * 4 * 64 + lane) * 4;
#pragma unroll
    for (int q = 0; q < 4; ++q) bq[q] = *(const f32x4*)(bp + q * 256);
  }
  __builtin_amdgcn_sched_barrier(0);
  f32x16 a[4];
#pragma unroll
  for (int t = 0; t < 4; ++t)
#pragma unroll
    for (int i = 0; i < 16; ++i) a[t][i] = 0.f;
#pragma unroll
  for (int i = 0; i < 8; ++i) {
#pragma unroll
    for (int t = 0; t < 4; ++t) a[t] = MFMA(fw[i][t >> 1], fo[i][t & 1], a[t]);
  }
#pragma unroll
  for (int t = 0; t < 4; ++t) {
#pragma unroll
    for (int q = 0; q < 4; ++q) {
      f32x4 v = {a[t][4 * q], a[t][4 * q + 1], a[t][4 * q + 2], a[t][4 * q + 3]};
      *(f32x4*)&red[t][w][q][lane][0] = v;
    }
  }
  __syncthreads();
  const float g1 = rbf(1.f - g);
#pragma unroll
  for (int q = 0; q < 4; ++q) {
    f32x4 s = *(const f32x4*)&red[w][0][q][lane][0];
#pragma unroll
    for (int u = 1; u < 4; ++u) s += *(const f32x4*)&red[w][u][q][lane][0];
    u32x2 o;
    o[0] = pk2(g * s[0] + g1 * bq[q][0], g * s[1] + g1 * bq[q][1]);
    o[1] = pk2(g * s[2] + g1 * bq[q][2], g * s[3] + g1 * bq[q][3]);
    *(u32x2*)(out + (size_t)tok * D_ + nb * 32 + 8 * q + 4 * h) = o;
  }
}

extern "C" void kernel_launch(void* const* d_in, const int* in_sizes, int n_in, void* d_out, int out_size,
                              void* d_ws, size_t ws_size, hipStream_t stream) {
  const u16* x = (const u16*)d_in[0];
  const u16* wqkv = (const u16*)d_in[1];
  const u16* wo = (const u16*)d_in[2];
  const u16* w1 = (const u16*)d_in[4];
  const u16* b1 = (const u16*)d_in[5];
  const u16* w2 = (const u16*)d_in[6];
  const u16* gw = (const u16*)d_in[7];
  char* ws = (char*)d_ws;
  k1<<<256, 768, 0, stream>>>(x, wqkv, wo, w1, b1, w2, gw, ws);
  k2<<<256, 768, 0, stream>>>(ws);
  k3<<<256, 256, 0, stream>>>(ws, (u16*)d_out);
}
```

```cpp
#include <hip/hip_runtime.h>
#include <hip/hip_bf16.h>
#include <stdint.h>

typedef short bf16x8 __attribute__((ext_vector_type(8)));
typedef short s16x4 __attribute__((ext_vector_type(4)));
typedef float f32x16 __attribute__((ext_vector_type(16)));
typedef float f32x4 __attribute__((ext_vector_type(4)));
typedef float f32x2 __attribute__((ext_vector_type(2)));
typedef __bf16 bf16x2_t __attribute__((ext_vector_type(2)));
typedef unsigned u32x4 __attribute__((ext_vector_type(4)));
typedef unsigned u32x2 __attribute__((ext_vector_type(2)));
typedef unsigned short u16;
#define DI __device__ __forceinline__
#define MFMA(a, b, c) __builtin_amdgcn_mfma_f32_32x32x16_bf16((a), (b), (c), 0, 0, 0)
#define LAS __attribute__((address_space(3)))

#define KSTAG ((idx+(idx>>3)*2)&7)
#define PFDELAY "8"
#define PACE 5
#define LD_PRIO 0
#define FFN_PRIO 0
constexpr int S_ = 2048, D_ = 512, H_ = 32, F_ = 1376;
constexpr unsigned WS_Q = 0, WS_K = 2u << 20, WS_V = 4u << 20, WS_ACT = 6u << 20, WS_O = 12u << 20, WS_W2P = 14u << 20,
                 WS_WOP = 16u << 20, WS_GATE = 17u << 20, WS_KMAX = (17u << 20) + 16384, WS_B = 18u << 20;

DI unsigned pk2(float a, float b) {
  f32x2 v = {a, b};
  bf16x2_t r = __builtin_convertvector(v, bf16x2_t);
  return __builtin_bit_cast(unsigned, r);
}
DI float bf_lo(unsigned u) { return __uint_as_float(u << 16); }
DI float bf_hi(unsigned u) { return __uint_as_float(u & 0xffff0000u); }
DI float rbf(float x) { return bf_lo(pk2(x, 0.f)); }
DI void gldsa16(const void* sbase, unsigned voff, unsigned dst) {
  unsigned keep;
  asm volatile("s_mov_b32 %0, m0\n\ts_mov_b32 m0, %3\n\ts_nop 0\n\tglobal_load_lds_dwordx4 %1, %2\n\ts_mov_b32 m0, %0"
               : "=&s"(keep) : "v"(voff), "s"(sbase), "s"(dst) : "memory");
}
DI void gldsa4(const void* sbase, unsigned voff, unsigned dst) {
  unsigned keep;
  asm volatile("s_mov_b32 %0, m0\n\ts_mov_b32 m0, %3\n\ts_nop 0\n\tglobal_load_lds_dword %1, %2\n\ts_mov_b32 m0, %0"
               : "=&s"(keep) : "v"(voff), "s"(sbase), "s"(dst) : "memory");
}
DI s16x4 ldtr(const char* l) {
  typedef short v4i16_t __attribute__((ext_vector_type(4)));
  return __builtin_amdgcn_ds_read_tr16_b64_v4i16((LAS v4i16_t*)l);
}
#define WAIT_V(n) asm volatile("s_waitcnt vmcnt(" #n ") lgkmcnt(0)" ::: "memory")
#define BAR() __builtin_amdgcn_s_barrier()
template <int CTRL> DI float dppf(float v) {
  return __int_as_float(__builtin_amdgcn_update_dpp(0, __float_as_int(v), CTRL, 0xF, 0xF, true));
}
DI float rdl(float v, int l) { return __int_as_float(__builtin_amdgcn_readlane(__float_as_int(v), l)); }
DI float row16_max(float v) {
  v = fmaxf(v, dppf<0xB1>(v)); v = fmaxf(v, dppf<0x4E>(v)); v = fmaxf(v, dppf<0x141>(v)); v = fmaxf(v, dppf<0x140>(v));
  return v;
}
DI float row16_sum(float v) {
  v += dppf<0xB1>(v); v += dppf<0x4E>(v); v += dppf<0x141>(v); v += dppf<0x140>(v);
  return v;
}
DI float add_half(float v) {
  auto rr = __builtin_amdgcn_permlane32_swap(__float_as_uint(v), __float_as_uint(v), false, false);
  return __uint_as_float(rr[0]) + __uint_as_float(rr[1]);
}
DI float max_half(float v) {
  auto rr = __builtin_amdgcn_permlane32_swap(__float_as_uint(v), __float_as_uint(v), false, false);
  return fmaxf(__uint_as_float(rr[0]), __uint_as_float(rr[1]));
}

#define ST_AUX 16
#define WS_RSRC(ws) __builtin_amdgcn_make_buffer_rsrc((void*)(ws), 0, 32u << 20, 0x00020000)
#define WST16(rs, off, v) __builtin_amdgcn_raw_buffer_store_b128(__builtin_bit_cast(u32x4, v), rs, (unsigned)(off), 0, ST_AUX)

DI float mish_f(float h) {
  float t = fminf(h * 1.4426950408889634f, 60.f);
  float w = __builtin_amdgcn_exp2f(t);
  float a = w + 2.f;
  float u = w * a;
  float d = __builtin_fmaf(w, a, 2.f);
  return h * u * __builtin_amdgcn_rcpf(d);
}

#define K1_LOOP_ASM0 \
  "s_sleep " PFDELAY "\n\t" \
  "s_barrier\n\t" \
  "ds_read_b128 v[48:51], v80\n\t" \
  "ds_read_b64_tr_b16 v[52:53], v84 offset:0\n\t" \
  "ds_read_b64_tr_b16 v[54:55], v84 offset:256\n\t" \
  "ds_read_b64_tr_b16 v[56:57], v84 offset:4096\n\t" \
  "ds_read_b64_tr_b16 v[58:59], v84 offset:4352\n\t" \
  "ds_read_b64_tr_b16 v[60:61], v84 offset:8192\n\t" \
  "ds_read_b64_tr_b16 v[62:63], v84 offset:8448\n\t" \
  "1:\n\t" \
  "ds_read_b128 v[64:67], v81\n\t" \
  "ds_read_b64_tr_b16 v[68:69], v84 offset:1024\n\t" \
  "ds_read_b64_tr_b16 v[70:71], v84 offset:1280\n\t" \
  "ds_read_b64_tr_b16 v[72:73], v84 offset:5120\n\t" \
  "ds_read_b64_tr_b16 v[74:75], v84 offset:5376\n\t" \
  "ds_read_b64_tr_b16 v[76:77], v84 offset:9216\n\t" \
  "ds_read_b64_tr_b16 v[78:79], v84 offset:9472\n\t" \
  "s_waitcnt lgkmcnt(7)\n\t" \
  "v_mfma_f32_32x32x16_bf16 v[0:15], v[52:55], v[48:51], v[0:15]\n\t" \
  "v_mfma_f32_32x32x16_bf16 v[16:31], v[56:59], v[48:51], v[16:31]\n\t" \
  "v_mfma_f32_32x32x16_bf16 v[32:47], v[48:51], v[60:63], v[32:47]\n\t" \
  "ds_read_b128 v[48:51], v82\n\t" \
  "ds_read_b64_tr_b16 v[52:53], v84 offset:2048\n\t" \
  "ds_read_b64_tr_b16 v[54:55], v84 offset:2304\n\t" \
  "ds_read_b64_tr_b16 v[56:57], v84 offset:6144\n\t" \
  "ds_read_b64_tr_b16 v[58:59], v84 offset:6400\n\t" \
  "ds_read_b64_tr_b16 v[60:61], v84 offset:10240\n\t" \
  "ds_read_b64_tr_b16 v[62:63], v84 offset:10496\n\t" \
  "s_waitcnt lgkmcnt(7)\n\t" \
  "v_mfma_f32_32x32x16_bf16 v[0:15], v[68:71], v[64:67], v[0:15]\n\t" \
  "v_mfma_f32_32x32x16_bf16 v[16:31], v[72:75], v[64:67], v[16:31]\n\t" \
  "v_mfma_f32_32x32x16_bf16 v[32:47], v[64:67], v[76:79], v[32:47]\n\t" \
  "ds_read_b128 v[64:67], v83\n\t" \
  "ds_read_b64_tr_b16 v[68:69], v84 offset:3072\n\t" \
  "ds_read_b64_tr_b16 v[70:71], v84 offset:3328\n\t" \
  "ds_read_b64_tr_b16 v[72:73], v84 offset:7168\n\t" \
  "ds_read_b64_tr_b16 v[74:75], v84 offset:7424\n\t" \
  "ds_read_b64_tr_b16 v[76:77], v84 offset:11264\n\t" \
  "ds_read_b64_tr_b16 v[78:79], v84 offset:11520\n\t" \
  "s_waitcnt lgkmcnt(7)\n\t" \
  "v_mfma_f32_32x32x16_bf16 v[0:15], v[52:55], v[48:51], v[0:15]\n\t" \
  "v_mfma_f32_32x32x16_bf16 v[16:31], v[56:59], v[48:51], v[16:31]\n\t" \
  "v_mfma_f32_32x32x16_bf16 v[32:47], v[48:51], v[60:63], v[32:47]\n\t" \
  "s_cmp_eq_u32 %[kt], 7\n\t" \
  "s_cbranch_scc1 9f\n\t" \
  "s_add_u32 %[rd], %[rd], 1\n\t" \
  "s_cmp_eq_u32 %[rd], 4\n\t" \
  "s_cselect_b32 %[t0], %[cm80], %[c40]\n\t" \
  "s_cselect_b32 %[rd], 0, %[rd]\n\t" \
  "v_add_u32 v80, %[t0], v80\n\t" \
  "v_add_u32 v81, %[t0], v81\n\t" \
  "v_add_u32 v82, %[t0], v82\n\t" \
  "v_add_u32 v83, %[t0], v83\n\t" \
  "v_add_u32 v84, %[t0], v84\n\t" \
  "s_add_u32 %[kt], %[kt], 1\n\t" \
  "s_waitcnt lgkmcnt(0)\n\t" \
  "s_barrier\n\t" \
  "ds_read_b128 v[48:51], v80\n\t" \
  "ds_read_b64_tr_b16 v[52:53], v84 offset:0\n\t" \
  "ds_read_b64_tr_b16 v[54:55], v84 offset:256\n\t" \
  "ds_read_b64_tr_b16 v[56:57], v84 offset:4096\n\t" \
  "ds_read_b64_tr_b16 v[58:59], v84 offset:4352\n\t" \
  "ds_read_b64_tr_b16 v[60:61], v84 offset:8192\n\t" \
  "ds_read_b64_tr_b16 v[62:63], v84 offset:8448\n\t" \
  "v_mfma_f32_32x32x16_bf16 v[0:15], v[68:71], v[64:67], v[0:15]\n\t" \
  "v_mfma_f32_32x32x16_bf16 v[16:31], v[72:75], v[64:67], v[16:31]\n\t" \
  "v_mfma_f32_32x32x16_bf16 v[32:47], v[64:67], v[76:79], v[32:47]\n\t" \
  "s_branch 1b\n\t" \
  "9:\n\t" \
  "s_waitcnt lgkmcnt(0)\n\t" \
  "v_mfma_f32_32x32x16_bf16 v[0:15], v[68:71], v[64:67], v[0:15]\n\t" \
  "v_mfma_f32_32x32x16_bf16 v[16:31], v[72:75], v[64:67], v[16:31]\n\t" \
  "v_mfma_f32_32x32x16_bf16 v[32:47], v[64:67], v[76:79], v[32:47]\n\t" \
  "s_nop 15\n\t" \
  ""
#define K1_LOOP_ASM1 \
  "s_sleep " PFDELAY "\n\t" \
  "s_barrier\n\t" \
  "ds_read_b128 v[48:51], v80\n\t" \
  "ds_read_b64_tr_b16 v[52:53], v84 offset:0\n\t" \
  "ds_read_b64_tr_b16 v[54:55], v84 offset:256\n\t" \
  "ds_read_b64_tr_b16 v[56:57], v84 offset:4096\n\t" \
  "ds_read_b64_tr_b16 v[58:59], v84 offset:4352\n\t" \
  "ds_read_b64_tr_b16 v[60:61], v84 offset:8192\n\t" \
  "ds_read_b64_tr_b16 v[62:63], v84 offset:8448\n\t" \
  "1:\n\t" \
  "ds_read_b128 v[64:67], v81\n\t" \
  "ds_read_b64_tr_b16 v[68:69], v84 offset:1024\n\t" \
  "ds_read_b64_tr_b16 v[70:71], v84 offset:1280\n\t" \
  "ds_read_b64_tr_b16 v[72:73], v84 offset:5120\n\t" \
  "ds_read_b64_tr_b16 v[74:75], v84 offset:5376\n\t" \
  "ds_read_b64_tr_b16 v[76:77], v84 offset:9216\n\t" \
  "ds_read_b64_tr_b16 v[78:79], v84 offset:9472\n\t" \
  "s_waitcnt lgkmcnt(7)\n\t" \
  "v_mfma_f32_32x32x16_bf16 v[0:15], v[52:55], v[48:51], v[0:15]\n\t" \
  "v_mfma_f32_32x32x16_bf16 v[16:31], v[56:59], v[48:51], v[16:31]\n\t" \
  "v_mfma_f32_32x32x16_bf16 v[32:47], v[60:63], v[48:51], v[32:47]\n\t" \
  "ds_read_b128 v[48:51], v82\n\t" \
  "ds_read_b64_tr_b16 v[52:53], v84 offset:2048\n\t" \
  "ds_read_b64_tr_b16 v[54:55], v84 offset:2304\n\t" \
  "ds_read_b64_tr_b16 v[56:57], v84 offset:6144\n\t" \
  "ds_read_b64_tr_b16 v[58:59], v84 offset:6400\n\t" \
  "ds_read_b64_tr_b16 v[60:61], v84 offset:10240\n\t" \
  "ds_read_b64_tr_b16 v[62:63], v84 offset:10496\n\t" \
  "s_waitcnt lgkmcnt(7)\n\t" \
  "v_mfma_f32_32x32x16_bf16 v[0:15], v[68:71], v[64:67], v[0:15]\n\t" \
  "v_mfma_f32_32x32x16_bf16 v[16:31], v[72:75], v[64:67], v[16:31]\n\t" \
  "v_mfma_f32_32x32x16_bf16 v[32:47], v[76:79], v[64:67], v[32:47]\n\t" \
  "ds_read_b128 v[64:67], v83\n\t" \
  "ds_read_b64_tr_b16 v[68:69], v84 offset:3072\n\t" \
  "ds_read_b64_tr_b16 v[70:71], v84 offset:3328\n\t" \
  "ds_read_b64_tr_b16 v[72:73], v84 offset:7168\n\t" \
  "ds_read_b64_tr_b16 v[74:75], v84 offset:7424\n\t" \
  "ds_read_b64_tr_b16 v[76:77], v84 offset:11264\n\t" \
  "ds_read_b64_tr_b16 v[78:79], v84 offset:11520\n\t" \
  "s_waitcnt lgkmcnt(7)\n\t" \
  "v_mfma_f32_32x32x16_bf16 v[0:15], v[52:55], v[48:51], v[0:15]\n\t" \
  "v_mfma_f32_32x32x16_bf16 v[16:31], v[56:59], v[48:51], v[16:31]\n\t" \
  "v_mfma_f32_32x32x16_bf16 v[32:47], v[60:63], v[48:51], v[32:47]\n\t" \
  "s_cmp_eq_u32 %[kt], 7\n\t" \
  "s_cbranch_scc1 9f\n\t" \
  "s_add_u32 %[rd], %[rd], 1\n\t" \
  "s_cmp_eq_u32 %[rd], 4\n\t" \
  "s_cselect_b32 %[t0], %[cm80], %[c40]\n\t" \
  "s_cselect_b32 %[rd], 0, %[rd]\n\t" \
  "v_add_u32 v80, %[t0], v80\n\t" \
  "v_add_u32 v81, %[t0], v81\n\t" \
  "v_add_u32 v82, %[t0], v82\n\t" \
  "v_add_u32 v83, %[t0], v83\n\t" \
  "v_add_u32 v84, %[t0], v84\n\t" \
  "s_add_u32 %[kt], %[kt], 1\n\t" \
  "s_waitcnt lgkmcnt(0)\n\t" \
  "s_barrier\n\t" \
  "ds_read_b128 v[48:51], v80\n\t" \
  "ds_read_b64_tr_b16 v[52:53], v84 offset:0\n\t" \
  "ds_read_b64_tr_b16 v[54:55], v84 offset:256\n\t" \
  "ds_read_b64_tr_b16 v[56:57], v84 offset:4096\n\t" \
  "ds_read_b64_tr_b16 v[58:59], v84 offset:4352\n\t" \
  "ds_read_b64_tr_b16 v[60:61], v84 offset:8192\n\t" \
  "ds_read_b64_tr_b16 v[62:63], v84 offset:8448\n\t" \
  "v_mfma_f32_32x32x16_bf16 v[0:15], v[68:71], v[64:67], v[0:15]\n\t" \
  "v_mfma_f32_32x32x16_bf16 v[16:31], v[72:75], v[64:67], v[16:31]\n\t" \
  "v_mfma_f32_32x32x16_bf16 v[32:47], v[76:79], v[64:67], v[32:47]\n\t" \
  "s_branch 1b\n\t" \
  "9:\n\t" \
  "s_waitcnt lgkmcnt(0)\n\t" \
  "v_mfma_f32_32x32x16_bf16 v[0:15], v[68:71], v[64:67], v[0:15]\n\t" \
  "v_mfma_f32_32x32x16_bf16 v[16:31], v[72:75], v[64:67], v[16:31]\n\t" \
  "v_mfma_f32_32x32x16_bf16 v[32:47], v[76:79], v[64:67], v[32:47]\n\t" \
  "s_nop 15\n\t" \
  ""

__global__ __launch_bounds__(768) void k1(const u16* __restrict__ x, const u16* __restrict__ wqkv,
                                          const u16* __restrict__ wo, const u16* __restrict__ w1,
                                          const u16* __restrict__ b1, const u16* __restrict__ w2,
                                          const u16* __restrict__ gw, char* __restrict__ ws) {
  __shared__ __attribute__((aligned(16))) char lds[4 * 40960];
  const int bid = blockIdx.x, xcd = bid & 7, idx = bid >> 3;
  const int rb = (xcd & 1) * 8 + (idx & 7), j = (xcd >> 1) * 4 + (idx >> 3);
  const int tid = threadIdx.x, lane = tid & 63;
  const int w = __builtin_amdgcn_readfirstlane(tid >> 6);
  const int r = lane & 31, h = lane >> 5, m = w & 3, nh = w >> 2;
  const unsigned lb = (unsigned)(size_t)(LAS char*)lds;
  const int tile = rb * 4 + m;
  const int q4 = (lane & 15) >> 2, p4 = lane & 3, grp = (lane >> 4) & 1;
  const auto rs = WS_RSRC(ws);
  if (w >= 8) {
    const unsigned xo0 = (unsigned)((lane >> 3) * D_ + (((lane & 7) ^ ((lane >> 4) & 7)) * 8)) * 2u;
    const unsigned xo1 = (unsigned)((8 + (lane >> 3)) * D_ + (((lane & 7) ^ ((4 + (lane >> 4)) & 7)) * 8)) * 2u;
    const unsigned wqo = (unsigned)((lane >> 2) * 1536 + (lane & 3) * 8) * 2u;
    const unsigned w1o = (unsigned)((lane >> 2) * F_ + (lane & 3) * 8) * 2u;
    const int fb0 = min(3 * j, 42), fb1 = min(3 * j + 1, 42), fb2 = min(3 * j + 2, 42);
    int kq = KSTAG;
    const char* xb = (const char*)(x + (size_t)(rb * 128 + 32 * m) * D_ + 64 * kq);
    const char* wqb = (const char*)(wqkv + (size_t)(16 * m + 64 * kq) * 1536 + 64 * j);
    const char* w1b = (const char*)(w1 + (size_t)(16 * m + 64 * kq) * F_);
    const unsigned vo_ = wqo + 2048 - 64 * j;
    const unsigned f0o = w1o + 64 * fb0, f1o = w1o + 64 * fb1, f2o = w1o + 64 * fb2;
    unsigned xl = lb + m * 4096, wl = lb + 16384 + m * 1024;
#define STAGE()                                                                                   \
  do {                                                                                            \
    gldsa16(wqb, wqo, wl);                                                                        \
    gldsa16(wqb, wqo + 64, wl + 4096);                                                            \
    gldsa16(w1b, f0o, wl + 12288);                                                                \
    gldsa16(w1b, f1o, wl + 16384);                                                                \
    gldsa16(w1b, f2o, wl + 20480);                                                                \
    gldsa16(wqb, vo_, wl + 8192);                                                                 \
    gldsa16(xb, xo0, xl);                                                                         \
    gldsa16(xb, xo1, xl + 1024);                                                                  \
    gldsa16(xb + 16 * D_ * 2, xo0, xl + 2048);                                                    \
    gldsa16(xb + 16 * D_ * 2, xo1, xl + 3072);                                                    \
    kq = (kq + 1) & 7;                                                                            \
    { const int dk_ = kq ? 128 : -896; xb += dk_; wqb += dk_ * 1536; w1b += dk_ * F_; }           \
    const bool wrap_ = xl + 40960 >= lb + 4 * 40960;                                              \
    xl += wrap_ ? 0u - 122880u : 40960u; wl += wrap_ ? 0u - 122880u : 40960u;                     \
  } while (0)
    STAGE();
    STAGE();
    WAIT_V(10); BAR();
    STAGE();
#pragma unroll 1
    for (int kt = 0; kt < 5; ++kt) {
      STAGE();
      WAIT_V(20); BAR();
    }
#undef STAGE
    const int fid = bid * 8 + 2 * m;
    const int tok = rb * 128 + j * 8 + 2 * m;
    const unsigned sl = lb + m * 5120;
    {
      const int fc = min(fid, 1886);
      const u16* psrc = fc < 1376 ? w2 + (size_t)(fc >> 4) * 16 * D_ + (fc & 15) * 32
                                  : wo + (size_t)((fc - 1376) >> 4) * 16 * D_ + (fc & 15) * 32;
      const unsigned po = (unsigned)((lane >> 2) * D_ + (lane & 3) * 8) * 2u;
      gldsa16(psrc, po, sl);
      gldsa16(psrc, po + 64, sl + 1024);
      gldsa16(x + (size_t)tok * D_, lane * 16, sl + 2048);
      gldsa16(x + (size_t)tok * D_, lane * 16 + 1024, sl + 3072);
      gldsa16(gw, lane * 16, sl + 4096);
    }
    WAIT_V(15); BAR();
    WAIT_V(5); BAR();
    WAIT_V(0);
    const char* sp = lds + m * 5120;
#pragma unroll
    for (int c = 0; c < 2; ++c) {
      const char* pp = sp + c * 1024 + (4 * h + q4) * 64 + grp * 32 + p4 * 8;
      const s16x4 lo = ldtr(pp), hi = ldtr(pp + 512);
      const bf16x8 f = __builtin_shufflevector(lo, hi, 0, 1, 2, 3, 4, 5, 6, 7);
      const int fi = fid + c;
      if (fi < 1888) WST16(rs, (fi < 1376 ? WS_W2P + fi * 1024 : WS_WOP + (fi - 1376) * 1024) + lane * 16, f);
    }
    const u32x4 gg = *(const u32x4*)(sp + 4096 + lane * 16);
#pragma unroll
    for (int t = 0; t < 2; ++t) {
      const u32x4 gx = *(const u32x4*)(sp + 2048 + t * 1024 + lane * 16);
      float s = 0.f;
#pragma unroll
      for (int e = 0; e < 4; ++e) {
        s += rbf(bf_lo(gx[e]) * bf_lo(gg[e]));
        s += rbf(bf_hi(gx[e]) * bf_hi(gg[e]));
      }
      s = row16_sum(s);
      s = (rdl(s, 0) + rdl(s, 16)) + (rdl(s, 32) + rdl(s, 48));
      s = rbf(s);
      const float g = rbf(__builtin_amdgcn_rcpf(1.f + __builtin_amdgcn_exp2f(-s * 1.4426950408889634f)));
      if (lane == 0) ((float*)(ws + WS_GATE))[tok + t] = g;
    }
    return;
  }

  f32x16 acc[3];
#pragma unroll
  for (int b = 0; b < 3; ++b)
#pragma unroll
    for (int i = 0; i < 16; ++i) acc[b][i] = 0.f;
  u32x2 bia[3][4];
#pragma unroll
  for (int b = 0; b < 3; ++b)
#pragma unroll
    for (int g = 0; g < 4; ++g) bia[b][g] = *(const u32x2*)(b1 + 32 * min(3 * j + b, 42) + 8 * g + 4 * h);
  {
    const int sw = (r >> 1) & 7;
    const unsigned xr0 = lb + m * 4096 + r * 128;
    unsigned xa0 = xr0 + ((0 + h) ^ sw) * 16, xa1 = xr0 + ((2 + h) ^ sw) * 16, xa2 = xr0 + ((4 + h) ^ sw) * 16,
             xa3 = xr0 + ((6 + h) ^ sw) * 16;
    unsigned wa = lb + 16384 + nh * 12288 + (8 * h + q4) * 64 + grp * 32 + p4 * 8;
    unsigned kt = 0, rd = 0, t0;
    const int pc = idx * 8 + w;
    const char* pb;
    unsigned pstep, pv;
    if (pc < 128) {
      pb = (const char*)(x + (size_t)((xcd & 1) * 1024 + pc * 8) * D_) + 256;
      pstep = 128; pv = (lane >> 3) * 1024 + (lane & 7) * 16;
    } else {
      const int q = min(pc - 128, 95), jj = (xcd >> 1) * 4 + q / 24, sl_ = (q % 24) >> 2, kr = (q & 3) * 16 + 128;
      const int ld2 = sl_ < 3 ? 3072 : 2 * F_;
      const int cb_ = sl_ < 2 ? 128 * jj + 64 * sl_ : (sl_ == 2 ? 2048 + 64 * jj : 64 * min(3 * jj + sl_ - 3, 42));
      pb = (const char*)(sl_ < 3 ? wqkv : w1) + (size_t)kr * ld2 + cb_;
      pstep = 64 * ld2; pv = (lane >> 2) * ld2 + (lane & 3) * 16;
    }
    const unsigned long long pbu =
        ((unsigned long long)(unsigned)__builtin_amdgcn_readfirstlane((int)((size_t)pb >> 32)) << 32) |
        (unsigned)__builtin_amdgcn_readfirstlane((int)(size_t)pb);
#define K1_RUN(ASM)                                                                                            \
    asm volatile(ASM                                                                                           \
                 : "+{v[0:15]}"(acc[0]), "+{v[16:31]}"(acc[1]), "+{v[32:47]}"(acc[2]), "+{v80}"(xa0),          \
                   "+{v81}"(xa1), "+{v82}"(xa2), "+{v83}"(xa3), "+{v84}"(wa), [kt] "+s"(kt), [rd] "+s"(rd),    \
                   [t0] "=&s"(t0), "+{v90}"(pv)                                                                \
                 : "{s[64:65]}"(pbu), "{v96}"(pstep), [c40] "s"(40960u), [cm80] "s"(0u - 122880u)                                                 \
                 : "memory", "scc", "v48", "v49", "v50", "v51", "v52", "v53", "v54", "v55", "v56", "v57",      \
                   "v58", "v59", "v60", "v61", "v62", "v63", "v64", "v65", "v66", "v67", "v68", "v69", "v70",  \
                   "v71", "v72", "v73", "v74", "v75", "v76", "v77", "v78", "v79", "v92", "v93", "v94", "v95")
    if (nh) K1_RUN(K1_LOOP_ASM1); else K1_RUN(K1_LOOP_ASM0);
  }
  if (nh == 0) {
#pragma unroll
    for (int b = 0; b < 2; ++b) {
      const int c0 = 64 * j + 32 * b;
      const bool isq = c0 < 512;
      const float sc = isq ? 0.36067376022224085f : 1.0f;
      const int head0 = (c0 & 511) >> 4;
      const unsigned dst = isq ? WS_Q : WS_K;
#pragma unroll
      for (int hh = 0; hh < 2; ++hh) {
        u32x4 o;
#pragma unroll
        for (int e = 0; e < 4; ++e) o[e] = pk2(acc[b][8 * hh + 2 * e] * sc, acc[b][8 * hh + 2 * e + 1] * sc);
        WST16(rs, dst + (((head0 + hh) * 64 + tile) * 64 + lane) * 16, o);
        if (!isq) {
          float ss = 0.f;
#pragma unroll
          for (int e = 0; e < 4; ++e) {
            const float a = bf_lo(o[e]), c = bf_hi(o[e]);
            ss = __builtin_fmaf(a, a, ss);
            ss = __builtin_fmaf(c, c, ss);
          }
          ss = row16_max(add_half(ss));
          ss = fmaxf(rdl(ss, 0), rdl(ss, 16));
          if (lane == 0) ((float*)(ws + WS_KMAX))[(head0 + hh) * 64 + tile] = ss;
        }
      }
    }
    {
      const int head = 2 * j + (r >> 4), d = r & 15;
#pragma unroll
      for (int s = 0; s < 2; ++s) {
        u32x4 o;
#pragma unroll
        for (int e = 0; e < 4; ++e) o[e] = pk2(acc[2][8 * s + 2 * e], acc[2][8 * s + 2 * e + 1]);
        WST16(rs, WS_V + (head * 64 + tile) * 1024 + s * 512 + h * 256 + d * 16, o);
      }
    }
  } else {
#pragma unroll
    for (int b = 0; b < 3; ++b) {
      if (3 * j + b < 43) {
#pragma unroll
        for (int hh = 0; hh < 2; ++hh) {
          u32x4 o;
#pragma unroll
          for (int gq = 0; gq < 2; ++gq) {
            const int g = 2 * hh + gq;
            const u32x2 bb = bia[b][g];
            const float h0 = acc[b][4 * g + 0] + bf_lo(bb[0]);
            const float h1 = acc[b][4 * g + 1] + bf_hi(bb[0]);
            const float h2 = acc[b][4 * g + 2] + bf_lo(bb[1]);
            const float h3 = acc[b][4 * g + 3] + bf_hi(bb[1]);
            o[2 * gq] = pk2(mish_f(h0), mish_f(h1));
            o[2 * gq + 1] = pk2(mish_f(h2), mish_f(h3));
          }
          WST16(rs, WS_ACT + (((2 * (3 * j + b) + hh) * 64 + tile) * 64 + lane) * 16, o);
        }
      }
    }
  }
}

#define ATTN_LOOP_ASM \
  "1:\n\t" \
  "v_exp_f32 v32, v32\n\t" \
  "v_exp_f32 v33, v33\n\t" \
  "v_exp_f32 v34, v34\n\t" \
  "v_cvt_pk_bf16_f32 v88, v32, v33\n\t" \
  "v_mfma_f32_32x32x16_bf16 v[0:15], v[84:87], v[92:95], v[0:15]\n\t" \
  "s_mov_b64 exec, %[mask]\n\t" \
  "ds_read_b128 v[80:83], v97 offset:1024\n\t" \
  "ds_read_b128 v[84:87], v97 offset:1536\n\t" \
  "s_mov_b64 exec, -1\n\t" \
  "v_exp_f32 v35, v35\n\t" \
  "v_exp_f32 v36, v36\n\t" \
  "v_cvt_pk_bf16_f32 v89, v34, v35\n\t" \
  "v_exp_f32 v37, v37\n\t" \
  "v_exp_f32 v38, v38\n\t" \
  "v_cvt_pk_bf16_f32 v90, v36, v37\n\t" \
  "v_exp_f32 v39, v39\n\t" \
  "v_exp_f32 v40, v40\n\t" \
  "v_cvt_pk_bf16_f32 v91, v38, v39\n\t" \
  "v_exp_f32 v41, v41\n\t" \
  "v_exp_f32 v42, v42\n\t" \
  "v_mfma_f32_32x32x16_bf16 v[0:15], v[72:75], v[88:91], v[0:15]\n\t" \
  "v_cvt_pk_bf16_f32 v92, v40, v41\n\t" \
  "v_exp_f32 v43, v43\n\t" \
  "v_exp_f32 v44, v44\n\t" \
  "v_cvt_pk_bf16_f32 v93, v42, v43\n\t" \
  "v_exp_f32 v45, v45\n\t" \
  "v_exp_f32 v46, v46\n\t" \
  "v_cvt_pk_bf16_f32 v94, v44, v45\n\t" \
  "v_exp_f32 v47, v47\n\t" \
  "s_waitcnt lgkmcnt(0)\n\t" \
  "v_cvt_pk_bf16_f32 v95, v46, v47\n\t" \
  "v_mfma_f32_32x32x16_bf16 v[32:47], v[68:71], v[64:67], v[16:31]\n\t" \
  "ds_read_b128 v[68:71], v96 offset:3072\n\t" \
  "v_exp_f32 v48, v48\n\t" \
  "v_exp_f32 v49, v49\n\t" \
  "v_exp_f32 v50, v50\n\t" \
  "v_cvt_pk_bf16_f32 v88, v48, v49\n\t" \
  "v_mfma_f32_32x32x16_bf16 v[0:15], v[76:79], v[92:95], v[0:15]\n\t" \
  "s_mov_b64 exec, %[mask]\n\t" \
  "ds_read_b128 v[72:75], v97 offset:2048\n\t" \
  "ds_read_b128 v[76:79], v97 offset:2560\n\t" \
  "s_mov_b64 exec, -1\n\t" \
  "v_exp_f32 v51, v51\n\t" \
  "v_exp_f32 v52, v52\n\t" \
  "v_cvt_pk_bf16_f32 v89, v50, v51\n\t" \
  "v_exp_f32 v53, v53\n\t" \
  "v_exp_f32 v54, v54\n\t" \
  "v_cvt_pk_bf16_f32 v90, v52, v53\n\t" \
  "v_exp_f32 v55, v55\n\t" \
  "v_exp_f32 v56, v56\n\t" \
  "v_cvt_pk_bf16_f32 v91, v54, v55\n\t" \
  "v_exp_f32 v57, v57\n\t" \
  "v_exp_f32 v58, v58\n\t" \
  "v_mfma_f32_32x32x16_bf16 v[0:15], v[80:83], v[88:91], v[0:15]\n\t" \
  "v_cvt_pk_bf16_f32 v92, v56, v57\n\t" \
  "v_exp_f32 v59, v59\n\t" \
  "v_exp_f32 v60, v60\n\t" \
  "v_cvt_pk_bf16_f32 v93, v58, v59\n\t" \
  "v_exp_f32 v61, v61\n\t" \
  "v_exp_f32 v62, v62\n\t" \
  "v_cvt_pk_bf16_f32 v94, v60, v61\n\t" \
  "v_exp_f32 v63, v63\n\t" \
  "s_waitcnt lgkmcnt(0)\n\t" \
  "v_cvt_pk_bf16_f32 v95, v62, v63\n\t" \
  "v_mfma_f32_32x32x16_bf16 v[48:63], v[68:71], v[64:67], v[16:31]\n\t" \
  "ds_read_b128 v[68:71], v96 offset:4096\n\t" \
  "v_exp_f32 v32, v32\n\t" \
  "v_exp_f32 v33, v33\n\t" \
  "v_exp_f32 v34, v34\n\t" \
  "v_cvt_pk_bf16_f32 v88, v32, v33\n\t" \
  "v_mfma_f32_32x32x16_bf16 v[0:15], v[84:87], v[92:95], v[0:15]\n\t" \
  "s_mov_b64 exec, %[mask]\n\t" \
  "ds_read_b128 v[80:83], v97 offset:3072\n\t" \
  "ds_read_b128 v[84:87], v97 offset:3584\n\t" \
  "s_mov_b64 exec, -1\n\t" \
  "v_exp_f32 v35, v35\n\t" \
  "v_exp_f32 v36, v36\n\t" \
  "v_cvt_pk_bf16_f32 v89, v34, v35\n\t" \
  "v_exp_f32 v37, v37\n\t" \
  "v_exp_f32 v38, v38\n\t" \
  "v_cvt_pk_bf16_f32 v90, v36, v37\n\t" \
  "v_exp_f32 v39, v39\n\t" \
  "v_exp_f32 v40, v40\n\t" \
  "v_cvt_pk_bf16_f32 v91, v38, v39\n\t" \
  "v_exp_f32 v41, v41\n\t" \
  "v_exp_f32 v42, v42\n\t" \
  "v_mfma_f32_32x32x16_bf16 v[0:15], v[72:75], v[88:91], v[0:15]\n\t" \
  "v_cvt_pk_bf16_f32 v92, v40, v41\n\t" \
  "v_exp_f32 v43, v43\n\t" \
  "v_exp_f32 v44, v44\n\t" \
  "v_cvt_pk_bf16_f32 v93, v42, v43\n\t" \
  "v_exp_f32 v45, v45\n\t" \
  "v_exp_f32 v46, v46\n\t" \
  "v_cvt_pk_bf16_f32 v94, v44, v45\n\t" \
  "v_exp_f32 v47, v47\n\t" \
  "s_waitcnt lgkmcnt(0)\n\t" \
  "v_cvt_pk_bf16_f32 v95, v46, v47\n\t" \
  "v_mfma_f32_32x32x16_bf16 v[32:47], v[68:71], v[64:67], v[16:31]\n\t" \
  "ds_read_b128 v[68:71], v96 offset:5120\n\t" \
  "v_exp_f32 v48, v48\n\t" \
  "v_exp_f32 v49, v49\n\t" \
  "v_exp_f32 v50, v50\n\t" \
  "v_cvt_pk_bf16_f32 v88, v48, v49\n\t" \
  "v_mfma_f32_32x32x16_bf16 v[0:15], v[76:79], v[92:95], v[0:15]\n\t" \
  "s_mov_b64 exec, %[mask]\n\t" \
  "ds_read_b128 v[72:75], v97 offset:4096\n\t" \
  "ds_read_b128 v[76:79], v97 offset:4608\n\t" \
  "s_mov_b64 exec, -1\n\t" \
  "v_exp_f32 v51, v51\n\t" \
  "v_exp_f32 v52, v52\n\t" \
  "v_cvt_pk_bf16_f32 v89, v50, v51\n\t" \
  "v_exp_f32 v53, v53\n\t" \
  "v_exp_f32 v54, v54\n\t" \
  "v_cvt_pk_bf16_f32 v90, v52, v53\n\t" \
  "v_exp_f32 v55, v55\n\t" \
  "v_exp_f32 v56, v56\n\t" \
  "v_cvt_pk_bf16_f32 v91, v54, v55\n\t" \
  "v_exp_f32 v57, v57\n\t" \
  "v_exp_f32 v58, v58\n\t" \
  "v_mfma_f32_32x32x16_bf16 v[0:15], v[80:83], v[88:91], v[0:15]\n\t" \
  "v_cvt_pk_bf16_f32 v92, v56, v57\n\t" \
  "v_exp_f32 v59, v59\n\t" \
  "v_exp_f32 v60, v60\n\t" \
  "v_cvt_pk_bf16_f32 v93, v58, v59\n\t" \
  "v_exp_f32 v61, v61\n\t" \
  "v_exp_f32 v62, v62\n\t" \
  "v_cvt_pk_bf16_f32 v94, v60, v61\n\t" \
  "v_exp_f32 v63, v63\n\t" \
  "s_waitcnt lgkmcnt(0)\n\t" \
  "v_cvt_pk_bf16_f32 v95, v62, v63\n\t" \
  "v_mfma_f32_32x32x16_bf16 v[48:63], v[68:71], v[64:67], v[16:31]\n\t" \
  "ds_read_b128 v[68:71], v96 offset:6144\n\t" \
  "v_add_u32 v96, 0x1000, v96\n\t" \
  "v_add_u32 v97, 0x1000, v97\n\t" \
  "s_sub_u32 %[cnt], %[cnt], 1\n\t" \
  "s_and_b32 %[tmp], %[cnt], 13\n\t" \
  "s_cmp_lg_u32 %[tmp], 12\n\t" \
  "s_cbranch_scc1 2f\n\t" \
  "3:\n\t" \
  "ds_read_b32 v98, v99\n\t" \
  "s_waitcnt lgkmcnt(0)\n\t" \
  "v_readfirstlane_b32 %[tmp], v98\n\t" \
  "s_cmp_ge_u32 %[tmp], 4\n\t" \
  "s_cbranch_scc1 4f\n\t" \
  "s_sleep 1\n\t" \
  "s_branch 3b\n\t" \
  "4:\n\t" \
  "v_add_u32 v99, 4, v99\n\t" \
  "2:\n\t" \
  "s_cmp_lg_u32 %[cnt], 0\n\t" \
  "s_cbranch_scc1 1b\n\t" \
  "s_waitcnt lgkmcnt(0)\n\t" \
  "s_nop 15\n\t" \
  ""

__global__ __launch_bounds__(768) void k2(char* __restrict__ ws) {
  __shared__ __attribute__((aligned(16))) char lds[131072 + 4096];
  const int bid = blockIdx.x, xcd = bid & 7, idx = bid >> 3;
  const int head = xcd * 4 + (idx & 3), qb = idx >> 2;
  const int tid = threadIdx.x, lane = tid & 63;
  const int w = __builtin_amdgcn_readfirstlane(tid >> 6);
  const int h = lane >> 5;
  const unsigned lb = (unsigned)(size_t)(LAS char*)lds;
  const unsigned l16 = lane * 16;

  if (w >= 8) {
    __builtin_amdgcn_s_setprio(LD_PRIO);
    const int L = w - 8;
    const char* ksrc = ws + WS_K + (size_t)(head * 64 + L) * 1024;
    const char* vsrc = ws + WS_V + (size_t)(head * 64 + L) * 1024;
    const unsigned kdst = lb + L * 1024, vdst = lb + 65536 + L * 1024;
#pragma unroll
    for (int c = 0; c < 3; ++c) { gldsa16(ksrc + c * 4096, l16, kdst + c * 4096); gldsa16(vsrc + c * 4096, l16, vdst + c * 4096); }
    if (tid == 512) *(volatile LAS unsigned long long*)(lds + 131072) = 0;
    WAIT_V(0); BAR();
#pragma unroll 1
    for (int c = 3; c < 16; ++c) {
      gldsa16(ksrc + c * 4096, l16, kdst + c * 4096); gldsa16(vsrc + c * 4096, l16, vdst + c * 4096);
    }
    WAIT_V(14);
    if (lane == 0) __hip_atomic_fetch_add((LAS unsigned*)(lds + 131072), 1u, __ATOMIC_RELAXED, __HIP_MEMORY_SCOPE_WORKGROUP);
    WAIT_V(0);
    if (lane == 0) __hip_atomic_fetch_add((LAS unsigned*)(lds + 131076), 1u, __ATOMIC_RELAXED, __HIP_MEMORY_SCOPE_WORKGROUP);
    __builtin_amdgcn_s_setprio(FFN_PRIO);
    const int rb = (xcd & 3) * 8 + (idx & 7), cb = (xcd >> 2) * 4 + (idx >> 3);
    const int tile = rb * 2 + (L & 1), nb = cb * 2 + (L >> 1);
    const auto ra = __builtin_amdgcn_make_buffer_rsrc((void*)(ws + WS_ACT + (size_t)tile * 1024), 0, 85 * 65536 + 1024, 0x00020000);
    const auto rw = __builtin_amdgcn_make_buffer_rsrc((void*)(ws + WS_W2P + (size_t)nb * 1024), 0, 85 * 16384 + 1024, 0x00020000);
    f32x16 b;
#pragma unroll
    for (int i = 0; i < 16; ++i) b[i] = 0.f;
    constexpr int PD = 8;
    u32x4 fa[PD], fw[PD];
#pragma unroll
    for (int i = 0; i < PD; ++i) {
      fa[i] = __builtin_amdgcn_raw_buffer_load_b128(ra, l16, i * 65536, 0);
      fw[i] = __builtin_amdgcn_raw_buffer_load_b128(rw, l16, i * 16384, 0);
    }
    __builtin_amdgcn_sched_barrier(0);
#pragma unroll 1
    for (int k0 = PD; k0 < 88 + PD; k0 += PD) {
#pragma unroll
      for (int i = 0; i < PD; ++i) {
        b = MFMA(__builtin_bit_cast(bf16x8, fw[i]), __builtin_bit_cast(bf16x8, fa[i]), b);
        fa[i] = __builtin_amdgcn_raw_buffer_load_b128(ra, l16, (k0 + i) * 65536, 0);
        fw[i] = __builtin_amdgcn_raw_buffer_load_b128(rw, l16, (k0 + i) * 16384, 0);
        __builtin_amdgcn_sched_barrier(0);
      }
    }
    const auto rs = WS_RSRC(ws);
#pragma unroll
    for (int q = 0; q < 4; ++q) {
      f32x4 o = {b[4 * q], b[4 * q + 1], b[4 * q + 2], b[4 * q + 3]};
      WST16(rs, WS_B + (tile * 16 + nb) * 4096 + q * 1024 + l16, o);
    }
    return;
  }

  const int qt = qb * 8 + w;
  bf16x8 qf = *(const bf16x8*)(ws + WS_Q + (size_t)(head * 64 + qt) * 1024 + l16);
  float km = ((const float*)(ws + WS_KMAX))[head * 64 + lane];
  km = row16_max(km);
  km = fmaxf(fmaxf(rdl(km, 0), rdl(km, 16)), fmaxf(rdl(km, 32), rdl(km, 48)));
  float qs = 0.f;
  {
    const u32x4 qu = __builtin_bit_cast(u32x4, qf);
#pragma unroll
    for (int e = 0; e < 4; ++e) {
      const float a = bf_lo(qu[e]), c = bf_hi(qu[e]);
      qs = __builtin_fmaf(a, a, qs);
      qs = __builtin_fmaf(c, c, qs);
    }
    qs = add_half(qs);
  }
  const float mb = __builtin_amdgcn_sqrtf(qs * km) * 1.0001f;
  f32x16 negm, O;
#pragma unroll
  for (int i = 0; i < 16; ++i) { negm[i] = -mb; O[i] = 0.f; }

  const char* kp = lds + lane * 16;
  const char* vp = lds + 65536 + h * 256 + (lane & 15) * 16;
  const bool vact = (lane & 16) == 0;
  const short one = ((lane & 27) == 16) ? (short)0x3F80 : (short)0;
  bf16x8 va0, va1, vb0, vb1;
#pragma unroll
  for (int i = 0; i < 8; ++i) { va0[i] = one; va1[i] = one; vb0[i] = one; vb1[i] = one; }
  u32x4 p0 = {0, 0, 0, 0}, p1 = {0, 0, 0, 0};
  unsigned kaddr = lb + l16, vaddr = lb + 65536 + h * 256 + (lane & 15) * 16;
  const unsigned long long vmask = 0x0000FFFF0000FFFFull;

  BAR();
  asm volatile("" ::: "memory");
  f32x16 Sa = MFMA(*(const bf16x8*)(kp), qf, negm);
  f32x16 Sb = MFMA(*(const bf16x8*)(kp + 1024), qf, negm);
  bf16x8 kf = *(const bf16x8*)(kp + 2048);
  if (vact) { va0 = *(const bf16x8*)(vp); va1 = *(const bf16x8*)(vp + 512); }

#define ATTN_GROUPS(n)                                                                                       \
  do {                                                                                                       \
    unsigned cnt = (n), tmp, fa_ = lb + 131072;                                                                                 \
    asm volatile("s_nop 15\n\t" ATTN_LOOP_ASM                                                                \
                 : "+{v[0:15]}"(O), "+{v[32:47]}"(Sa), "+{v[48:63]}"(Sb), "+{v[68:71]}"(kf),                 \
                   "+{v[72:75]}"(va0), "+{v[76:79]}"(va1), "+{v[80:83]}"(vb0), "+{v[84:87]}"(vb1),           \
                   "+{v[88:91]}"(p0), "+{v[92:95]}"(p1), "+{v96}"(kaddr), "+{v97}"(vaddr), [cnt] "+s"(cnt), [tmp] "=&s"(tmp), "+{v99}"(fa_) \
                 : "{v[16:31]}"(negm), "{v[64:67]}"(qf), [mask] "s"(vmask)                                   \
                 : "memory", "scc", "v98");                                                                         \
  } while (0)

  ATTN_GROUPS(16);
  O = MFMA(vb1, __builtin_bit_cast(bf16x8, p1), O);

#define SOFTMAX_PV(Sx, v0, v1)                                                                     \
  do {                                                                                             \
    _Pragma("unroll") for (int e = 0; e < 4; ++e) {                                                \
      p0[e] = pk2(__builtin_amdgcn_exp2f(Sx[2 * e]), __builtin_amdgcn_exp2f(Sx[2 * e + 1]));       \
      p1[e] = pk2(__builtin_amdgcn_exp2f(Sx[8 + 2 * e]), __builtin_amdgcn_exp2f(Sx[8 + 2 * e + 1])); \
    }                                                                                              \
    O = MFMA(v0, __builtin_bit_cast(bf16x8, p0), O);                                               \
    O = MFMA(v1, __builtin_bit_cast(bf16x8, p1), O);                                               \
  } while (0)

  float l = O[8];
  if (__any(!(l >= 8.271806125530277e-25f))) {
    float m = -INFINITY;
    f32x16 zero;
#pragma unroll
    for (int i = 0; i < 16; ++i) { zero[i] = 0.f; O[i] = 0.f; }
    for (int t = 0; t < 64; ++t) {
      f32x16 Sx = MFMA(*(const bf16x8*)(kp + t * 1024), qf, zero);
      float mx = Sx[0];
#pragma unroll
      for (int i = 1; i < 16; ++i) mx = fmaxf(mx, Sx[i]);
      mx = max_half(mx);
      const float mn = fmaxf(m, mx);
      const float alpha = __builtin_amdgcn_exp2f(m - mn);
      m = mn;
#pragma unroll
      for (int i = 0; i < 16; ++i) { O[i] *= alpha; Sx[i] -= mn; }
      if (vact) { va0 = *(const bf16x8*)(vp + t * 1024); va1 = *(const bf16x8*)(vp + t * 1024 + 512); }
      SOFTMAX_PV(Sx, va0, va1);
    }
    l = O[8];
  }
  const float rl = 1.0f / l;
  u32x4 o;
#pragma unroll
  for (int e = 0; e < 4; ++e) o[e] = pk2(O[2 * e] * rl, O[2 * e + 1] * rl);
  WST16(WS_RSRC(ws), WS_O + (head * 64 + qt) * 1024 + l16, o);
}

__global__ __launch_bounds__(256) void k3(const char* __restrict__ ws, u16* __restrict__ out) {
  __shared__ __attribute__((aligned(16))) float red[4][4][4][64][4];
  const int bid = blockIdx.x, xcd = bid & 7, idx = bid >> 3;
  const int rb = (xcd & 3) * 8 + (idx & 7), cb = (xcd >> 2) * 4 + (idx >> 3);
  const int tid = threadIdx.x, lane = tid & 63;
  const int w = __builtin_amdgcn_readfirstlane(tid >> 6);
  const int r = lane & 31, h = lane >> 5;
  const char* op = ws + WS_O + (size_t)(w * 8) * 65536 + (size_t)(rb * 2) * 1024 + lane * 16;
  const char* wp = ws + WS_WOP + (size_t)(w * 8) * 16384 + (size_t)(cb * 2) * 1024 + lane * 16;
  bf16x8 fo[8][2], fw[8][2];
#pragma unroll
  for (int i = 0; i < 8; ++i) {
    fo[i][0] = *(const bf16x8*)(op + (size_t)i * 65536);
    fw[i][0] = *(const bf16x8*)(wp + (size_t)i * 16384);
    fo[i][1] = *(const bf16x8*)(op + (size_t)i * 65536 + 1024);
    fw[i][1] = *(const bf16x8*)(wp + (size_t)i * 16384 + 1024);
  }
  const int tile = rb * 2 + (w & 1), nb = cb * 2 + (w >> 1);
  const int tok = tile * 32 + r;
  const float g = ((const float*)(ws + WS_GATE))[tok];
  f32x4 bq[4];
  {
    const float* bp = (const float*)(ws + WS_B) + ((size_t)(tile * 16 + nb) * 4 * 64 + lane) * 4;
#pragma unroll
    for (int q = 0; q < 4; ++q) bq[q] = *(const f32x4*)(bp + q * 256);
  }
  __builtin_amdgcn_sched_barrier(0);
  f32x16 a[4];
#pragma unroll
  for (int t = 0; t < 4; ++t)
#pragma unroll
    for (int i = 0; i < 16; ++i) a[t][i] = 0.f;
#pragma unroll
  for (int i = 0; i < 8; ++i) {
#pragma unroll
    for (int t = 0; t < 4; ++t) a[t] = MFMA(fw[i][t >> 1], fo[i][t & 1], a[t]);
  }
#pragma unroll
  for (int t = 0; t < 4; ++t) {
#pragma unroll
    for (int q = 0; q < 4; ++q) {
      f32x4 v = {a[t][4 * q], a[t][4 * q + 1], a[t][4 * q + 2], a[t][4 * q + 3]};
      *(f32x4*)&red[t][w][q][lane][0] = v;
    }
  }
  __syncthreads();
  const float g1 = rbf(1.f - g);
#pragma unroll
  for (int q = 0; q < 4; ++q) {
    f32x4 s = *(const f32x4*)&red[w][0][q][lane][0];
#pragma unroll
    for (int u = 1; u < 4; ++u) s += *(const f32x4*)&red[w][u][q][lane][0];
    u32x2 o;
    o[0] = pk2(g * s[0] + g1 * bq[q][0], g * s[1] + g1 * bq[q][1]);
    o[1] = pk2(g * s[2] + g1 * bq[q][2], g * s[3] + g1 * bq[q][3]);
    *(u32x2*)(out + (size_t)tok * D_ + nb * 32 + 8 * q + 4 * h) = o;
  }
}

extern "C" void kernel_launch(void* const* d_in, const int* in_sizes, int n_in, void* d_out, int out_size,
                              void* d_ws, size_t ws_size, hipStream_t stream) {
  const u16* x = (const u16*)d_in[0];
  const u16* wqkv = (const u16*)d_in[1];
  const u16* wo = (const u16*)d_in[2];
  const u16* w1 = (const u16*)d_in[4];
  const u16* b1 = (const u16*)d_in[5];
  const u16* w2 = (const u16*)d_in[6];
  const u16* gw = (const u16*)d_in[7];
  char* ws = (char*)d_ws;
  k1<<<256, 768, 0, stream>>>(x, wqkv, wo, w1, b1, w2, gw, ws);
  k2<<<256, 768, 0, stream>>>(ws);
  k3<<<256, 256, 0, stream>>>(ws, (u16*)d_out);
}
```

```cpp
#include <hip/hip_runtime.h>
#include <hip/hip_bf16.h>
#include <stdint.h>

typedef short bf16x8 __attribute__((ext_vector_type(8)));
typedef short s16x4 __attribute__((ext_vector_type(4)));
typedef float f32x16 __attribute__((ext_vector_type(16)));
typedef float f32x4 __attribute__((ext_vector_type(4)));
typedef float f32x2 __attribute__((ext_vector_type(2)));
typedef __bf16 bf16x2_t __attribute__((ext_vector_type(2)));
typedef unsigned u32x4 __attribute__((ext_vector_type(4)));
typedef unsigned u32x2 __attribute__((ext_vector_type(2)));
typedef unsigned short u16;
#define DI __device__ __forceinline__
#define MFMA(a, b, c) __builtin_amdgcn_mfma_f32_32x32x16_bf16((a), (b), (c), 0, 0, 0)
#define MFMAU(a, b, c) MFMA(__builtin_bit_cast(bf16x8, a), __builtin_bit_cast(bf16x8, b), c)
#define LAS __attribute__((address_space(3)))
#define GAS __attribute__((address_space(1)))

#define KSTAG ((idx + (idx >> 3) * 2) & 7)
#define HO_AUX 16
#define FFN2_PRIO 0
#define MISH_PRIO 1
#define FFN_PRIO 1
constexpr int S_ = 2048, D_ = 512, H_ = 32, F_ = 1376;
constexpr unsigned WS_Q = 0, WS_K = 2u << 20, WS_V = 4u << 20, WS_ACT = 6u << 20, WS_O = 12u << 20, WS_W2P = 14u << 20,
                   WS_WOP = 16u << 20, WS_GATE = 17u << 20, WS_KMAX = (17u << 20) + 16384, WS_CNT = (17u << 20) + 32768,
                   WS_W1P = 18u << 20, WS_XF = 20u << 20;

DI unsigned pk2(float a, float b) {
  f32x2 v = {a, b};
  bf16x2_t r = __builtin_convertvector(v, bf16x2_t);
  return __builtin_bit_cast(unsigned, r);
}
DI float bf_lo(unsigned u) { return __uint_as_float(u << 16); }
DI float bf_hi(unsigned u) { return __uint_as_float(u & 0xffff0000u); }
DI float rbf(float x) { return bf_lo(pk2(x, 0.f)); }
DI void gldsa16(const void* sbase, unsigned voff, unsigned dst) {
  unsigned keep;
  asm volatile("s_mov_b32 %0, m0\n\ts_mov_b32 m0, %3\n\ts_nop 0\n\tglobal_load_lds_dwordx4 %1, %2\n\ts_mov_b32 m0, %0"
               : "=&s"(keep) : "v"(voff), "s"(sbase), "s"(dst) : "memory");
}
DI s16x4 ldtr(const char* l) {
  typedef short v4i16_t __attribute__((ext_vector_type(4)));
  return __builtin_amdgcn_ds_read_tr16_b64_v4i16((LAS v4i16_t*)l);
}
#define WAIT_V(n) asm volatile("s_waitcnt vmcnt(" #n ") lgkmcnt(0)" ::: "memory")
#define BAR() do { asm volatile("" ::: "memory"); __builtin_amdgcn_s_barrier(); asm volatile("" ::: "memory"); } while (0)
template <int CTRL> DI float dppf(float v) {
  return __int_as_float(__builtin_amdgcn_update_dpp(0, __float_as_int(v), CTRL, 0xF, 0xF, true));
}
DI float rdl(float v, int l) { return __int_as_float(__builtin_amdgcn_readlane(__float_as_int(v), l)); }
DI float row16_max(float v) {
  v = fmaxf(v, dppf<0xB1>(v)); v = fmaxf(v, dppf<0x4E>(v)); v = fmaxf(v, dppf<0x141>(v)); v = fmaxf(v, dppf<0x140>(v));
  return v;
}
DI float row16_sum(float v) {
  v += dppf<0xB1>(v); v += dppf<0x4E>(v); v += dppf<0x141>(v); v += dppf<0x140>(v);
  return v;
}
DI float add_half(float v) {
  auto rr = __builtin_amdgcn_permlane32_swap(__float_as_uint(v), __float_as_uint(v), false, false);
  return __uint_as_float(rr[0]) + __uint_as_float(rr[1]);
}
DI float max_half(float v) {
  auto rr = __builtin_amdgcn_permlane32_swap(__float_as_uint(v), __float_as_uint(v), false, false);
  return fmaxf(__uint_as_float(rr[0]), __uint_as_float(rr[1]));
}
#define WS_RSRC(ws) __builtin_amdgcn_make_buffer_rsrc((void*)(ws), 0, 32u << 20, 0x00020000)
#define WST16(rs, off, v) __builtin_amdgcn_raw_buffer_store_b128(__builtin_bit_cast(u32x4, v), rs, (unsigned)(off), 0, 16)
#define BLD(rs, voff, soff, aux) __builtin_amdgcn_raw_buffer_load_b128(rs, voff, soff, aux)
#define RLX_AGENT __ATOMIC_RELAXED, __HIP_MEMORY_SCOPE_AGENT
#define RLX_WG __ATOMIC_RELAXED, __HIP_MEMORY_SCOPE_WORKGROUP

DI float mish_f(float h) {
  float t = fminf(h * 1.4426950408889634f, 60.f);
  float w = __builtin_amdgcn_exp2f(t);
  float a = w + 2.f;
  float u = w * a;
  float d = __builtin_fmaf(w, a, 2.f);
  return h * u * __builtin_amdgcn_rcpf(d);
}
DI void wait_global(char* p, unsigned want) {
  for (unsigned spins = 0; spins < (1u << 22); ++spins) {
    if ((unsigned)__builtin_amdgcn_readfirstlane(__hip_atomic_load((GAS unsigned*)p, RLX_AGENT)) >= want) break;
    __builtin_amdgcn_s_sleep(2);
  }
  asm volatile("" ::: "memory");
}
DI void wait_lds(char* p, unsigned want) {
  while ((unsigned)__builtin_amdgcn_readfirstlane(__hip_atomic_load((LAS unsigned*)p, RLX_WG)) < want)
    __builtin_amdgcn_s_sleep(1);
  asm volatile("" ::: "memory");
}
DI unsigned add_lds(char* p, int lane) {
  unsigned old = 0;
  if (lane == 0) old = __hip_atomic_fetch_add((LAS unsigned*)p, 1u, RLX_WG);
  return (unsigned)__builtin_amdgcn_readfirstlane(old);
}

#define K1_LOOP_ASM \
  "s_barrier\n\t" \
  "ds_read_b128 v[48:51], v80\n\t" \
  "ds_read_b64_tr_b16 v[52:53], v84 offset:0\n\t" \
  "ds_read_b64_tr_b16 v[54:55], v84 offset:256\n\t" \
  "ds_read_b64_tr_b16 v[56:57], v84 offset:4096\n\t" \
  "ds_read_b64_tr_b16 v[58:59], v84 offset:4352\n\t" \
  "ds_read_b64_tr_b16 v[60:61], v84 offset:8192\n\t" \
  "ds_read_b64_tr_b16 v[62:63], v84 offset:8448\n\t" \
  "1:\n\t" \
  "ds_read_b128 v[64:67], v81\n\t" \
  "ds_read_b64_tr_b16 v[68:69], v84 offset:1024\n\t" \
  "ds_read_b64_tr_b16 v[70:71], v84 offset:1280\n\t" \
  "ds_read_b64_tr_b16 v[72:73], v84 offset:5120\n\t" \
  "ds_read_b64_tr_b16 v[74:75], v84 offset:5376\n\t" \
  "ds_read_b64_tr_b16 v[76:77], v84 offset:9216\n\t" \
  "ds_read_b64_tr_b16 v[78:79], v84 offset:9472\n\t" \
  "s_waitcnt lgkmcnt(7)\n\t" \
  "v_mfma_f32_32x32x16_bf16 v[0:15], v[52:55], v[48:51], v[0:15]\n\t" \
  "v_mfma_f32_32x32x16_bf16 v[16:31], v[56:59], v[48:51], v[16:31]\n\t" \
  "v_mfma_f32_32x32x16_bf16 v[32:47], v[48:51], v[60:63], v[32:47]\n\t" \
  "ds_read_b128 v[48:51], v82\n\t" \
  "ds_read_b64_tr_b16 v[52:53], v84 offset:2048\n\t" \
  "ds_read_b64_tr_b16 v[54:55], v84 offset:2304\n\t" \
  "ds_read_b64_tr_b16 v[56:57], v84 offset:6144\n\t" \
  "ds_read_b64_tr_b16 v[58:59], v84 offset:6400\n\t" \
  "ds_read_b64_tr_b16 v[60:61], v84 offset:10240\n\t" \
  "ds_read_b64_tr_b16 v[62:63], v84 offset:10496\n\t" \
  "s_waitcnt lgkmcnt(7)\n\t" \
  "v_mfma_f32_32x32x16_bf16 v[0:15], v[68:71], v[64:67], v[0:15]\n\t" \
  "v_mfma_f32_32x32x16_bf16 v[16:31], v[72:75], v[64:67], v[16:31]\n\t" \
  "v_mfma_f32_32x32x16_bf16 v[32:47], v[64:67], v[76:79], v[32:47]\n\t" \
  "ds_read_b128 v[64:67], v83\n\t" \
  "ds_read_b64_tr_b16 v[68:69], v84 offset:3072\n\t" \
  "ds_read_b64_tr_b16 v[70:71], v84 offset:3328\n\t" \
  "ds_read_b64_tr_b16 v[72:73], v84 offset:7168\n\t" \
  "ds_read_b64_tr_b16 v[74:75], v84 offset:7424\n\t" \
  "ds_read_b64_tr_b16 v[76:77], v84 offset:11264\n\t" \
  "ds_read_b64_tr_b16 v[78:79], v84 offset:11520\n\t" \
  "s_waitcnt lgkmcnt(7)\n\t" \
  "v_mfma_f32_32x32x16_bf16 v[0:15], v[52:55], v[48:51], v[0:15]\n\t" \
  "v_mfma_f32_32x32x16_bf16 v[16:31], v[56:59], v[48:51], v[16:31]\n\t" \
  "v_mfma_f32_32x32x16_bf16 v[32:47], v[48:51], v[60:63], v[32:47]\n\t" \
  "s_cmp_eq_u32 %[kt], 7\n\t" \
  "s_cbranch_scc1 9f\n\t" \
  "s_add_u32 %[kt], %[kt], 1\n\t" \
  "s_and_b32 %[t0], %[kt], 3\n\t" \
  "s_cmp_eq_u32 %[t0], 0\n\t" \
  "s_cselect_b32 %[t0], %[cmw], %[cst]\n\t" \
  "v_add_u32 v80, %[t0], v80\n\t" \
  "v_add_u32 v81, %[t0], v81\n\t" \
  "v_add_u32 v82, %[t0], v82\n\t" \
  "v_add_u32 v83, %[t0], v83\n\t" \
  "v_add_u32 v84, %[t0], v84\n\t" \
  "s_cmp_lt_u32 %[kt], 4\n\t" \
  "s_cbranch_scc1 3f\n\t" \
  "s_waitcnt vmcnt(0)\n\t" \
  "3:\n\t" \
  "s_waitcnt lgkmcnt(0)\n\t" \
  "s_barrier\n\t" \
  "ds_read_b128 v[48:51], v80\n\t" \
  "ds_read_b64_tr_b16 v[52:53], v84 offset:0\n\t" \
  "ds_read_b64_tr_b16 v[54:55], v84 offset:256\n\t" \
  "ds_read_b64_tr_b16 v[56:57], v84 offset:4096\n\t" \
  "ds_read_b64_tr_b16 v[58:59], v84 offset:4352\n\t" \
  "ds_read_b64_tr_b16 v[60:61], v84 offset:8192\n\t" \
  "ds_read_b64_tr_b16 v[62:63], v84 offset:8448\n\t" \
  "v_mfma_f32_32x32x16_bf16 v[0:15], v[68:71], v[64:67], v[0:15]\n\t" \
  "v_mfma_f32_32x32x16_bf16 v[16:31], v[72:75], v[64:67], v[16:31]\n\t" \
  "v_mfma_f32_32x32x16_bf16 v[32:47], v[64:67], v[76:79], v[32:47]\n\t" \
  "s_branch 1b\n\t" \
  "9:\n\t" \
  "s_waitcnt lgkmcnt(0)\n\t" \
  "v_mfma_f32_32x32x16_bf16 v[0:15], v[68:71], v[64:67], v[0:15]\n\t" \
  "v_mfma_f32_32x32x16_bf16 v[16:31], v[72:75], v[64:67], v[16:31]\n\t" \
  "v_mfma_f32_32x32x16_bf16 v[32:47], v[64:67], v[76:79], v[32:47]\n\t" \
  "s_nop 15\n\t" \
  ""

constexpr int K1_ST = 28672, K1_SIDE = 4 * K1_ST;
__global__ __launch_bounds__(512) void k1(const u16* __restrict__ x, const u16* __restrict__ wqkv,
                                          const u16* __restrict__ wo, const u16* __restrict__ w1,
                                          const u16* __restrict__ w2, const u16* __restrict__ gw,
                                          char* __restrict__ ws) {
  __shared__ __attribute__((aligned(16))) char lds[K1_SIDE + 4 * 6144 + 1024];
  const int bid = blockIdx.x, xcd = bid & 7, idx = bid >> 3;
  const int rb = (xcd & 1) * 8 + (idx & 7), j = (xcd >> 1) * 4 + (idx >> 3);
  const int tid = threadIdx.x, lane = tid & 63;
  const int w = __builtin_amdgcn_readfirstlane(tid >> 6);
  const int r = lane & 31, h = lane >> 5, m = w & 3;
  const unsigned lb = (unsigned)(size_t)(LAS char*)lds;
  const int tile = rb * 4 + m;
  const int q4 = (lane & 15) >> 2, p4 = lane & 3, grp = (lane >> 4) & 1;
  const int sw = (r >> 1) & 7;
  const auto rs = WS_RSRC(ws);
  const int u0 = (bid * 4 + m) * 2;
  const int tok = rb * 128 + j * 8 + 2 * m;
  if (w >= 4) {
    const unsigned xo0 = (unsigned)((lane >> 3) * D_ + (((lane & 7) ^ ((lane >> 4) & 7)) * 8)) * 2u;
    const unsigned xo1 = (unsigned)((8 + (lane >> 3)) * D_ + (((lane & 7) ^ ((4 + (lane >> 4)) & 7)) * 8)) * 2u;
    const unsigned wqo = (unsigned)((lane >> 2) * 1536 + (lane & 3) * 8) * 2u;
    const int kq0 = KSTAG;
    int kq = kq0;
    const char* xb = (const char*)(x + (size_t)(rb * 128 + 32 * m) * D_ + 64 * kq);
    const char* wqb = (const char*)(wqkv + (size_t)(16 * m + 64 * kq) * 1536 + 64 * j);
    const unsigned vo_ = wqo + 2048 - 64 * j;
    unsigned xl = lb + m * 4096, wl = lb + 16384 + m * 1024;
#define STAGE()                                                                                   \
  do {                                                                                            \
    gldsa16(wqb, wqo, wl);                                                                        \
    gldsa16(wqb, wqo + 64, wl + 4096);                                                            \
    gldsa16(wqb, vo_, wl + 8192);                                                                 \
    gldsa16(xb, xo0, xl);                                                                         \
    gldsa16(xb, xo1, xl + 1024);                                                                  \
    gldsa16(xb + 16 * D_ * 2, xo0, xl + 2048);                                                    \
    gldsa16(xb + 16 * D_ * 2, xo1, xl + 3072);                                                    \
    kq = (kq + 1) & 7;                                                                            \
    { const int dk_ = kq ? 128 : -896; xb += dk_; wqb += dk_ * 1536; }                            \
    const bool wrap_ = xl + K1_ST >= lb + 4 * K1_ST;                                              \
    xl += wrap_ ? 0u - 3u * K1_ST : K1_ST; wl += wrap_ ? 0u - 3u * K1_ST : K1_ST;                 \
  } while (0)
    const int sstar = ((j >> 1) - kq0) & 7;
    const char* xsrc = lds + (sstar & 3) * K1_ST + m * 4096 + r * 128;
    u32x4 xc0 = {0, 0, 0, 0}, xc1 = {0, 0, 0, 0};
#define XCOPY(s)                                                                                  \
  if ((s) == sstar) {                                                                             \
    xc0 = *(const u32x4*)(xsrc + (((4 * (j & 1) + h) ^ sw) * 16));                                \
    xc1 = *(const u32x4*)(xsrc + (((4 * (j & 1) + 2 + h) ^ sw) * 16));                            \
  }
    const char* sp = lds + K1_SIDE + m * 6144;
    auto prepack = [&](int c) {
      const int u = u0 + c;
      if (u < 1648) {
        const bool is1 = u < 704;
        const int uu = is1 ? u : u - 704;
        const int kb = is1 ? uu / 22 : uu >> 3, pr = is1 ? uu % 22 : uu & 7;
        const char* pp = sp + c * 2048 + ((is1 ? 8 : 4) * h + q4) * 128 + grp * 32 + p4 * 8;
        const int hi_off = is1 ? 512 : 1024;
#pragma unroll
        for (int n = 0; n < 2; ++n) {
          const s16x4 lo = ldtr(pp + n * 64), hi = ldtr(pp + n * 64 + hi_off);
          const bf16x8 f = __builtin_shufflevector(lo, hi, 0, 1, 2, 3, 4, 5, 6, 7);
          const int blk = 2 * pr + n;
          const unsigned dst = is1 ? WS_W1P + (kb * 43 + blk) * 1024
                                   : (kb < 86 ? WS_W2P + (kb * 16 + blk) * 1024 : WS_WOP + ((kb - 86) * 16 + blk) * 1024);
          if (!is1 || blk < 43) WST16(rs, dst + lane * 16, f);
        }
      }
    };
    if (bid == 0 && w == 4 && lane < 32) ((unsigned*)(ws + WS_CNT))[lane * 32] = 0;
    STAGE();
    STAGE();
    WAIT_V(7); BAR();
    XCOPY(0);
    STAGE();
#pragma unroll 1
    for (int kt = 1; kt < 6; ++kt) {
      STAGE();
      WAIT_V(14); BAR();
      XCOPY(kt);
    }
#undef STAGE
    WAIT_V(0);
    prepack(0);
    asm volatile("s_waitcnt lgkmcnt(0)" ::: "memory"); BAR();
    XCOPY(6);
    prepack(1);
    asm volatile("s_waitcnt lgkmcnt(0)" ::: "memory"); BAR();
    XCOPY(7);
    WST16(rs, WS_XF + ((4 * (j >> 1) + 2 * (j & 1)) * 64 + tile) * 1024 + lane * 16, xc0);
    WST16(rs, WS_XF + ((4 * (j >> 1) + 2 * (j & 1) + 1) * 64 + tile) * 1024 + lane * 16, xc1);
    {
      const u32x4 ones = {0xFFFFFFFFu, 0xFFFFFFFFu, 0xFFFFFFFFu, 0xFFFFFFFFu};
      WST16(rs, WS_O + (bid * 8 + 2 * m) * 1024 + lane * 16, ones);
      WST16(rs, WS_O + (bid * 8 + 2 * m + 1) * 1024 + lane * 16, ones);
    }
    const u32x4 gg = *(const u32x4*)(lds + K1_SIDE + 24576 + lane * 16);
#pragma unroll
    for (int t = 0; t < 2; ++t) {
      const u32x4 gx = *(const u32x4*)(sp + 4096 + t * 1024 + lane * 16);
      float s = 0.f;
#pragma unroll
      for (int e = 0; e < 4; ++e) {
        s += rbf(bf_lo(gx[e]) * bf_lo(gg[e]));
        s += rbf(bf_hi(gx[e]) * bf_hi(gg[e]));
      }
      s = row16_sum(s);
      s = (rdl(s, 0) + rdl(s, 16)) + (rdl(s, 32) + rdl(s, 48));
      s = rbf(s);
      const float g = rbf(__builtin_amdgcn_rcpf(1.f + __builtin_amdgcn_exp2f(-s * 1.4426950408889634f)));
      if (lane == 0) ((float*)(ws + WS_GATE))[tok + t] = g;
    }
    return;
  }

  __builtin_amdgcn_s_sleep(12);
  {
    const unsigned sl = lb + K1_SIDE + m * 6144;
#pragma unroll
    for (int c = 0; c < 2; ++c) {
      const int u = min(u0 + c, 1647);
      const bool is1 = u < 704;
      const int uu = is1 ? u : u - 704;
      const int kb = is1 ? uu / 22 : uu >> 3, pr = is1 ? uu % 22 : uu & 7;
      const int ld = is1 ? F_ : D_;
      const u16* src = is1 ? w1 + (size_t)kb * 16 * F_ : (kb < 86 ? w2 + (size_t)kb * 16 * D_ : wo + (size_t)(kb - 86) * 16 * D_);
      const unsigned po = (unsigned)((lane >> 3) * ld + min(64 * pr + (lane & 7) * 8, ld - 8)) * 2u;
      gldsa16(src, po, sl + c * 2048);
      gldsa16(src + 8 * ld, po, sl + c * 2048 + 1024);
    }
    gldsa16(x + (size_t)tok * D_, lane * 16, sl + 4096);
    gldsa16(x + (size_t)tok * D_, lane * 16 + 1024, sl + 5120);
    if (m == 0) gldsa16(gw, lane * 16, lb + K1_SIDE + 24576);
  }
  f32x16 acc[3];
#pragma unroll
  for (int b = 0; b < 3; ++b)
#pragma unroll
    for (int i = 0; i < 16; ++i) acc[b][i] = 0.f;
  {
    const unsigned xr0 = lb + m * 4096 + r * 128;
    unsigned xa0 = xr0 + ((0 + h) ^ sw) * 16, xa1 = xr0 + ((2 + h) ^ sw) * 16, xa2 = xr0 + ((4 + h) ^ sw) * 16,
             xa3 = xr0 + ((6 + h) ^ sw) * 16;
    unsigned wa = lb + 16384 + (8 * h + q4) * 64 + grp * 32 + p4 * 8;
    unsigned kt = 0, t0;
    asm volatile(K1_LOOP_ASM
                 : "+{v[0:15]}"(acc[0]), "+{v[16:31]}"(acc[1]), "+{v[32:47]}"(acc[2]), "+{v80}"(xa0),
                   "+{v81}"(xa1), "+{v82}"(xa2), "+{v83}"(xa3), "+{v84}"(wa), [kt] "+s"(kt), [t0] "=&s"(t0)
                 : [cst] "s"((unsigned)K1_ST), [cmw] "s"(0u - 3u * K1_ST)
                 : "memory", "scc", "v48", "v49", "v50", "v51", "v52", "v53", "v54", "v55", "v56", "v57",
                   "v58", "v59", "v60", "v61", "v62", "v63", "v64", "v65", "v66", "v67", "v68", "v69", "v70",
                   "v71", "v72", "v73", "v74", "v75", "v76", "v77", "v78", "v79");
  }
#pragma unroll
  for (int b = 0; b < 2; ++b) {
    const int c0 = 64 * j + 32 * b;
    const bool isq = c0 < 512;
    const float sc = isq ? 0.36067376022224085f : 1.0f;
    const int head0 = (c0 & 511) >> 4;
    const unsigned dst = isq ? WS_Q : WS_K;
#pragma unroll
    for (int hh = 0; hh < 2; ++hh) {
      u32x4 o;
#pragma unroll
      for (int e = 0; e < 4; ++e) o[e] = pk2(acc[b][8 * hh + 2 * e] * sc, acc[b][8 * hh + 2 * e + 1] * sc);
      WST16(rs, dst + (((head0 + hh) * 64 + tile) * 64 + lane) * 16, o);
      if (!isq) {
        float ss = 0.f;
#pragma unroll
        for (int e = 0; e < 4; ++e) {
          const float a = bf_lo(o[e]), c = bf_hi(o[e]);
          ss = __builtin_fmaf(a, a, ss);
          ss = __builtin_fmaf(c, c, ss);
        }
        ss = row16_max(add_half(ss));
        ss = fmaxf(rdl(ss, 0), rdl(ss, 16));
        if (lane == 0) ((float*)(ws + WS_KMAX))[(head0 + hh) * 64 + tile] = ss;
      }
    }
  }
  {
    const int head = 2 * j + (r >> 4), d = r & 15;
#pragma unroll
    for (int s = 0; s < 2; ++s) {
      u32x4 o;
#pragma unroll
      for (int e = 0; e < 4; ++e) o[e] = pk2(acc[2][8 * s + 2 * e], acc[2][8 * s + 2 * e + 1]);
      WST16(rs, WS_V + (head * 64 + tile) * 1024 + s * 512 + h * 256 + d * 16, o);
    }
  }
}

#define ATTN_LOOP_ASM \
  "1:\n\t" \
  "v_exp_f32 v32, v32\n\t" \
  "v_exp_f32 v33, v33\n\t" \
  "v_exp_f32 v34, v34\n\t" \
  "v_cvt_pk_bf16_f32 v88, v32, v33\n\t" \
  "v_mfma_f32_32x32x16_bf16 v[0:15], v[84:87], v[92:95], v[0:15]\n\t" \
  "s_mov_b64 exec, %[mask]\n\t" \
  "ds_read_b128 v[80:83], v97 offset:1024\n\t" \
  "ds_read_b128 v[84:87], v97 offset:1536\n\t" \
  "s_mov_b64 exec, -1\n\t" \
  "v_exp_f32 v35, v35\n\t" \
  "v_exp_f32 v36, v36\n\t" \
  "v_cvt_pk_bf16_f32 v89, v34, v35\n\t" \
  "v_exp_f32 v37, v37\n\t" \
  "v_exp_f32 v38, v38\n\t" \
  "v_cvt_pk_bf16_f32 v90, v36, v37\n\t" \
  "v_exp_f32 v39, v39\n\t" \
  "v_exp_f32 v40, v40\n\t" \
  "v_cvt_pk_bf16_f32 v91, v38, v39\n\t" \
  "v_exp_f32 v41, v41\n\t" \
  "v_exp_f32 v42, v42\n\t" \
  "v_mfma_f32_32x32x16_bf16 v[0:15], v[72:75], v[88:91], v[0:15]\n\t" \
  "v_cvt_pk_bf16_f32 v92, v40, v41\n\t" \
  "v_exp_f32 v43, v43\n\t" \
  "v_exp_f32 v44, v44\n\t" \
  "v_cvt_pk_bf16_f32 v93, v42, v43\n\t" \
  "v_exp_f32 v45, v45\n\t" \
  "v_exp_f32 v46, v46\n\t" \
  "v_cvt_pk_bf16_f32 v94, v44, v45\n\t" \
  "v_exp_f32 v47, v47\n\t" \
  "s_waitcnt lgkmcnt(0)\n\t" \
  "v_cvt_pk_bf16_f32 v95, v46, v47\n\t" \
  "v_mfma_f32_32x32x16_bf16 v[32:47], v[68:71], v[64:67], v[16:31]\n\t" \
  "ds_read_b128 v[68:71], v96 offset:3072\n\t" \
  "v_exp_f32 v48, v48\n\t" \
  "v_exp_f32 v49, v49\n\t" \
  "v_exp_f32 v50, v50\n\t" \
  "v_cvt_pk_bf16_f32 v88, v48, v49\n\t" \
  "v_mfma_f32_32x32x16_bf16 v[0:15], v[76:79], v[92:95], v[0:15]\n\t" \
  "s_mov_b64 exec, %[mask]\n\t" \
  "ds_read_b128 v[72:75], v97 offset:2048\n\t" \
  "ds_read_b128 v[76:79], v97 offset:2560\n\t" \
  "s_mov_b64 exec, -1\n\t" \
  "v_exp_f32 v51, v51\n\t" \
  "v_exp_f32 v52, v52\n\t" \
  "v_cvt_pk_bf16_f32 v89, v50, v51\n\t" \
  "v_exp_f32 v53, v53\n\t" \
  "v_exp_f32 v54, v54\n\t" \
  "v_cvt_pk_bf16_f32 v90, v52, v53\n\t" \
  "v_exp_f32 v55, v55\n\t" \
  "v_exp_f32 v56, v56\n\t" \
  "v_cvt_pk_bf16_f32 v91, v54, v55\n\t" \
  "v_exp_f32 v57, v57\n\t" \
  "v_exp_f32 v58, v58\n\t" \
  "v_mfma_f32_32x32x16_bf16 v[0:15], v[80:83], v[88:91], v[0:15]\n\t" \
  "v_cvt_pk_bf16_f32 v92, v56, v57\n\t" \
  "v_exp_f32 v59, v59\n\t" \
  "v_exp_f32 v60, v60\n\t" \
  "v_cvt_pk_bf16_f32 v93, v58, v59\n\t" \
  "v_exp_f32 v61, v61\n\t" \
  "v_exp_f32 v62, v62\n\t" \
  "v_cvt_pk_bf16_f32 v94, v60, v61\n\t" \
  "v_exp_f32 v63, v63\n\t" \
  "s_waitcnt lgkmcnt(0)\n\t" \
  "v_cvt_pk_bf16_f32 v95, v62, v63\n\t" \
  "v_mfma_f32_32x32x16_bf16 v[48:63], v[68:71], v[64:67], v[16:31]\n\t" \
  "ds_read_b128 v[68:71], v96 offset:4096\n\t" \
  "v_exp_f32 v32, v32\n\t" \
  "v_exp_f32 v33, v33\n\t" \
  "v_exp_f32 v34, v34\n\t" \
  "v_cvt_pk_bf16_f32 v88, v32, v33\n\t" \
  "v_mfma_f32_32x32x16_bf16 v[0:15], v[84:87], v[92:95], v[0:15]\n\t" \
  "s_mov_b64 exec, %[mask]\n\t" \
  "ds_read_b128 v[80:83], v97 offset:3072\n\t" \
  "ds_read_b128 v[84:87], v97 offset:3584\n\t" \
  "s_mov_b64 exec, -1\n\t" \
  "v_exp_f32 v35, v35\n\t" \
  "v_exp_f32 v36, v36\n\t" \
  "v_cvt_pk_bf16_f32 v89, v34, v35\n\t" \
  "v_exp_f32 v37, v37\n\t" \
  "v_exp_f32 v38, v38\n\t" \
  "v_cvt_pk_bf16_f32 v90, v36, v37\n\t" \
  "v_exp_f32 v39, v39\n\t" \
  "v_exp_f32 v40, v40\n\t" \
  "v_cvt_pk_bf16_f32 v91, v38, v39\n\t" \
  "v_exp_f32 v41, v41\n\t" \
  "v_exp_f32 v42, v42\n\t" \
  "v_mfma_f32_32x32x16_bf16 v[0:15], v[72:75], v[88:91], v[0:15]\n\t" \
  "v_cvt_pk_bf16_f32 v92, v40, v41\n\t" \
  "v_exp_f32 v43, v43\n\t" \
  "v_exp_f32 v44, v44\n\t" \
  "v_cvt_pk_bf16_f32 v93, v42, v43\n\t" \
  "v_exp_f32 v45, v45\n\t" \
  "v_exp_f32 v46, v46\n\t" \
  "v_cvt_pk_bf16_f32 v94, v44, v45\n\t" \
  "v_exp_f32 v47, v47\n\t" \
  "s_waitcnt lgkmcnt(0)\n\t" \
  "v_cvt_pk_bf16_f32 v95, v46, v47\n\t" \
  "v_mfma_f32_32x32x16_bf16 v[32:47], v[68:71], v[64:67], v[16:31]\n\t" \
  "ds_read_b128 v[68:71], v96 offset:5120\n\t" \
  "v_exp_f32 v48, v48\n\t" \
  "v_exp_f32 v49, v49\n\t" \
  "v_exp_f32 v50, v50\n\t" \
  "v_cvt_pk_bf16_f32 v88, v48, v49\n\t" \
  "v_mfma_f32_32x32x16_bf16 v[0:15], v[76:79], v[92:95], v[0:15]\n\t" \
  "s_mov_b64 exec, %[mask]\n\t" \
  "ds_read_b128 v[72:75], v97 offset:4096\n\t" \
  "ds_read_b128 v[76:79], v97 offset:4608\n\t" \
  "s_mov_b64 exec, -1\n\t" \
  "v_exp_f32 v51, v51\n\t" \
  "v_exp_f32 v52, v52\n\t" \
  "v_cvt_pk_bf16_f32 v89, v50, v51\n\t" \
  "v_exp_f32 v53, v53\n\t" \
  "v_exp_f32 v54, v54\n\t" \
  "v_cvt_pk_bf16_f32 v90, v52, v53\n\t" \
  "v_exp_f32 v55, v55\n\t" \
  "v_exp_f32 v56, v56\n\t" \
  "v_cvt_pk_bf16_f32 v91, v54, v55\n\t" \
  "v_exp_f32 v57, v57\n\t" \
  "v_exp_f32 v58, v58\n\t" \
  "v_mfma_f32_32x32x16_bf16 v[0:15], v[80:83], v[88:91], v[0:15]\n\t" \
  "v_cvt_pk_bf16_f32 v92, v56, v57\n\t" \
  "v_exp_f32 v59, v59\n\t" \
  "v_exp_f32 v60, v60\n\t" \
  "v_cvt_pk_bf16_f32 v93, v58, v59\n\t" \
  "v_exp_f32 v61, v61\n\t" \
  "v_exp_f32 v62, v62\n\t" \
  "v_cvt_pk_bf16_f32 v94, v60, v61\n\t" \
  "v_exp_f32 v63, v63\n\t" \
  "s_waitcnt lgkmcnt(0)\n\t" \
  "v_cvt_pk_bf16_f32 v95, v62, v63\n\t" \
  "v_mfma_f32_32x32x16_bf16 v[48:63], v[68:71], v[64:67], v[16:31]\n\t" \
  "ds_read_b128 v[68:71], v96 offset:6144\n\t" \
  "v_add_u32 v96, 0x1000, v96\n\t" \
  "v_add_u32 v97, 0x1000, v97\n\t" \
  "s_sub_u32 %[cnt], %[cnt], 1\n\t" \
  "s_and_b32 %[tmp], %[cnt], 13\n\t" \
  "s_cmp_lg_u32 %[tmp], 12\n\t" \
  "s_cbranch_scc1 2f\n\t" \
  "3:\n\t" \
  "ds_read_b32 v98, v99\n\t" \
  "s_waitcnt lgkmcnt(0)\n\t" \
  "v_readfirstlane_b32 %[tmp], v98\n\t" \
  "s_cmp_ge_u32 %[tmp], 4\n\t" \
  "s_cbranch_scc1 4f\n\t" \
  "s_sleep 1\n\t" \
  "s_branch 3b\n\t" \
  "4:\n\t" \
  "v_add_u32 v99, 4, v99\n\t" \
  "2:\n\t" \
  "s_cmp_lg_u32 %[cnt], 7\n\t" \
  "s_cbranch_scc1 5f\n\t" \
  "v_mov_b32 v98, 1\n\t" \
  "s_mov_b64 exec, 1\n\t" \
  "ds_add_u32 v99, v98\n\t" \
  "s_mov_b64 exec, -1\n\t" \
  "5:\n\t" \
  "s_cmp_lg_u32 %[cnt], 0\n\t" \
  "s_cbranch_scc1 1b\n\t" \
  "s_waitcnt lgkmcnt(0)\n\t" \
  "s_nop 15\n\t" \
  ""

constexpr int L2_FLAG = 131072;
__global__ __launch_bounds__(768) void k2(char* __restrict__ ws, const u16* __restrict__ b1, u16* __restrict__ out) {
  __shared__ __attribute__((aligned(16))) char lds[L2_FLAG + 4096];
  const int bid = blockIdx.x, xcd = bid & 7, idx = bid >> 3;
  const int head = xcd * 4 + (idx & 3), qb = idx >> 2;
  const int tp = xcd >> 1, cp = (xcd & 1) * 4 + (idx & 3);
  const int T0 = qb * 8 + 2 * tp;
  const int tid = threadIdx.x, lane = tid & 63;
  const int w = __builtin_amdgcn_readfirstlane(tid >> 6);
  const int r = lane & 31, h = lane >> 5;
  const unsigned lb = (unsigned)(size_t)(LAS char*)lds;
  const unsigned l16 = lane * 16;
  const auto rs = WS_RSRC(ws);
  char* cntF = ws + WS_CNT + (qb * 4 + tp) * 128;
  if (w >= 8) {
    const int L = w - 8;
    {
      const char* ksrc = ws + WS_K + (size_t)(head * 64 + L) * 1024;
      const char* vsrc = ws + WS_V + (size_t)(head * 64 + L) * 1024;
      const unsigned kdst = lb + L * 1024, vdst = lb + 65536 + L * 1024;
#pragma unroll
      for (int c = 0; c < 3; ++c) { gldsa16(ksrc + c * 4096, l16, kdst + c * 4096); gldsa16(vsrc + c * 4096, l16, vdst + c * 4096); }
      if (tid >= 512 && tid < 520) ((volatile LAS unsigned*)(lds + L2_FLAG))[tid - 512] = 0;
      WAIT_V(0); BAR();
#pragma unroll 1
      for (int c = 3; c < 16; ++c) {
        gldsa16(ksrc + c * 4096, l16, kdst + c * 4096); gldsa16(vsrc + c * 4096, l16, vdst + c * 4096);
      }
    }
    {
      const int T = T0 + (L & 1);
      const int f0 = cp + 24 * (L >> 1);
      const int fbc2 = min(f0 + 16, 42);
      const auto rx = __builtin_amdgcn_make_buffer_rsrc((void*)(ws + WS_XF + T * 1024), 0, 31 * 65536 + 1024, 0x00020000);
      const auto rw = __builtin_amdgcn_make_buffer_rsrc((void*)(ws + WS_W1P), 0, 32 * 43 * 1024, 0x00020000);
      const unsigned wv0 = l16 + f0 * 1024, wv1 = l16 + (f0 + 8) * 1024, wv2 = l16 + fbc2 * 1024;
      constexpr int PD = 6;
      u32x4 fx[PD], fw[PD][3];
#pragma unroll
      for (int i = 0; i < PD; ++i) {
        fx[i] = BLD(rx, l16, i * 65536, 0);
        fw[i][0] = BLD(rw, wv0, i * 44032, 0); fw[i][1] = BLD(rw, wv1, i * 44032, 0); fw[i][2] = BLD(rw, wv2, i * 44032, 0);
      }
      WAIT_V(38);
      add_lds(lds + L2_FLAG, lane);
      WAIT_V(24);
      add_lds(lds + L2_FLAG + 4, lane);
      __builtin_amdgcn_s_setprio(FFN_PRIO);
      f32x16 a[3];
#pragma unroll
      for (int g = 0; g < 4; ++g) {
        const u32x2 b0 = *(const u32x2*)(b1 + 32 * f0 + 8 * g + 4 * h);
        const u32x2 b1_ = *(const u32x2*)(b1 + 32 * (f0 + 8) + 8 * g + 4 * h);
        const u32x2 b2 = *(const u32x2*)(b1 + 32 * fbc2 + 8 * g + 4 * h);
        a[0][4 * g] = bf_lo(b0[0]); a[0][4 * g + 1] = bf_hi(b0[0]); a[0][4 * g + 2] = bf_lo(b0[1]); a[0][4 * g + 3] = bf_hi(b0[1]);
        a[1][4 * g] = bf_lo(b1_[0]); a[1][4 * g + 1] = bf_hi(b1_[0]); a[1][4 * g + 2] = bf_lo(b1_[1]); a[1][4 * g + 3] = bf_hi(b1_[1]);
        a[2][4 * g] = bf_lo(b2[0]); a[2][4 * g + 1] = bf_hi(b2[0]); a[2][4 * g + 2] = bf_lo(b2[1]); a[2][4 * g + 3] = bf_hi(b2[1]);
      }
      __builtin_amdgcn_sched_barrier(0);
#pragma unroll 1
      for (int k0 = PD; k0 < 30 + PD; k0 += PD) {
#pragma unroll
        for (int i = 0; i < PD; ++i) {
          a[0] = MFMAU(fw[i][0], fx[i], a[0]);
          a[1] = MFMAU(fw[i][1], fx[i], a[1]);
          a[2] = MFMAU(fw[i][2], fx[i], a[2]);
          fx[i] = BLD(rx, l16, (k0 + i) * 65536, 0);
          fw[i][0] = BLD(rw, wv0, (k0 + i) * 44032, 0); fw[i][1] = BLD(rw, wv1, (k0 + i) * 44032, 0);
          fw[i][2] = BLD(rw, wv2, (k0 + i) * 44032, 0);
          __builtin_amdgcn_sched_barrier(0);
        }
      }
#pragma unroll
      for (int i = 0; i < 2; ++i) {
        a[0] = MFMAU(fw[i][0], fx[i], a[0]);
        a[1] = MFMAU(fw[i][1], fx[i], a[1]);
        a[2] = MFMAU(fw[i][2], fx[i], a[2]);
      }
      const float g1 = rbf(1.f - ((const float*)(ws + WS_GATE))[T * 32 + r]);
      __builtin_amdgcn_s_setprio(MISH_PRIO);
#pragma unroll
      for (int b = 0; b < 3; ++b) {
        if (f0 + 8 * b < 43) {
#pragma unroll
          for (int hh = 0; hh < 2; ++hh) {
            u32x4 o;
#pragma unroll
            for (int gq = 0; gq < 2; ++gq) {
              const int g = 2 * hh + gq;
              const float h0 = a[b][4 * g + 0], h1 = a[b][4 * g + 1], h2 = a[b][4 * g + 2], h3 = a[b][4 * g + 3];
              o[2 * gq] = pk2(mish_f(h0) * g1, mish_f(h1) * g1);
              o[2 * gq + 1] = pk2(mish_f(h2) * g1, mish_f(h3) * g1);
            }
            WST16(rs, WS_ACT + ((2 * (f0 + 8 * b) + hh) * 64 + T) * 1024 + l16, o);
          }
        }
      }
      __builtin_amdgcn_s_setprio(FFN2_PRIO);
      WAIT_V(0);
      if (add_lds(lds + L2_FLAG + 12, lane) == 3 && lane == 0) __hip_atomic_fetch_add((GAS unsigned*)cntF, 1u, RLX_AGENT);
    }
    {
      const int klast = min(22 * L + 21, 85);
      const auto ra = __builtin_amdgcn_make_buffer_rsrc((void*)(ws + WS_ACT + T0 * 1024), 0, klast * 65536 + 2048, 0x00020000);
      const auto rw = __builtin_amdgcn_make_buffer_rsrc((void*)(ws + WS_W2P + cp * 2048), 0, klast * 16384 + 2048, 0x00020000);
      constexpr int PD = 5;
      u32x4 fa[PD][2], fw[PD][2];
      const int kb0 = 22 * L;
      wait_global(cntF, 8);
#pragma unroll
      for (int i = 0; i < PD; ++i) {
        fa[i][0] = BLD(ra, l16, (kb0 + i) * 65536, HO_AUX); fa[i][1] = BLD(ra, l16 + 1024, (kb0 + i) * 65536, HO_AUX);
        fw[i][0] = BLD(rw, l16, (kb0 + i) * 16384, 0); fw[i][1] = BLD(rw, l16 + 1024, (kb0 + i) * 16384, 0);
        __builtin_amdgcn_sched_barrier(0);
      }
      f32x16 b[4];
#pragma unroll
      for (int t = 0; t < 4; ++t)
#pragma unroll
        for (int i = 0; i < 16; ++i) b[t][i] = 0.f;
      __builtin_amdgcn_sched_barrier(0);
#pragma unroll 1
      for (int k0 = PD; k0 < 20 + PD; k0 += PD) {
#pragma unroll
        for (int i = 0; i < PD; ++i) {
#pragma unroll
          for (int t = 0; t < 4; ++t) b[t] = MFMAU(fw[i][t >> 1], fa[i][t & 1], b[t]);
          const int kn = kb0 + k0 + i;
          fa[i][0] = BLD(ra, l16, kn * 65536, HO_AUX); fa[i][1] = BLD(ra, l16 + 1024, kn * 65536, HO_AUX);
          fw[i][0] = BLD(rw, l16, kn * 16384, 0); fw[i][1] = BLD(rw, l16 + 1024, kn * 16384, 0);
          __builtin_amdgcn_sched_barrier(0);
        }
      }
#pragma unroll
      for (int i = 0; i < 2; ++i)
#pragma unroll
        for (int t = 0; t < 4; ++t) b[t] = MFMAU(fw[i][t >> 1], fa[i][t & 1], b[t]);
      wait_lds(lds + L2_FLAG + 8, 8);
      {
        const char* wsrc = ws + WS_WOP + cp * 2048 + (size_t)L * 8 * 16384;
        const unsigned wdst = lb + (L >> 1) * 65536 + (L & 1) * 16384;
#pragma unroll 1
        for (int i = 0; i < 8; ++i) {
          gldsa16(wsrc + i * 16384, l16, wdst + i * 2048);
          gldsa16(wsrc + i * 16384, l16 + 1024, wdst + i * 2048 + 1024);
        }
      }
      WAIT_V(0);
      BAR();
      char* bs = lds + (L >> 1) * 65536 + 32768 + (L & 1) * 16384 + l16;
#pragma unroll
      for (int t = 0; t < 4; ++t)
#pragma unroll
        for (int q = 0; q < 4; ++q) {
          f32x4 v = {b[t][4 * q], b[t][4 * q + 1], b[t][4 * q + 2], b[t][4 * q + 3]};
          *(f32x4*)(bs + (t & 1) * 8192 + (t >> 1) * 4096 + q * 1024) = v;
        }
      WAIT_V(0);
      BAR();
      BAR();
    }
    return;
  }

  const int qt = qb * 8 + w;
  bf16x8 qf = *(const bf16x8*)(ws + WS_Q + (size_t)(head * 64 + qt) * 1024 + l16);
  float km = ((const float*)(ws + WS_KMAX))[head * 64 + lane];
  const float gq = ((const float*)(ws + WS_GATE))[qt * 32 + r];
  km = row16_max(km);
  km = fmaxf(fmaxf(rdl(km, 0), rdl(km, 16)), fmaxf(rdl(km, 32), rdl(km, 48)));
  float qs = 0.f;
  {
    const u32x4 qu = __builtin_bit_cast(u32x4, qf);
#pragma unroll
    for (int e = 0; e < 4; ++e) {
      const float a = bf_lo(qu[e]), c = bf_hi(qu[e]);
      qs = __builtin_fmaf(a, a, qs);
      qs = __builtin_fmaf(c, c, qs);
    }
    qs = add_half(qs);
  }
  const float mb = __builtin_amdgcn_sqrtf(qs * km) * 1.0001f;
  f32x16 negm, O;
#pragma unroll
  for (int i = 0; i < 16; ++i) { negm[i] = -mb; O[i] = 0.f; }

  const char* kp = lds + lane * 16;
  const char* vp = lds + 65536 + h * 256 + (lane & 15) * 16;
  const bool vact = (lane & 16) == 0;
  const short one = ((lane & 27) == 16) ? (short)0x3F80 : (short)0;
  bf16x8 va0, va1, vb0, vb1;
#pragma unroll
  for (int i = 0; i < 8; ++i) { va0[i] = one; va1[i] = one; vb0[i] = one; vb1[i] = one; }
  u32x4 p0 = {0, 0, 0, 0}, p1 = {0, 0, 0, 0};
  unsigned kaddr = lb + l16, vaddr = lb + 65536 + h * 256 + (lane & 15) * 16;
  const unsigned long long vmask = 0x0000FFFF0000FFFFull;

  BAR();
  f32x16 Sa = MFMA(*(const bf16x8*)(kp), qf, negm);
  f32x16 Sb = MFMA(*(const bf16x8*)(kp + 1024), qf, negm);
  bf16x8 kf = *(const bf16x8*)(kp + 2048);
  if (vact) { va0 = *(const bf16x8*)(vp); va1 = *(const bf16x8*)(vp + 512); }
  {
    unsigned cnt = 16, tmp, fa_ = lb + L2_FLAG;
    asm volatile("s_nop 15\n\t" ATTN_LOOP_ASM
                 : "+{v[0:15]}"(O), "+{v[32:47]}"(Sa), "+{v[48:63]}"(Sb), "+{v[68:71]}"(kf),
                   "+{v[72:75]}"(va0), "+{v[76:79]}"(va1), "+{v[80:83]}"(vb0), "+{v[84:87]}"(vb1),
                   "+{v[88:91]}"(p0), "+{v[92:95]}"(p1), "+{v96}"(kaddr), "+{v97}"(vaddr), [cnt] "+s"(cnt),
                   [tmp] "=&s"(tmp), "+{v99}"(fa_)
                 : "{v[16:31]}"(negm), "{v[64:67]}"(qf), [mask] "s"(vmask)
                 : "memory", "scc", "v98");
  }
  O = MFMA(vb1, __builtin_bit_cast(bf16x8, p1), O);

  float l = O[8];
  if (__any(!(l >= 8.271806125530277e-25f))) {
    float m = -INFINITY;
    f32x16 zero;
#pragma unroll
    for (int i = 0; i < 16; ++i) { zero[i] = 0.f; O[i] = 0.f; }
    const char* kg = ws + WS_K + (size_t)head * 65536 + l16;
    const char* vg = ws + WS_V + (size_t)head * 65536 + h * 256 + (lane & 15) * 16;
#pragma unroll 1
    for (int t = 0; t < 64; ++t) {
      f32x16 Sx = MFMA(*(const bf16x8*)(kg + t * 1024), qf, zero);
      float mx = Sx[0];
#pragma unroll
      for (int i = 1; i < 16; ++i) mx = fmaxf(mx, Sx[i]);
      mx = max_half(mx);
      const float mn = fmaxf(m, mx);
      const float alpha = __builtin_amdgcn_exp2f(m - mn);
      m = mn;
#pragma unroll
      for (int i = 0; i < 16; ++i) { O[i] *= alpha; Sx[i] -= mn; }
      if (vact) { va0 = *(const bf16x8*)(vg + t * 1024); va1 = *(const bf16x8*)(vg + t * 1024 + 512); }
#pragma unroll
      for (int e = 0; e < 4; ++e) {
        p0[e] = pk2(__builtin_amdgcn_exp2f(Sx[2 * e]), __builtin_amdgcn_exp2f(Sx[2 * e + 1]));
        p1[e] = pk2(__builtin_amdgcn_exp2f(Sx[8 + 2 * e]), __builtin_amdgcn_exp2f(Sx[8 + 2 * e + 1]));
      }
      O = MFMAU(va0, p0, O);
      O = MFMAU(va1, p1, O);
    }
    l = O[8];
  }
  {
    const float rl = gq / l;
    u32x4 o;
#pragma unroll
    for (int e = 0; e < 4; ++e) o[e] = pk2(O[2 * e] * rl, O[2 * e + 1] * rl);
    WST16(rs, WS_O + (head * 64 + qt) * 1024 + l16, o);
  }
  BAR();
  {
    const int kq = w >> 1;
    u32x4 fo[8];
#pragma unroll
    for (int i = 0; i < 8; ++i) fo[i] = BLD(rs, l16, WS_O + ((kq * 8 + i) * 64 + T0 + (w & 1)) * 1024, HO_AUX);
    const char* wp = lds + (kq >> 1) * 65536 + (kq & 1) * 16384 + l16;
    char* pp = lds + (kq >> 1) * 65536 + 32768 + (kq & 1) * 16384 + (w & 1) * 8192 + l16;
    BAR();
#pragma unroll 1
    for (unsigned spins = 0; spins < (1u << 20); ++spins) {
      bool ok = true;
#pragma unroll
      for (int i = 0; i < 8; ++i) {
        const unsigned mx = max(max(fo[i][0], fo[i][1]), max(fo[i][2], fo[i][3]));
        if (__any(mx == 0xFFFFFFFFu)) {
          fo[i] = BLD(rs, l16, WS_O + ((kq * 8 + i) * 64 + T0 + (w & 1)) * 1024, HO_AUX);
          ok = false;
        }
      }
      if (ok) break;
    }
    f32x16 a0, a1;
#pragma unroll
    for (int q = 0; q < 4; ++q) {
      const f32x4 v0 = *(const f32x4*)(pp + q * 1024), v1 = *(const f32x4*)(pp + 4096 + q * 1024);
#pragma unroll
      for (int e = 0; e < 4; ++e) { a0[4 * q + e] = v0[e]; a1[4 * q + e] = v1[e]; }
    }
#pragma unroll
    for (int i = 0; i < 8; ++i) {
      a0 = MFMAU(*(const u32x4*)(wp + i * 2048), fo[i], a0);
      a1 = MFMAU(*(const u32x4*)(wp + i * 2048 + 1024), fo[i], a1);
    }
#pragma unroll
    for (int q = 0; q < 4; ++q) {
      f32x4 v0 = {a0[4 * q], a0[4 * q + 1], a0[4 * q + 2], a0[4 * q + 3]};
      f32x4 v1 = {a1[4 * q], a1[4 * q + 1], a1[4 * q + 2], a1[4 * q + 3]};
      *(f32x4*)(pp + q * 1024) = v0;
      *(f32x4*)(pp + 4096 + q * 1024) = v1;
    }
  }
  WAIT_V(0);
  BAR();
  {
    const int t = w & 3, tt = t & 1, n = t >> 1;
    const int tok = (T0 + tt) * 32 + r;
#pragma unroll
    for (int e = 0; e < 2; ++e) {
      const int q = 2 * (w >> 2) + e;
      const char* sp = lds + 32768 + tt * 8192 + n * 4096 + q * 1024 + l16;
      const f32x4 s = (*(const f32x4*)(sp) + *(const f32x4*)(sp + 16384)) +
                      (*(const f32x4*)(sp + 65536) + *(const f32x4*)(sp + 65536 + 16384));
      u32x2 o = {pk2(s[0], s[1]), pk2(s[2], s[3])};
      *(u32x2*)(out + (size_t)tok * D_ + (2 * cp + n) * 32 + 8 * q + 4 * h) = o;
    }
  }
}

extern "C" void kernel_launch(void* const* d_in, const int* in_sizes, int n_in, void* d_out, int out_size,
                              void* d_ws, size_t ws_size, hipStream_t stream) {
  const u16* x = (const u16*)d_in[0];
  const u16* wqkv = (const u16*)d_in[1];
  const u16* wo = (const u16*)d_in[2];
  const u16* w1 = (const u16*)d_in[4];
  const u16* b1 = (const u16*)d_in[5];
  const u16* w2 = (const u16*)d_in[6];
  const u16* gw = (const u16*)d_in[7];
  char* ws = (char*)d_ws;
  k1<<<256, 512, 0, stream>>>(x, wqkv, wo, w1, w2, gw, ws);
  k2<<<256, 768, 0, stream>>>(ws, b1, (u16*)d_out);
}
```

```cpp
#include <hip/hip_runtime.h>
#include <hip/hip_bf16.h>
#include <stdint.h>

typedef short bf16x8 __attribute__((ext_vector_type(8)));
typedef short s16x4 __attribute__((ext_vector_type(4)));
typedef float f32x16 __attribute__((ext_vector_type(16)));
typedef float f32x4 __attribute__((ext_vector_type(4)));
typedef float f32x2 __attribute__((ext_vector_type(2)));
typedef __bf16 bf16x2_t __attribute__((ext_vector_type(2)));
typedef unsigned u32x4 __attribute__((ext_vector_type(4)));
typedef unsigned u32x2 __attribute__((ext_vector_type(2)));
typedef unsigned short u16;
#define DI __device__ __forceinline__
#define MFMA(a, b, c) __builtin_amdgcn_mfma_f32_32x32x16_bf16((a), (b), (c), 0, 0, 0)
#define MFMAU(a, b, c) MFMA(__builtin_bit_cast(bf16x8, a), __builtin_bit_cast(bf16x8, b), c)
#define LAS __attribute__((address_space(3)))
#define GAS __attribute__((address_space(1)))

#define KSTAG ((idx + (idx >> 3) * 2) & 7)
#define HO_AUX 16
#define FFN2_PRIO 0
#define SIDE_DELAY 40
#define SENT_AT 5
#define MISH_PRIO 1
#define FFN_PRIO 1
constexpr int S_ = 2048, D_ = 512, H_ = 32, F_ = 1376;
constexpr unsigned WS_Q = 0, WS_K = 2u << 20, WS_V = 4u << 20, WS_ACT = 6u << 20, WS_O = 12u << 20, WS_W2P = 14u << 20,
                   WS_WOP = 16u << 20, WS_GATE = 17u << 20, WS_KMAX = (17u << 20) + 16384, WS_CNT = (17u << 20) + 32768,
                   WS_W1P = 18u << 20, WS_XF = 20u << 20;

DI unsigned pk2(float a, float b) {
  f32x2 v = {a, b};
  bf16x2_t r = __builtin_convertvector(v, bf16x2_t);
  return __builtin_bit_cast(unsigned, r);
}
DI float bf_lo(unsigned u) { return __uint_as_float(u << 16); }
DI float bf_hi(unsigned u) { return __uint_as_float(u & 0xffff0000u); }
DI float rbf(float x) { return bf_lo(pk2(x, 0.f)); }
DI void gldsa16(const void* sbase, unsigned voff, unsigned dst) {
  unsigned keep;
  asm volatile("s_mov_b32 %0, m0\n\ts_mov_b32 m0, %3\n\ts_nop 0\n\tglobal_load_lds_dwordx4 %1, %2\n\ts_mov_b32 m0, %0"
               : "=&s"(keep) : "v"(voff), "s"(sbase), "s"(dst) : "memory");
}
DI s16x4 ldtr(const char* l) {
  typedef short v4i16_t __attribute__((ext_vector_type(4)));
  return __builtin_amdgcn_ds_read_tr16_b64_v4i16((LAS v4i16_t*)l);
}
#define WAIT_V(n) asm volatile("s_waitcnt vmcnt(" #n ") lgkmcnt(0)" ::: "memory")
#define BAR() do { asm volatile("" ::: "memory"); __builtin_amdgcn_s_barrier(); asm volatile("" ::: "memory"); } while (0)
template <int CTRL> DI float dppf(float v) {
  return __int_as_float(__builtin_amdgcn_update_dpp(0, __float_as_int(v), CTRL, 0xF, 0xF, true));
}
DI float rdl(float v, int l) { return __int_as_float(__builtin_amdgcn_readlane(__float_as_int(v), l)); }
DI float row16_max(float v) {
  v = fmaxf(v, dppf<0xB1>(v)); v = fmaxf(v, dppf<0x4E>(v)); v = fmaxf(v, dppf<0x141>(v)); v = fmaxf(v, dppf<0x140>(v));
  return v;
}
DI float row16_sum(float v) {
  v += dppf<0xB1>(v); v += dppf<0x4E>(v); v += dppf<0x141>(v); v += dppf<0x140>(v);
  return v;
}
DI float add_half(float v) {
  auto rr = __builtin_amdgcn_permlane32_swap(__float_as_uint(v), __float_as_uint(v), false, false);
  return __uint_as_float(rr[0]) + __uint_as_float(rr[1]);
}
DI float max_half(float v) {
  auto rr = __builtin_amdgcn_permlane32_swap(__float_as_uint(v), __float_as_uint(v), false, false);
  return fmaxf(__uint_as_float(rr[0]), __uint_as_float(rr[1]));
}
#define WS_RSRC(ws) __builtin_amdgcn_make_buffer_rsrc((void*)(ws), 0, 32u << 20, 0x00020000)
#define WST16(rs, off, v) __builtin_amdgcn_raw_buffer_store_b128(__builtin_bit_cast(u32x4, v), rs, (unsigned)(off), 0, 16)
#define BLD(rs, voff, soff, aux) __builtin_amdgcn_raw_buffer_load_b128(rs, voff, soff, aux)
#define RLX_AGENT __ATOMIC_RELAXED, __HIP_MEMORY_SCOPE_AGENT
#define RLX_WG __ATOMIC_RELAXED, __HIP_MEMORY_SCOPE_WORKGROUP

DI float mish_g(float h, float g) {
  const float w = __builtin_amdgcn_exp2f(h * 1.4426950408889634f);
  const float d = __builtin_fmaf(w, w + 2.f, 2.f);
  const float hg = h * g;
  return __builtin_fmaf(-2.f * hg, __builtin_amdgcn_rcpf(d), hg);
}
DI float mish_f(float h) {
  float t = fminf(h * 1.4426950408889634f, 60.f);
  float w = __builtin_amdgcn_exp2f(t);
  float a = w + 2.f;
  float u = w * a;
  float d = __builtin_fmaf(w, a, 2.f);
  return h * u * __builtin_amdgcn_rcpf(d);
}
DI void wait_global(char* p, unsigned want) {
  for (unsigned spins = 0; spins < (1u << 22); ++spins) {
    if ((unsigned)__builtin_amdgcn_readfirstlane(__hip_atomic_load((GAS unsigned*)p, RLX_AGENT)) >= want) break;
    __builtin_amdgcn_s_sleep(2);
  }
  asm volatile("" ::: "memory");
}
DI void wait_lds(char* p, unsigned want) {
  while ((unsigned)__builtin_amdgcn_readfirstlane(__hip_atomic_load((LAS unsigned*)p, RLX_WG)) < want)
    __builtin_amdgcn_s_sleep(1);
  asm volatile("" ::: "memory");
}
DI unsigned add_lds(char* p, int lane) {
  unsigned old = 0;
  if (lane == 0) old = __hip_atomic_fetch_add((LAS unsigned*)p, 1u, RLX_WG);
  return (unsigned)__builtin_amdgcn_readfirstlane(old);
}

#define K1_LOOP_ASM \
  "s_barrier\n\t" \
  "ds_read_b128 v[48:51], v80\n\t" \
  "ds_read_b64_tr_b16 v[52:53], v84 offset:0\n\t" \
  "ds_read_b64_tr_b16 v[54:55], v84 offset:256\n\t" \
  "ds_read_b64_tr_b16 v[56:57], v84 offset:4096\n\t" \
  "ds_read_b64_tr_b16 v[58:59], v84 offset:4352\n\t" \
  "ds_read_b64_tr_b16 v[60:61], v84 offset:8192\n\t" \
  "ds_read_b64_tr_b16 v[62:63], v84 offset:8448\n\t" \
  "1:\n\t" \
  "ds_read_b128 v[64:67], v81\n\t" \
  "ds_read_b64_tr_b16 v[68:69], v84 offset:1024\n\t" \
  "ds_read_b64_tr_b16 v[70:71], v84 offset:1280\n\t" \
  "ds_read_b64_tr_b16 v[72:73], v84 offset:5120\n\t" \
  "ds_read_b64_tr_b16 v[74:75], v84 offset:5376\n\t" \
  "ds_read_b64_tr_b16 v[76:77], v84 offset:9216\n\t" \
  "ds_read_b64_tr_b16 v[78:79], v84 offset:9472\n\t" \
  "s_waitcnt lgkmcnt(7)\n\t" \
  "v_mfma_f32_32x32x16_bf16 v[0:15], v[52:55], v[48:51], v[0:15]\n\t" \
  "v_mfma_f32_32x32x16_bf16 v[16:31], v[56:59], v[48:51], v[16:31]\n\t" \
  "v_mfma_f32_32x32x16_bf16 v[32:47], v[48:51], v[60:63], v[32:47]\n\t" \
  "ds_read_b128 v[48:51], v82\n\t" \
  "ds_read_b64_tr_b16 v[52:53], v84 offset:2048\n\t" \
  "ds_read_b64_tr_b16 v[54:55], v84 offset:2304\n\t" \
  "ds_read_b64_tr_b16 v[56:57], v84 offset:6144\n\t" \
  "ds_read_b64_tr_b16 v[58:59], v84 offset:6400\n\t" \
  "ds_read_b64_tr_b16 v[60:61], v84 offset:10240\n\t" \
  "ds_read_b64_tr_b16 v[62:63], v84 offset:10496\n\t" \
  "s_waitcnt lgkmcnt(7)\n\t" \
  "v_mfma_f32_32x32x16_bf16 v[0:15], v[68:71], v[64:67], v[0:15]\n\t" \
  "v_mfma_f32_32x32x16_bf16 v[16:31], v[72:75], v[64:67], v[16:31]\n\t" \
  "v_mfma_f32_32x32x16_bf16 v[32:47], v[64:67], v[76:79], v[32:47]\n\t" \
  "ds_read_b128 v[64:67], v83\n\t" \
  "ds_read_b64_tr_b16 v[68:69], v84 offset:3072\n\t" \
  "ds_read_b64_tr_b16 v[70:71], v84 offset:3328\n\t" \
  "ds_read_b64_tr_b16 v[72:73], v84 offset:7168\n\t" \
  "ds_read_b64_tr_b16 v[74:75], v84 offset:7424\n\t" \
  "ds_read_b64_tr_b16 v[76:77], v84 offset:11264\n\t" \
  "ds_read_b64_tr_b16 v[78:79], v84 offset:11520\n\t" \
  "s_waitcnt lgkmcnt(7)\n\t" \
  "v_mfma_f32_32x32x16_bf16 v[0:15], v[52:55], v[48:51], v[0:15]\n\t" \
  "v_mfma_f32_32x32x16_bf16 v[16:31], v[56:59], v[48:51], v[16:31]\n\t" \
  "v_mfma_f32_32x32x16_bf16 v[32:47], v[48:51], v[60:63], v[32:47]\n\t" \
  "s_cmp_eq_u32 %[kt], 7\n\t" \
  "s_cbranch_scc1 9f\n\t" \
  "s_add_u32 %[kt], %[kt], 1\n\t" \
  "s_and_b32 %[t0], %[kt], 3\n\t" \
  "s_cmp_eq_u32 %[t0], 0\n\t" \
  "s_cselect_b32 %[t0], %[cmw], %[cst]\n\t" \
  "v_add_u32 v80, %[t0], v80\n\t" \
  "v_add_u32 v81, %[t0], v81\n\t" \
  "v_add_u32 v82, %[t0], v82\n\t" \
  "v_add_u32 v83, %[t0], v83\n\t" \
  "v_add_u32 v84, %[t0], v84\n\t" \
  "s_waitcnt lgkmcnt(0)\n\t" \
  "s_barrier\n\t" \
  "ds_read_b128 v[48:51], v80\n\t" \
  "ds_read_b64_tr_b16 v[52:53], v84 offset:0\n\t" \
  "ds_read_b64_tr_b16 v[54:55], v84 offset:256\n\t" \
  "ds_read_b64_tr_b16 v[56:57], v84 offset:4096\n\t" \
  "ds_read_b64_tr_b16 v[58:59], v84 offset:4352\n\t" \
  "ds_read_b64_tr_b16 v[60:61], v84 offset:8192\n\t" \
  "ds_read_b64_tr_b16 v[62:63], v84 offset:8448\n\t" \
  "v_mfma_f32_32x32x16_bf16 v[0:15], v[68:71], v[64:67], v[0:15]\n\t" \
  "v_mfma_f32_32x32x16_bf16 v[16:31], v[72:75], v[64:67], v[16:31]\n\t" \
  "v_mfma_f32_32x32x16_bf16 v[32:47], v[64:67], v[76:79], v[32:47]\n\t" \
  "s_branch 1b\n\t" \
  "9:\n\t" \
  "s_waitcnt lgkmcnt(0)\n\t" \
  "v_mfma_f32_32x32x16_bf16 v[0:15], v[68:71], v[64:67], v[0:15]\n\t" \
  "v_mfma_f32_32x32x16_bf16 v[16:31], v[72:75], v[64:67], v[16:31]\n\t" \
  "v_mfma_f32_32x32x16_bf16 v[32:47], v[64:67], v[76:79], v[32:47]\n\t" \
  "s_nop 15\n\t" \
  ""

constexpr int K1_ST = 28672, K1_SIDE = 4 * K1_ST;
__global__ __launch_bounds__(768) void k1(const u16* __restrict__ x, const u16* __restrict__ wqkv,
                                          const u16* __restrict__ wo, const u16* __restrict__ w1,
                                          const u16* __restrict__ w2, const u16* __restrict__ gw,
                                          char* __restrict__ ws) {
  __shared__ __attribute__((aligned(16))) char lds[K1_SIDE + 4 * 6144 + 4096];
  const int bid = blockIdx.x, xcd = bid & 7, idx = bid >> 3;
  const int rb = (xcd & 1) * 8 + (idx & 7), j = (xcd >> 1) * 4 + (idx >> 3);
  const int tid = threadIdx.x, lane = tid & 63;
  const int w = __builtin_amdgcn_readfirstlane(tid >> 6);
  const int r = lane & 31, h = lane >> 5, m = w & 3;
  const unsigned lb = (unsigned)(size_t)(LAS char*)lds;
  const int tile = rb * 4 + m;
  const int q4 = (lane & 15) >> 2, p4 = lane & 3, grp = (lane >> 4) & 1;
  const int sw = (r >> 1) & 7;
  const auto rs = WS_RSRC(ws);
  const int u0 = (bid * 4 + m) * 2;
  const int tok = rb * 128 + j * 8 + 2 * m;
  const int kq0 = KSTAG;
  if (w >= 8) {
    __builtin_amdgcn_s_sleep(SIDE_DELAY);
    const unsigned sl = lb + K1_SIDE + m * 6144;
#pragma unroll
    for (int c = 0; c < 2; ++c) {
      const int u = min(u0 + c, 1647);
      const bool is1 = u < 704;
      const int uu = is1 ? u : u - 704;
      const int kb = is1 ? uu / 22 : uu >> 3, pr = is1 ? uu % 22 : uu & 7;
      const int ld = is1 ? F_ : D_;
      const u16* src = is1 ? w1 + (size_t)kb * 16 * F_ : (kb < 86 ? w2 + (size_t)kb * 16 * D_ : wo + (size_t)(kb - 86) * 16 * D_);
      const unsigned po = (unsigned)((lane >> 3) * ld + min(64 * pr + (lane & 7) * 8, ld - 8)) * 2u;
      gldsa16(src, po, sl + c * 2048);
      gldsa16(src + 8 * ld, po, sl + c * 2048 + 1024);
    }
    gldsa16(x + (size_t)tok * D_, lane * 16, sl + 4096);
    gldsa16(x + (size_t)tok * D_, lane * 16 + 1024, sl + 5120);
    gldsa16(gw, lane * 16, lb + K1_SIDE + 24576 + m * 1024);
    if (bid == 0 && w == 8 && lane < 32) ((unsigned*)(ws + WS_CNT))[lane * 32] = 0;
    const int sstar = ((j >> 1) - kq0) & 7;
    const char* xsrc = lds + (sstar & 3) * K1_ST + m * 4096 + r * 128;
    const char* sp = lds + K1_SIDE + m * 6144;
    int job = -1;
#pragma unroll 1
    for (int s = 0; s < 8 || job < 3; ++s) {
      if (s < 8) {
        asm volatile("s_waitcnt lgkmcnt(0)" ::: "memory");
        BAR();
      } else if (job < 0) {
        WAIT_V(0);
        job = 0;
      }
      if (job < 0) {
        const unsigned ib = __builtin_amdgcn_s_getreg(7 | (31 << 11));
        if (((ib & 15u) | ((ib >> 18) & 48u)) == 0) { job = 0; asm volatile("" ::: "memory"); }
      }
      if (s == sstar) {
        const u32x4 xc0 = *(const u32x4*)(xsrc + (((4 * (j & 1) + h) ^ sw) * 16));
        const u32x4 xc1 = *(const u32x4*)(xsrc + (((4 * (j & 1) + 2 + h) ^ sw) * 16));
        WST16(rs, WS_XF + ((4 * (j >> 1) + 2 * (j & 1)) * 64 + tile) * 1024 + lane * 16, xc0);
        WST16(rs, WS_XF + ((4 * (j >> 1) + 2 * (j & 1) + 1) * 64 + tile) * 1024 + lane * 16, xc1);
      }
      if (job == 0 || job == 1) {
        const int c = job, u = u0 + c;
        if (u < 1648) {
          const bool is1 = u < 704;
          const int uu = is1 ? u : u - 704;
          const int kb = is1 ? uu / 22 : uu >> 3, pr = is1 ? uu % 22 : uu & 7;
          const char* pp = sp + c * 2048 + ((is1 ? 8 : 4) * h + q4) * 128 + grp * 32 + p4 * 8;
          const int hi_off = is1 ? 512 : 1024;
#pragma unroll
          for (int n = 0; n < 2; ++n) {
            const s16x4 lo = ldtr(pp + n * 64), hi = ldtr(pp + n * 64 + hi_off);
            const bf16x8 f = __builtin_shufflevector(lo, hi, 0, 1, 2, 3, 4, 5, 6, 7);
            const int blk = 2 * pr + n;
            const unsigned dst = is1 ? WS_W1P + (kb * 43 + blk) * 1024
                                     : (kb < 86 ? WS_W2P + (kb * 16 + blk) * 1024 : WS_WOP + ((kb - 86) * 16 + blk) * 1024);
            if (!is1 || blk < 43) WST16(rs, dst + lane * 16, f);
          }
        }
      }
      if (s == SENT_AT) {
        const u32x4 ones = {0xFFFFFFFFu, 0xFFFFFFFFu, 0xFFFFFFFFu, 0xFFFFFFFFu};
        WST16(rs, WS_O + (bid * 8 + 2 * m) * 1024 + lane * 16, ones);
        WST16(rs, WS_O + (bid * 8 + 2 * m + 1) * 1024 + lane * 16, ones);
      }
      if (job == 2) {
        const u32x4 gg = *(const u32x4*)(lds + K1_SIDE + 24576 + m * 1024 + lane * 16);
#pragma unroll
        for (int t = 0; t < 2; ++t) {
          const u32x4 gx = *(const u32x4*)(sp + 4096 + t * 1024 + lane * 16);
          float sm = 0.f;
#pragma unroll
          for (int e = 0; e < 4; ++e) {
            sm += rbf(bf_lo(gx[e]) * bf_lo(gg[e]));
            sm += rbf(bf_hi(gx[e]) * bf_hi(gg[e]));
          }
          sm = row16_sum(sm);
          sm = (rdl(sm, 0) + rdl(sm, 16)) + (rdl(sm, 32) + rdl(sm, 48));
          sm = rbf(sm);
          const float g = rbf(__builtin_amdgcn_rcpf(1.f + __builtin_amdgcn_exp2f(-sm * 1.4426950408889634f)));
          if (lane == 0) ((float*)(ws + WS_GATE))[tok + t] = g;
        }
      }
      if (job >= 0) ++job;
    }
    return;
  }
  if (w >= 4) {
    const unsigned xo0 = (unsigned)((lane >> 3) * D_ + (((lane & 7) ^ ((lane >> 4) & 7)) * 8)) * 2u;
    const unsigned xo1 = (unsigned)((8 + (lane >> 3)) * D_ + (((lane & 7) ^ ((4 + (lane >> 4)) & 7)) * 8)) * 2u;
    const unsigned wqo = (unsigned)((lane >> 2) * 1536 + (lane & 3) * 8) * 2u;
    int kq = kq0;
    const char* xb = (const char*)(x + (size_t)(rb * 128 + 32 * m) * D_ + 64 * kq);
    const char* wqb = (const char*)(wqkv + (size_t)(16 * m + 64 * kq) * 1536 + 64 * j);
    const unsigned vo_ = wqo + 2048 - 64 * j;
    unsigned xl = lb + m * 4096, wl = lb + 16384 + m * 1024;
#define STAGE()                                                                                   \
  do {                                                                                            \
    gldsa16(wqb, wqo, wl);                                                                        \
    gldsa16(wqb, wqo + 64, wl + 4096);                                                            \
    gldsa16(wqb, vo_, wl + 8192);                                                                 \
    gldsa16(xb, xo0, xl);                                                                         \
    gldsa16(xb, xo1, xl + 1024);                                                                  \
    gldsa16(xb + 16 * D_ * 2, xo0, xl + 2048);                                                    \
    gldsa16(xb + 16 * D_ * 2, xo1, xl + 3072);                                                    \
    kq = (kq + 1) & 7;                                                                            \
    { const int dk_ = kq ? 128 : -896; xb += dk_; wqb += dk_ * 1536; }                            \
    const bool wrap_ = xl + K1_ST >= lb + 4 * K1_ST;                                              \
    xl += wrap_ ? 0u - 3u * K1_ST : K1_ST; wl += wrap_ ? 0u - 3u * K1_ST : K1_ST;                 \
  } while (0)
    STAGE();
    STAGE();
    WAIT_V(7); BAR();
    STAGE();
    WAIT_V(7); BAR();
    STAGE();
#pragma unroll 1
    for (int kt = 2; kt < 6; ++kt) {
      STAGE();
      WAIT_V(14); BAR();
    }
#undef STAGE
    WAIT_V(7); BAR();
    WAIT_V(0); BAR();
    return;
  }

  f32x16 acc[3];
#pragma unroll
  for (int b = 0; b < 3; ++b)
#pragma unroll
    for (int i = 0; i < 16; ++i) acc[b][i] = 0.f;
  {
    const unsigned xr0 = lb + m * 4096 + r * 128;
    unsigned xa0 = xr0 + ((0 + h) ^ sw) * 16, xa1 = xr0 + ((2 + h) ^ sw) * 16, xa2 = xr0 + ((4 + h) ^ sw) * 16,
             xa3 = xr0 + ((6 + h) ^ sw) * 16;
    unsigned wa = lb + 16384 + (8 * h + q4) * 64 + grp * 32 + p4 * 8;
    unsigned kt = 0, t0;
    asm volatile(K1_LOOP_ASM
                 : "+{v[0:15]}"(acc[0]), "+{v[16:31]}"(acc[1]), "+{v[32:47]}"(acc[2]), "+{v80}"(xa0),
                   "+{v81}"(xa1), "+{v82}"(xa2), "+{v83}"(xa3), "+{v84}"(wa), [kt] "+s"(kt), [t0] "=&s"(t0)
                 : [cst] "s"((unsigned)K1_ST), [cmw] "s"(0u - 3u * K1_ST)
                 : "memory", "scc", "v48", "v49", "v50", "v51", "v52", "v53", "v54", "v55", "v56", "v57",
                   "v58", "v59", "v60", "v61", "v62", "v63", "v64", "v65", "v66", "v67", "v68", "v69", "v70",
                   "v71", "v72", "v73", "v74", "v75", "v76", "v77", "v78", "v79");
  }
#pragma unroll
  for (int b = 0; b < 2; ++b) {
    const int c0 = 64 * j + 32 * b;
    const bool isq = c0 < 512;
    const float sc = isq ? 0.36067376022224085f : 1.0f;
    const int head0 = (c0 & 511) >> 4;
    const unsigned dst = isq ? WS_Q : WS_K;
#pragma unroll
    for (int hh = 0; hh < 2; ++hh) {
      u32x4 o;
#pragma unroll
      for (int e = 0; e < 4; ++e) o[e] = pk2(acc[b][8 * hh + 2 * e] * sc, acc[b][8 * hh + 2 * e + 1] * sc);
      WST16(rs, dst + (((head0 + hh) * 64 + tile) * 64 + lane) * 16, o);
      if (!isq) {
        float ss = 0.f;
#pragma unroll
        for (int e = 0; e < 4; ++e) {
          const float a = bf_lo(o[e]), c = bf_hi(o[e]);
          ss = __builtin_fmaf(a, a, ss);
          ss = __builtin_fmaf(c, c, ss);
        }
        ss = row16_max(add_half(ss));
        ss = fmaxf(rdl(ss, 0), rdl(ss, 16));
        if (lane == 0) ((float*)(ws + WS_KMAX))[(head0 + hh) * 64 + tile] = ss;
      }
    }
  }
  {
    const int head = 2 * j + (r >> 4), d = r & 15;
#pragma unroll
    for (int s = 0; s < 2; ++s) {
      u32x4 o;
#pragma unroll
      for (int e = 0; e < 4; ++e) o[e] = pk2(acc[2][8 * s + 2 * e], acc[2][8 * s + 2 * e + 1]);
      WST16(rs, WS_V + (head * 64 + tile) * 1024 + s * 512 + h * 256 + d * 16, o);
    }
  }
}

#define ATTN_LOOP_ASM \
  "1:\n\t" \
  "v_exp_f32 v32, v32\n\t" \
  "v_exp_f32 v33, v33\n\t" \
  "v_exp_f32 v34, v34\n\t" \
  "v_cvt_pk_bf16_f32 v88, v32, v33\n\t" \
  "v_mfma_f32_32x32x16_bf16 v[0:15], v[84:87], v[92:95], v[0:15]\n\t" \
  "s_mov_b64 exec, %[mask]\n\t" \
  "ds_read_b128 v[80:83], v97 offset:1024\n\t" \
  "ds_read_b128 v[84:87], v97 offset:1536\n\t" \
  "s_mov_b64 exec, -1\n\t" \
  "v_exp_f32 v35, v35\n\t" \
  "v_exp_f32 v36, v36\n\t" \
  "v_cvt_pk_bf16_f32 v89, v34, v35\n\t" \
  "v_exp_f32 v37, v37\n\t" \
  "v_exp_f32 v38, v38\n\t" \
  "v_cvt_pk_bf16_f32 v90, v36, v37\n\t" \
  "v_exp_f32 v39, v39\n\t" \
  "v_exp_f32 v40, v40\n\t" \
  "v_cvt_pk_bf16_f32 v91, v38, v39\n\t" \
  "v_exp_f32 v41, v41\n\t" \
  "v_exp_f32 v42, v42\n\t" \
  "v_mfma_f32_32x32x16_bf16 v[0:15], v[72:75], v[88:91], v[0:15]\n\t" \
  "v_cvt_pk_bf16_f32 v92, v40, v41\n\t" \
  "v_exp_f32 v43, v43\n\t" \
  "v_exp_f32 v44, v44\n\t" \
  "v_cvt_pk_bf16_f32 v93, v42, v43\n\t" \
  "v_exp_f32 v45, v45\n\t" \
  "v_exp_f32 v46, v46\n\t" \
  "v_cvt_pk_bf16_f32 v94, v44, v45\n\t" \
  "v_exp_f32 v47, v47\n\t" \
  "s_waitcnt lgkmcnt(0)\n\t" \
  "v_cvt_pk_bf16_f32 v95, v46, v47\n\t" \
  "v_mfma_f32_32x32x16_bf16 v[32:47], v[68:71], v[64:67], v[16:31]\n\t" \
  "ds_read_b128 v[68:71], v96 offset:3072\n\t" \
  "v_exp_f32 v48, v48\n\t" \
  "v_exp_f32 v49, v49\n\t" \
  "v_exp_f32 v50, v50\n\t" \
  "v_cvt_pk_bf16_f32 v88, v48, v49\n\t" \
  "v_mfma_f32_32x32x16_bf16 v[0:15], v[76:79], v[92:95], v[0:15]\n\t" \
  "s_mov_b64 exec, %[mask]\n\t" \
  "ds_read_b128 v[72:75], v97 offset:2048\n\t" \
  "ds_read_b128 v[76:79], v97 offset:2560\n\t" \
  "s_mov_b64 exec, -1\n\t" \
  "v_exp_f32 v51, v51\n\t" \
  "v_exp_f32 v52, v52\n\t" \
  "v_cvt_pk_bf16_f32 v89, v50, v51\n\t" \
  "v_exp_f32 v53, v53\n\t" \
  "v_exp_f32 v54, v54\n\t" \
  "v_cvt_pk_bf16_f32 v90, v52, v53\n\t" \
  "v_exp_f32 v55, v55\n\t" \
  "v_exp_f32 v56, v56\n\t" \
  "v_cvt_pk_bf16_f32 v91, v54, v55\n\t" \
  "v_exp_f32 v57, v57\n\t" \
  "v_exp_f32 v58, v58\n\t" \
  "v_mfma_f32_32x32x16_bf16 v[0:15], v[80:83], v[88:91], v[0:15]\n\t" \
  "v_cvt_pk_bf16_f32 v92, v56, v57\n\t" \
  "v_exp_f32 v59, v59\n\t" \
  "v_exp_f32 v60, v60\n\t" \
  "v_cvt_pk_bf16_f32 v93, v58, v59\n\t" \
  "v_exp_f32 v61, v61\n\t" \
  "v_exp_f32 v62, v62\n\t" \
  "v_cvt_pk_bf16_f32 v94, v60, v61\n\t" \
  "v_exp_f32 v63, v63\n\t" \
  "s_waitcnt lgkmcnt(0)\n\t" \
  "v_cvt_pk_bf16_f32 v95, v62, v63\n\t" \
  "v_mfma_f32_32x32x16_bf16 v[48:63], v[68:71], v[64:67], v[16:31]\n\t" \
  "ds_read_b128 v[68:71], v96 offset:4096\n\t" \
  "v_exp_f32 v32, v32\n\t" \
  "v_exp_f32 v33, v33\n\t" \
  "v_exp_f32 v34, v34\n\t" \
  "v_cvt_pk_bf16_f32 v88, v32, v33\n\t" \
  "v_mfma_f32_32x32x16_bf16 v[0:15], v[84:87], v[92:95], v[0:15]\n\t" \
  "s_mov_b64 exec, %[mask]\n\t" \
  "ds_read_b128 v[80:83], v97 offset:3072\n\t" \
  "ds_read_b128 v[84:87], v97 offset:3584\n\t" \
  "s_mov_b64 exec, -1\n\t" \
  "v_exp_f32 v35, v35\n\t" \
  "v_exp_f32 v36, v36\n\t" \
  "v_cvt_pk_bf16_f32 v89, v34, v35\n\t" \
  "v_exp_f32 v37, v37\n\t" \
  "v_exp_f32 v38, v38\n\t" \
  "v_cvt_pk_bf16_f32 v90, v36, v37\n\t" \
  "v_exp_f32 v39, v39\n\t" \
  "v_exp_f32 v40, v40\n\t" \
  "v_cvt_pk_bf16_f32 v91, v38, v39\n\t" \
  "v_exp_f32 v41, v41\n\t" \
  "v_exp_f32 v42, v42\n\t" \
  "v_mfma_f32_32x32x16_bf16 v[0:15], v[72:75], v[88:91], v[0:15]\n\t" \
  "v_cvt_pk_bf16_f32 v92, v40, v41\n\t" \
  "v_exp_f32 v43, v43\n\t" \
  "v_exp_f32 v44, v44\n\t" \
  "v_cvt_pk_bf16_f32 v93, v42, v43\n\t" \
  "v_exp_f32 v45, v45\n\t" \
  "v_exp_f32 v46, v46\n\t" \
  "v_cvt_pk_bf16_f32 v94, v44, v45\n\t" \
  "v_exp_f32 v47, v47\n\t" \
  "s_waitcnt lgkmcnt(0)\n\t" \
  "v_cvt_pk_bf16_f32 v95, v46, v47\n\t" \
  "v_mfma_f32_32x32x16_bf16 v[32:47], v[68:71], v[64:67], v[16:31]\n\t" \
  "ds_read_b128 v[68:71], v96 offset:5120\n\t" \
  "v_exp_f32 v48, v48\n\t" \
  "v_exp_f32 v49, v49\n\t" \
  "v_exp_f32 v50, v50\n\t" \
  "v_cvt_pk_bf16_f32 v88, v48, v49\n\t" \
  "v_mfma_f32_32x32x16_bf16 v[0:15], v[76:79], v[92:95], v[0:15]\n\t" \
  "s_mov_b64 exec, %[mask]\n\t" \
  "ds_read_b128 v[72:75], v97 offset:4096\n\t" \
  "ds_read_b128 v[76:79], v97 offset:4608\n\t" \
  "s_mov_b64 exec, -1\n\t" \
  "v_exp_f32 v51, v51\n\t" \
  "v_exp_f32 v52, v52\n\t" \
  "v_cvt_pk_bf16_f32 v89, v50, v51\n\t" \
  "v_exp_f32 v53, v53\n\t" \
  "v_exp_f32 v54, v54\n\t" \
  "v_cvt_pk_bf16_f32 v90, v52, v53\n\t" \
  "v_exp_f32 v55, v55\n\t" \
  "v_exp_f32 v56, v56\n\t" \
  "v_cvt_pk_bf16_f32 v91, v54, v55\n\t" \
  "v_exp_f32 v57, v57\n\t" \
  "v_exp_f32 v58, v58\n\t" \
  "v_mfma_f32_32x32x16_bf16 v[0:15], v[80:83], v[88:91], v[0:15]\n\t" \
  "v_cvt_pk_bf16_f32 v92, v56, v57\n\t" \
  "v_exp_f32 v59, v59\n\t" \
  "v_exp_f32 v60, v60\n\t" \
  "v_cvt_pk_bf16_f32 v93, v58, v59\n\t" \
  "v_exp_f32 v61, v61\n\t" \
  "v_exp_f32 v62, v62\n\t" \
  "v_cvt_pk_bf16_f32 v94, v60, v61\n\t" \
  "v_exp_f32 v63, v63\n\t" \
  "s_waitcnt lgkmcnt(0)\n\t" \
  "v_cvt_pk_bf16_f32 v95, v62, v63\n\t" \
  "v_mfma_f32_32x32x16_bf16 v[48:63], v[68:71], v[64:67], v[16:31]\n\t" \
  "ds_read_b128 v[68:71], v96 offset:6144\n\t" \
  "v_add_u32 v96, 0x1000, v96\n\t" \
  "v_add_u32 v97, 0x1000, v97\n\t" \
  "s_sub_u32 %[cnt], %[cnt], 1\n\t" \
  "s_and_b32 %[tmp], %[cnt], 13\n\t" \
  "s_cmp_lg_u32 %[tmp], 12\n\t" \
  "s_cbranch_scc1 2f\n\t" \
  "3:\n\t" \
  "ds_read_b32 v98, v99\n\t" \
  "s_waitcnt lgkmcnt(0)\n\t" \
  "v_readfirstlane_b32 %[tmp], v98\n\t" \
  "s_cmp_ge_u32 %[tmp], 4\n\t" \
  "s_cbranch_scc1 4f\n\t" \
  "s_sleep 1\n\t" \
  "s_branch 3b\n\t" \
  "4:\n\t" \
  "v_add_u32 v99, 4, v99\n\t" \
  "2:\n\t" \
  "s_cmp_lg_u32 %[cnt], 7\n\t" \
  "s_cbranch_scc1 5f\n\t" \
  "v_mov_b32 v98, 1\n\t" \
  "s_mov_b64 exec, 1\n\t" \
  "ds_add_u32 v99, v98\n\t" \
  "s_mov_b64 exec, -1\n\t" \
  "5:\n\t" \
  "s_cmp_lg_u32 %[cnt], 0\n\t" \
  "s_cbranch_scc1 1b\n\t" \
  "s_waitcnt lgkmcnt(0)\n\t" \
  "s_nop 15\n\t" \
  ""

constexpr int L2_FLAG = 131072;
__global__ __launch_bounds__(768) void k2(char* __restrict__ ws, const u16* __restrict__ b1, u16* __restrict__ out) {
  __shared__ __attribute__((aligned(16))) char lds[L2_FLAG + 4096];
  const int bid = blockIdx.x, xcd = bid & 7, idx = bid >> 3;
  const int head = xcd * 4 + (idx & 3), qb = idx >> 2;
  const int tp = xcd >> 1, cp = (xcd & 1) * 4 + (idx & 3);
  const int T0 = qb * 8 + 2 * tp;
  const int tid = threadIdx.x, lane = tid & 63;
  const int w = __builtin_amdgcn_readfirstlane(tid >> 6);
  const int r = lane & 31, h = lane >> 5;
  const unsigned lb = (unsigned)(size_t)(LAS char*)lds;
  const unsigned l16 = lane * 16;
  const auto rs = WS_RSRC(ws);
  char* cntF = ws + WS_CNT + (qb * 4 + tp) * 128;
  if (w >= 8) {
    const int L = w - 8;
    {
      const char* ksrc = ws + WS_K + (size_t)(head * 64 + L) * 1024;
      const char* vsrc = ws + WS_V + (size_t)(head * 64 + L) * 1024;
      const unsigned kdst = lb + L * 1024, vdst = lb + 65536 + L * 1024;
#pragma unroll
      for (int c = 0; c < 3; ++c) { gldsa16(ksrc + c * 4096, l16, kdst + c * 4096); gldsa16(vsrc + c * 4096, l16, vdst + c * 4096); }
      if (tid >= 512 && tid < 520) ((volatile LAS unsigned*)(lds + L2_FLAG))[tid - 512] = 0;
      WAIT_V(0); BAR();
#pragma unroll 1
      for (int c = 3; c < 16; ++c) {
        gldsa16(ksrc + c * 4096, l16, kdst + c * 4096); gldsa16(vsrc + c * 4096, l16, vdst + c * 4096);
      }
    }
    {
      const int T = T0 + (L & 1);
      const int f0 = cp + 24 * (L >> 1);
      const int fbc2 = min(f0 + 16, 42);
      const auto rx = __builtin_amdgcn_make_buffer_rsrc((void*)(ws + WS_XF + T * 1024), 0, 31 * 65536 + 1024, 0x00020000);
      const auto rw = __builtin_amdgcn_make_buffer_rsrc((void*)(ws + WS_W1P), 0, 32 * 43 * 1024, 0x00020000);
      const unsigned wv0 = l16 + f0 * 1024, wv1 = l16 + (f0 + 8) * 1024, wv2 = l16 + fbc2 * 1024;
      constexpr int PD = 6;
      u32x4 fx[PD], fw[PD][3];
#pragma unroll
      for (int i = 0; i < PD; ++i) {
        fx[i] = BLD(rx, l16, i * 65536, 0);
        fw[i][0] = BLD(rw, wv0, i * 44032, 0); fw[i][1] = BLD(rw, wv1, i * 44032, 0); fw[i][2] = BLD(rw, wv2, i * 44032, 0);
      }
      WAIT_V(38);
      add_lds(lds + L2_FLAG, lane);
      WAIT_V(24);
      add_lds(lds + L2_FLAG + 4, lane);
      __builtin_amdgcn_s_setprio(FFN_PRIO);
      f32x16 a[3];
#pragma unroll
      for (int g = 0; g < 4; ++g) {
        const u32x2 b0 = *(const u32x2*)(b1 + 32 * f0 + 8 * g + 4 * h);
        const u32x2 b1_ = *(const u32x2*)(b1 + 32 * (f0 + 8) + 8 * g + 4 * h);
        const u32x2 b2 = *(const u32x2*)(b1 + 32 * fbc2 + 8 * g + 4 * h);
        a[0][4 * g] = bf_lo(b0[0]); a[0][4 * g + 1] = bf_hi(b0[0]); a[0][4 * g + 2] = bf_lo(b0[1]); a[0][4 * g + 3] = bf_hi(b0[1]);
        a[1][4 * g] = bf_lo(b1_[0]); a[1][4 * g + 1] = bf_hi(b1_[0]); a[1][4 * g + 2] = bf_lo(b1_[1]); a[1][4 * g + 3] = bf_hi(b1_[1]);
        a[2][4 * g] = bf_lo(b2[0]); a[2][4 * g + 1] = bf_hi(b2[0]); a[2][4 * g + 2] = bf_lo(b2[1]); a[2][4 * g + 3] = bf_hi(b2[1]);
      }
      __builtin_amdgcn_sched_barrier(0);
#pragma unroll 1
      for (int k0 = PD; k0 < 30 + PD; k0 += PD) {
#pragma unroll
        for (int i = 0; i < PD; ++i) {
          a[0] = MFMAU(fw[i][0], fx[i], a[0]);
          a[1] = MFMAU(fw[i][1], fx[i], a[1]);
          a[2] = MFMAU(fw[i][2], fx[i], a[2]);
          fx[i] = BLD(rx, l16, (k0 + i) * 65536, 0);
          fw[i][0] = BLD(rw, wv0, (k0 + i) * 44032, 0); fw[i][1] = BLD(rw, wv1, (k0 + i) * 44032, 0);
          fw[i][2] = BLD(rw, wv2, (k0 + i) * 44032, 0);
          __builtin_amdgcn_sched_barrier(0);
        }
      }
#pragma unroll
      for (int i = 0; i < 2; ++i) {
        a[0] = MFMAU(fw[i][0], fx[i], a[0]);
        a[1] = MFMAU(fw[i][1], fx[i], a[1]);
        a[2] = MFMAU(fw[i][2], fx[i], a[2]);
      }
      const float g1 = rbf(1.f - ((const float*)(ws + WS_GATE))[T * 32 + r]);
      __builtin_amdgcn_s_setprio(MISH_PRIO);
#pragma unroll
      for (int b = 0; b < 3; ++b) {
        if (f0 + 8 * b < 43) {
#pragma unroll
          for (int hh = 0; hh < 2; ++hh) {
            u32x4 o;
#pragma unroll
            for (int gq = 0; gq < 2; ++gq) {
              const int g = 2 * hh + gq;
              const float h0 = a[b][4 * g + 0], h1 = a[b][4 * g + 1], h2 = a[b][4 * g + 2], h3 = a[b][4 * g + 3];
              o[2 * gq] = pk2(mish_g(h0, g1), mish_g(h1, g1));
              o[2 * gq + 1] = pk2(mish_g(h2, g1), mish_g(h3, g1));
            }
            WST16(rs, WS_ACT + ((2 * (f0 + 8 * b) + hh) * 64 + T) * 1024 + l16, o);
          }
        }
      }
      __builtin_amdgcn_s_setprio(FFN2_PRIO);
      WAIT_V(0);
      if (add_lds(lds + L2_FLAG + 12, lane) == 3 && lane == 0) __hip_atomic_fetch_add((GAS unsigned*)cntF, 1u, RLX_AGENT);
    }
    {
      const int klast = min(22 * L + 21, 85);
      const auto ra = __builtin_amdgcn_make_buffer_rsrc((void*)(ws + WS_ACT + T0 * 1024), 0, klast * 65536 + 2048, 0x00020000);
      const auto rw = __builtin_amdgcn_make_buffer_rsrc((void*)(ws + WS_W2P + cp * 2048), 0, klast * 16384 + 2048, 0x00020000);
      constexpr int PD = 5;
      u32x4 fa[PD][2], fw[PD][2];
      const int kb0 = 22 * L;
      wait_global(cntF, 8);
#pragma unroll
      for (int i = 0; i < PD; ++i) {
        fa[i][0] = BLD(ra, l16, (kb0 + i) * 65536, HO_AUX); fa[i][1] = BLD(ra, l16 + 1024, (kb0 + i) * 65536, HO_AUX);
        fw[i][0] = BLD(rw, l16, (kb0 + i) * 16384, 0); fw[i][1] = BLD(rw, l16 + 1024, (kb0 + i) * 16384, 0);
        __builtin_amdgcn_sched_barrier(0);
      }
      f32x16 b[4];
#pragma unroll
      for (int t = 0; t < 4; ++t)
#pragma unroll
        for (int i = 0; i < 16; ++i) b[t][i] = 0.f;
      __builtin_amdgcn_sched_barrier(0);
#pragma unroll 1
      for (int k0 = PD; k0 < 20 + PD; k0 += PD) {
#pragma unroll
        for (int i = 0; i < PD; ++i) {
#pragma unroll
          for (int t = 0; t < 4; ++t) b[t] = MFMAU(fw[i][t >> 1], fa[i][t & 1], b[t]);
          const int kn = kb0 + k0 + i;
          fa[i][0] = BLD(ra, l16, kn * 65536, HO_AUX); fa[i][1] = BLD(ra, l16 + 1024, kn * 65536, HO_AUX);
          fw[i][0] = BLD(rw, l16, kn * 16384, 0); fw[i][1] = BLD(rw, l16 + 1024, kn * 16384, 0);
          __builtin_amdgcn_sched_barrier(0);
        }
      }
#pragma unroll
      for (int i = 0; i < 2; ++i)
#pragma unroll
        for (int t = 0; t < 4; ++t) b[t] = MFMAU(fw[i][t >> 1], fa[i][t & 1], b[t]);
      wait_lds(lds + L2_FLAG + 8, 8);
      {
        const char* wsrc = ws + WS_WOP + cp * 2048 + (size_t)L * 8 * 16384;
        const unsigned wdst = lb + (L >> 1) * 65536 + (L & 1) * 16384;
#pragma unroll 1
        for (int i = 0; i < 8; ++i) {
          gldsa16(wsrc + i * 16384, l16, wdst + i * 2048);
          gldsa16(wsrc + i * 16384, l16 + 1024, wdst + i * 2048 + 1024);
        }
      }
      WAIT_V(0);
      BAR();
      char* bs = lds + (L >> 1) * 65536 + 32768 + (L & 1) * 16384 + l16;
#pragma unroll
      for (int t = 0; t < 4; ++t)
#pragma unroll
        for (int q = 0; q < 4; ++q) {
          f32x4 v = {b[t][4 * q], b[t][4 * q + 1], b[t][4 * q + 2], b[t][4 * q + 3]};
          *(f32x4*)(bs + (t & 1) * 8192 + (t >> 1) * 4096 + q * 1024) = v;
        }
      WAIT_V(0);
      BAR();
      BAR();
    }
    return;
  }

  const int qt = qb * 8 + w;
  bf16x8 qf = *(const bf16x8*)(ws + WS_Q + (size_t)(head * 64 + qt) * 1024 + l16);
  float km = ((const float*)(ws + WS_KMAX))[head * 64 + lane];
  const float gq = ((const float*)(ws + WS_GATE))[qt * 32 + r];
  km = row16_max(km);
  km = fmaxf(fmaxf(rdl(km, 0), rdl(km, 16)), fmaxf(rdl(km, 32), rdl(km, 48)));
  float qs = 0.f;
  {
    const u32x4 qu = __builtin_bit_cast(u32x4, qf);
#pragma unroll
    for (int e = 0; e < 4; ++e) {
      const float a = bf_lo(qu[e]), c = bf_hi(qu[e]);
      qs = __builtin_fmaf(a, a, qs);
      qs = __builtin_fmaf(c, c, qs);
    }
    qs = add_half(qs);
  }
  const float mb = __builtin_amdgcn_sqrtf(qs * km) * 1.0001f;
  f32x16 negm, O;
#pragma unroll
  for (int i = 0; i < 16; ++i) { negm[i] = -mb; O[i] = 0.f; }

  const char* kp = lds + lane * 16;
  const char* vp = lds + 65536 + h * 256 + (lane & 15) * 16;
  const bool vact = (lane & 16) == 0;
  const short one = ((lane & 27) == 16) ? (short)0x3F80 : (short)0;
  bf16x8 va0, va1, vb0, vb1;
#pragma unroll
  for (int i = 0; i < 8; ++i) { va0[i] = one; va1[i] = one; vb0[i] = one; vb1[i] = one; }
  u32x4 p0 = {0, 0, 0, 0}, p1 = {0, 0, 0, 0};
  unsigned kaddr = lb + l16, vaddr = lb + 65536 + h * 256 + (lane & 15) * 16;
  const unsigned long long vmask = 0x0000FFFF0000FFFFull;

  BAR();
  f32x16 Sa = MFMA(*(const bf16x8*)(kp), qf, negm);
  f32x16 Sb = MFMA(*(const bf16x8*)(kp + 1024), qf, negm);
  bf16x8 kf = *(const bf16x8*)(kp + 2048);
  if (vact) { va0 = *(const bf16x8*)(vp); va1 = *(const bf16x8*)(vp + 512); }
  {
    unsigned cnt = 16, tmp, fa_ = lb + L2_FLAG;
    asm volatile("s_nop 15\n\t" ATTN_LOOP_ASM
                 : "+{v[0:15]}"(O), "+{v[32:47]}"(Sa), "+{v[48:63]}"(Sb), "+{v[68:71]}"(kf),
                   "+{v[72:75]}"(va0), "+{v[76:79]}"(va1), "+{v[80:83]}"(vb0), "+{v[84:87]}"(vb1),
                   "+{v[88:91]}"(p0), "+{v[92:95]}"(p1), "+{v96}"(kaddr), "+{v97}"(vaddr), [cnt] "+s"(cnt),
                   [tmp] "=&s"(tmp), "+{v99}"(fa_)
                 : "{v[16:31]}"(negm), "{v[64:67]}"(qf), [mask] "s"(vmask)
                 : "memory", "scc", "v98");
  }
  O = MFMA(vb1, __builtin_bit_cast(bf16x8, p1), O);

  float l = O[8];
  if (__any(!(l >= 8.271806125530277e-25f))) {
    float m = -INFINITY;
    f32x16 zero;
#pragma unroll
    for (int i = 0; i < 16; ++i) { zero[i] = 0.f; O[i] = 0.f; }
    const char* kg = ws + WS_K + (size_t)head * 65536 + l16;
    const char* vg = ws + WS_V + (size_t)head * 65536 + h * 256 + (lane & 15) * 16;
#pragma unroll 1
    for (int t = 0; t < 64; ++t) {
      f32x16 Sx = MFMA(*(const bf16x8*)(kg + t * 1024), qf, zero);
      float mx = Sx[0];
#pragma unroll
      for (int i = 1; i < 16; ++i) mx = fmaxf(mx, Sx[i]);
      mx = max_half(mx);
      const float mn = fmaxf(m, mx);
      const float alpha = __builtin_amdgcn_exp2f(m - mn);
      m = mn;
#pragma unroll
      for (int i = 0; i < 16; ++i) { O[i] *= alpha; Sx[i] -= mn; }
      if (vact) { va0 = *(const bf16x8*)(vg + t * 1024); va1 = *(const bf16x8*)(vg + t * 1024 + 512); }
#pragma unroll
      for (int e = 0; e < 4; ++e) {
        p0[e] = pk2(__builtin_amdgcn_exp2f(Sx[2 * e]), __builtin_amdgcn_exp2f(Sx[2 * e + 1]));
        p1[e] = pk2(__builtin_amdgcn_exp2f(Sx[8 + 2 * e]), __builtin_amdgcn_exp2f(Sx[8 + 2 * e + 1]));
      }
      O = MFMAU(va0, p0, O);
      O = MFMAU(va1, p1, O);
    }
    l = O[8];
  }
  {
    const float rl = gq / l;
    u32x4 o;
#pragma unroll
    for (int e = 0; e < 4; ++e) o[e] = pk2(O[2 * e] * rl, O[2 * e + 1] * rl);
    WST16(rs, WS_O + (head * 64 + qt) * 1024 + l16, o);
  }
  BAR();
  {
    const int kq = w >> 1;
    u32x4 fo[8];
#pragma unroll
    for (int i = 0; i < 8; ++i) fo[i] = BLD(rs, l16, WS_O + ((kq * 8 + i) * 64 + T0 + (w & 1)) * 1024, HO_AUX);
    const char* wp = lds + (kq >> 1) * 65536 + (kq & 1) * 16384 + l16;
    char* pp = lds + (kq >> 1) * 65536 + 32768 + (kq & 1) * 16384 + (w & 1) * 8192 + l16;
    BAR();
#pragma unroll 1
    for (unsigned spins = 0; spins < (1u << 20); ++spins) {
      bool ok = true;
#pragma unroll
      for (int i = 0; i < 8; ++i) {
        const unsigned mx = max(max(fo[i][0], fo[i][1]), max(fo[i][2], fo[i][3]));
        if (__any(mx == 0xFFFFFFFFu)) {
          fo[i] = BLD(rs, l16, WS_O + ((kq * 8 + i) * 64 + T0 + (w & 1)) * 1024, HO_AUX);
          ok = false;
        }
      }
      if (ok) break;
    }
    f32x16 a0, a1;
#pragma unroll
    for (int q = 0; q < 4; ++q) {
      const f32x4 v0 = *(const f32x4*)(pp + q * 1024), v1 = *(const f32x4*)(pp + 4096 + q * 1024);
#pragma unroll
      for (int e = 0; e < 4; ++e) { a0[4 * q + e] = v0[e]; a1[4 * q + e] = v1[e]; }
    }
#pragma unroll
    for (int i = 0; i < 8; ++i) {
      a0 = MFMAU(*(const u32x4*)(wp + i * 2048), fo[i], a0);
      a1 = MFMAU(*(const u32x4*)(wp + i * 2048 + 1024), fo[i], a1);
    }
#pragma unroll
    for (int q = 0; q < 4; ++q) {
      f32x4 v0 = {a0[4 * q], a0[4 * q + 1], a0[4 * q + 2], a0[4 * q + 3]};
      f32x4 v1 = {a1[4 * q], a1[4 * q + 1], a1[4 * q + 2], a1[4 * q + 3]};
      *(f32x4*)(pp + q * 1024) = v0;
      *(f32x4*)(pp + 4096 + q * 1024) = v1;
    }
  }
  WAIT_V(0);
  BAR();
  {
    const int t = w & 3, tt = t & 1, n = t >> 1;
    const int tok = (T0 + tt) * 32 + r;
#pragma unroll
    for (int e = 0; e < 2; ++e) {
      const int q = 2 * (w >> 2) + e;
      const char* sp = lds + 32768 + tt * 8192 + n * 4096 + q * 1024 + l16;
      const f32x4 s = (*(const f32x4*)(sp) + *(const f32x4*)(sp + 16384)) +
                      (*(const f32x4*)(sp + 65536) + *(const f32x4*)(sp + 65536 + 16384));
      u32x2 o = {pk2(s[0], s[1]), pk2(s[2], s[3])};
      *(u32x2*)(out + (size_t)tok * D_ + (2 * cp + n) * 32 + 8 * q + 4 * h) = o;
    }
  }
}

extern "C" void kernel_launch(void* const* d_in, const int* in_sizes, int n_in, void* d_out, int out_size,
                              void* d_ws, size_t ws_size, hipStream_t stream) {
  const u16* x = (const u16*)d_in[0];
  const u16* wqkv = (const u16*)d_in[1];
  const u16* wo = (const u16*)d_in[2];
  const u16* w1 = (const u16*)d_in[4];
  const u16* b1 = (const u16*)d_in[5];
  const u16* w2 = (const u16*)d_in[6];
  const u16* gw = (const u16*)d_in[7];
  char* ws = (char*)d_ws;
  k1<<<256, 768, 0, stream>>>(x, wqkv, wo, w1, w2, gw, ws);
  k2<<<256, 768, 0, stream>>>(ws, b1, (u16*)d_out);
}
```

```cpp
#include <hip/hip_runtime.h>
#include <hip/hip_bf16.h>
#include <stdint.h>

typedef short bf16x8 __attribute__((ext_vector_type(8)));
typedef short s16x4 __attribute__((ext_vector_type(4)));
typedef float f32x16 __attribute__((ext_vector_type(16)));
typedef float f32x4 __attribute__((ext_vector_type(4)));
typedef float f32x2 __attribute__((ext_vector_type(2)));
typedef __bf16 bf16x2_t __attribute__((ext_vector_type(2)));
typedef unsigned u32x4 __attribute__((ext_vector_type(4)));
typedef unsigned u32x2 __attribute__((ext_vector_type(2)));
typedef unsigned short u16;
#define DI __device__ __forceinline__
#define MFMA(a, b, c) __builtin_amdgcn_mfma_f32_32x32x16_bf16((a), (b), (c), 0, 0, 0)
#define MFMAU(a, b, c) MFMA(__builtin_bit_cast(bf16x8, a), __builtin_bit_cast(bf16x8, b), c)
#define LAS __attribute__((address_space(3)))
#define GAS __attribute__((address_space(1)))

#define KSTAG ((idx + (idx >> 3) * 2) & 7)
#define HO_AUX 16
#define FFN2_PRIO 0
#define SIDE_DELAY 8
#define SENT_AT 5
#define FFN1_PRIO 1
#define MISH_PRIO 1
#define FFN_PRIO 1
constexpr int S_ = 2048, D_ = 512, H_ = 32, F_ = 1376;
constexpr unsigned WS_Q = 0, WS_K = 2u << 20, WS_V = 4u << 20, WS_ACT = 6u << 20, WS_O = 12u << 20, WS_W2P = 14u << 20,
                   WS_WOP = 16u << 20, WS_GATE = 17u << 20, WS_KMAX = (17u << 20) + 16384, WS_CNT = (17u << 20) + 32768,
                   WS_W1P = 18u << 20, WS_XF = 20u << 20;

DI unsigned pk2(float a, float b) {
  f32x2 v = {a, b};
  bf16x2_t r = __builtin_convertvector(v, bf16x2_t);
  return __builtin_bit_cast(unsigned, r);
}
DI float bf_lo(unsigned u) { return __uint_as_float(u << 16); }
DI float bf_hi(unsigned u) { return __uint_as_float(u & 0xffff0000u); }
DI float rbf(float x) { return bf_lo(pk2(x, 0.f)); }
DI void gldsa16(const void* sbase, unsigned voff, unsigned dst) {
  unsigned keep;
  asm volatile("s_mov_b32 %0, m0\n\ts_mov_b32 m0, %3\n\ts_nop 0\n\tglobal_load_lds_dwordx4 %1, %2\n\ts_mov_b32 m0, %0"
               : "=&s"(keep) : "v"(voff), "s"(sbase), "s"(dst) : "memory");
}
DI s16x4 ldtr(const char* l) {
  typedef short v4i16_t __attribute__((ext_vector_type(4)));
  return __builtin_amdgcn_ds_read_tr16_b64_v4i16((LAS v4i16_t*)l);
}
#define WAIT_V(n) asm volatile("s_waitcnt vmcnt(" #n ") lgkmcnt(0)" ::: "memory")
#define BAR() do { asm volatile("" ::: "memory"); __builtin_amdgcn_s_barrier(); asm volatile("" ::: "memory"); } while (0)
template <int CTRL> DI float dppf(float v) {
  return __int_as_float(__builtin_amdgcn_update_dpp(0, __float_as_int(v), CTRL, 0xF, 0xF, true));
}
DI float rdl(float v, int l) { return __int_as_float(__builtin_amdgcn_readlane(__float_as_int(v), l)); }
DI float row16_max(float v) {
  v = fmaxf(v, dppf<0xB1>(v)); v = fmaxf(v, dppf<0x4E>(v)); v = fmaxf(v, dppf<0x141>(v)); v = fmaxf(v, dppf<0x140>(v));
  return v;
}
DI float row16_sum(float v) {
  v += dppf<0xB1>(v); v += dppf<0x4E>(v); v += dppf<0x141>(v); v += dppf<0x140>(v);
  return v;
}
DI float add_half(float v) {
  auto rr = __builtin_amdgcn_permlane32_swap(__float_as_uint(v), __float_as_uint(v), false, false);
  return __uint_as_float(rr[0]) + __uint_as_float(rr[1]);
}
DI float max_half(float v) {
  auto rr = __builtin_amdgcn_permlane32_swap(__float_as_uint(v), __float_as_uint(v), false, false);
  return fmaxf(__uint_as_float(rr[0]), __uint_as_float(rr[1]));
}
#define WS_RSRC(ws) __builtin_amdgcn_make_buffer_rsrc((void*)(ws), 0, 32u << 20, 0x00020000)
#define WST16(rs, off, v) __builtin_amdgcn_raw_buffer_store_b128(__builtin_bit_cast(u32x4, v), rs, (unsigned)(off), 0, 16)
#define BLD(rs, voff, soff, aux) __builtin_amdgcn_raw_buffer_load_b128(rs, voff, soff, aux)
#define RLX_AGENT __ATOMIC_RELAXED, __HIP_MEMORY_SCOPE_AGENT
#define RLX_WG __ATOMIC_RELAXED, __HIP_MEMORY_SCOPE_WORKGROUP

DI float mish_g(float h, float g) {
  const float w = __builtin_amdgcn_exp2f(h * 1.4426950408889634f);
  const float d = __builtin_fmaf(w, w + 2.f, 2.f);
  const float hg = h * g;
  return __builtin_fmaf(-2.f * hg, __builtin_amdgcn_rcpf(d), hg);
}
DI float mish_f(float h) {
  float t = fminf(h * 1.4426950408889634f, 60.f);
  float w = __builtin_amdgcn_exp2f(t);
  float a = w + 2.f;
  float u = w * a;
  float d = __builtin_fmaf(w, a, 2.f);
  return h * u * __builtin_amdgcn_rcpf(d);
}
DI void wait_global(char* p, unsigned want) {
  for (unsigned spins = 0; spins < (1u << 17); ++spins) {
    if ((unsigned)__builtin_amdgcn_readfirstlane(__hip_atomic_load((GAS unsigned*)p, RLX_AGENT)) >= want) break;
    __builtin_amdgcn_s_sleep(2);
  }
  asm volatile("" ::: "memory");
}
DI void wait_lds(char* p, unsigned want) {
  while ((unsigned)__builtin_amdgcn_readfirstlane(__hip_atomic_load((LAS unsigned*)p, RLX_WG)) < want)
    __builtin_amdgcn_s_sleep(1);
  asm volatile("" ::: "memory");
}
DI unsigned add_lds(char* p, int lane) {
  unsigned old = 0;
  if (lane == 0) old = __hip_atomic_fetch_add((LAS unsigned*)p, 1u, RLX_WG);
  return (unsigned)__builtin_amdgcn_readfirstlane(old);
}

#define K1_LOOP_ASM \
  "s_barrier\n\t" \
  "ds_read_b128 v[48:51], v80\n\t" \
  "ds_read_b64_tr_b16 v[52:53], v84 offset:0\n\t" \
  "ds_read_b64_tr_b16 v[54:55], v84 offset:256\n\t" \
  "ds_read_b64_tr_b16 v[56:57], v84 offset:4096\n\t" \
  "ds_read_b64_tr_b16 v[58:59], v84 offset:4352\n\t" \
  "ds_read_b64_tr_b16 v[60:61], v84 offset:8192\n\t" \
  "ds_read_b64_tr_b16 v[62:63], v84 offset:8448\n\t" \
  "1:\n\t" \
  "ds_read_b128 v[64:67], v81\n\t" \
  "ds_read_b64_tr_b16 v[68:69], v84 offset:1024\n\t" \
  "ds_read_b64_tr_b16 v[70:71], v84 offset:1280\n\t" \
  "ds_read_b64_tr_b16 v[72:73], v84 offset:5120\n\t" \
  "ds_read_b64_tr_b16 v[74:75], v84 offset:5376\n\t" \
  "ds_read_b64_tr_b16 v[76:77], v84 offset:9216\n\t" \
  "ds_read_b64_tr_b16 v[78:79], v84 offset:9472\n\t" \
  "s_waitcnt lgkmcnt(7)\n\t" \
  "v_mfma_f32_32x32x16_bf16 v[0:15], v[52:55], v[48:51], v[0:15]\n\t" \
  "v_mfma_f32_32x32x16_bf16 v[16:31], v[56:59], v[48:51], v[16:31]\n\t" \
  "v_mfma_f32_32x32x16_bf16 v[32:47], v[48:51], v[60:63], v[32:47]\n\t" \
  "ds_read_b128 v[48:51], v82\n\t" \
  "ds_read_b64_tr_b16 v[52:53], v84 offset:2048\n\t" \
  "ds_read_b64_tr_b16 v[54:55], v84 offset:2304\n\t" \
  "ds_read_b64_tr_b16 v[56:57], v84 offset:6144\n\t" \
  "ds_read_b64_tr_b16 v[58:59], v84 offset:6400\n\t" \
  "ds_read_b64_tr_b16 v[60:61], v84 offset:10240\n\t" \
  "ds_read_b64_tr_b16 v[62:63], v84 offset:10496\n\t" \
  "s_waitcnt lgkmcnt(7)\n\t" \
  "v_mfma_f32_32x32x16_bf16 v[0:15], v[68:71], v[64:67], v[0:15]\n\t" \
  "v_mfma_f32_32x32x16_bf16 v[16:31], v[72:75], v[64:67], v[16:31]\n\t" \
  "v_mfma_f32_32x32x16_bf16 v[32:47], v[64:67], v[76:79], v[32:47]\n\t" \
  "ds_read_b128 v[64:67], v83\n\t" \
  "ds_read_b64_tr_b16 v[68:69], v84 offset:3072\n\t" \
  "ds_read_b64_tr_b16 v[70:71], v84 offset:3328\n\t" \
  "ds_read_b64_tr_b16 v[72:73], v84 offset:7168\n\t" \
  "ds_read_b64_tr_b16 v[74:75], v84 offset:7424\n\t" \
  "ds_read_b64_tr_b16 v[76:77], v84 offset:11264\n\t" \
  "ds_read_b64_tr_b16 v[78:79], v84 offset:11520\n\t" \
  "s_waitcnt lgkmcnt(7)\n\t" \
  "v_mfma_f32_32x32x16_bf16 v[0:15], v[52:55], v[48:51], v[0:15]\n\t" \
  "v_mfma_f32_32x32x16_bf16 v[16:31], v[56:59], v[48:51], v[16:31]\n\t" \
  "v_mfma_f32_32x32x16_bf16 v[32:47], v[48:51], v[60:63], v[32:47]\n\t" \
  "s_cmp_eq_u32 %[kt], 7\n\t" \
  "s_cbranch_scc1 9f\n\t" \
  "s_add_u32 %[kt], %[kt], 1\n\t" \
  "s_and_b32 %[t0], %[kt], 3\n\t" \
  "s_cmp_eq_u32 %[t0], 0\n\t" \
  "s_cselect_b32 %[t0], %[cmw], %[cst]\n\t" \
  "v_add_u32 v80, %[t0], v80\n\t" \
  "v_add_u32 v81, %[t0], v81\n\t" \
  "v_add_u32 v82, %[t0], v82\n\t" \
  "v_add_u32 v83, %[t0], v83\n\t" \
  "v_add_u32 v84, %[t0], v84\n\t" \
  "s_waitcnt lgkmcnt(0)\n\t" \
  "s_barrier\n\t" \
  "ds_read_b128 v[48:51], v80\n\t" \
  "ds_read_b64_tr_b16 v[52:53], v84 offset:0\n\t" \
  "ds_read_b64_tr_b16 v[54:55], v84 offset:256\n\t" \
  "ds_read_b64_tr_b16 v[56:57], v84 offset:4096\n\t" \
  "ds_read_b64_tr_b16 v[58:59], v84 offset:4352\n\t" \
  "ds_read_b64_tr_b16 v[60:61], v84 offset:8192\n\t" \
  "ds_read_b64_tr_b16 v[62:63], v84 offset:8448\n\t" \
  "v_mfma_f32_32x32x16_bf16 v[0:15], v[68:71], v[64:67], v[0:15]\n\t" \
  "v_mfma_f32_32x32x16_bf16 v[16:31], v[72:75], v[64:67], v[16:31]\n\t" \
  "v_mfma_f32_32x32x16_bf16 v[32:47], v[64:67], v[76:79], v[32:47]\n\t" \
  "s_branch 1b\n\t" \
  "9:\n\t" \
  "s_waitcnt lgkmcnt(0)\n\t" \
  "v_mfma_f32_32x32x16_bf16 v[0:15], v[68:71], v[64:67], v[0:15]\n\t" \
  "v_mfma_f32_32x32x16_bf16 v[16:31], v[72:75], v[64:67], v[16:31]\n\t" \
  "v_mfma_f32_32x32x16_bf16 v[32:47], v[64:67], v[76:79], v[32:47]\n\t" \
  "s_nop 15\n\t" \
  ""

constexpr int K1_ST = 28672, K1_SIDE = 4 * K1_ST;
__global__ __launch_bounds__(768) void k1(const u16* __restrict__ x, const u16* __restrict__ wqkv,
                                          const u16* __restrict__ wo, const u16* __restrict__ w1,
                                          const u16* __restrict__ gw,
                                          char* __restrict__ ws) {
  __shared__ __attribute__((aligned(16))) char lds[K1_SIDE + 4 * 6144 + 4096];
  const int bid = blockIdx.x, xcd = bid & 7, idx = bid >> 3;
  const int rb = (xcd & 1) * 8 + (idx & 7), j = (xcd >> 1) * 4 + (idx >> 3);
  const int tid = threadIdx.x, lane = tid & 63;
  const int w = __builtin_amdgcn_readfirstlane(tid >> 6);
  const int r = lane & 31, h = lane >> 5, m = w & 3;
  const unsigned lb = (unsigned)(size_t)(LAS char*)lds;
  const int tile = rb * 4 + m;
  const int q4 = (lane & 15) >> 2, p4 = lane & 3, grp = (lane >> 4) & 1;
  const int sw = (r >> 1) & 7;
  const auto rs = WS_RSRC(ws);
  const int u0 = bid * 4 + m;
  const int tok = rb * 128 + j * 8 + 2 * m;
  const int kq0 = KSTAG;
  if (w >= 8) {
    __builtin_amdgcn_s_sleep(SIDE_DELAY);
    const unsigned sl = lb + K1_SIDE + m * 6144;
    {
      const int u = min(u0, 959);
      const bool is1 = u < 704;
      const int uu = is1 ? u : u - 704;
      const int kb = is1 ? uu / 22 : uu >> 3, pr = is1 ? uu % 22 : uu & 7;
      const int ld = is1 ? F_ : D_;
      const u16* src = is1 ? w1 + (size_t)kb * 16 * F_ : wo + (size_t)kb * 16 * D_;
      const unsigned po = (unsigned)((lane >> 3) * ld + min(64 * pr + (lane & 7) * 8, ld - 8)) * 2u;
      gldsa16(src, po, sl);
      gldsa16(src + 8 * ld, po, sl + 1024);
    }
    gldsa16(x + (size_t)tok * D_, lane * 16, sl + 4096);
    gldsa16(x + (size_t)tok * D_, lane * 16 + 1024, sl + 5120);
    gldsa16(gw, lane * 16, lb + K1_SIDE + 24576 + m * 1024);
    if (bid == 0 && w == 8 && lane < 40) ((unsigned*)(ws + WS_CNT))[lane * 32] = 0;
    const int sstar = ((j >> 1) - kq0) & 7;
    const char* xsrc = lds + (sstar & 3) * K1_ST + m * 4096 + r * 128;
    const char* sp = lds + K1_SIDE + m * 6144;
    int job = -1;
#pragma unroll 1
    for (int s = 0; s < 8 || job < 2; ++s) {
      if (s < 8) {
        asm volatile("s_waitcnt lgkmcnt(0)" ::: "memory");
        BAR();
      } else if (job < 0) {
        WAIT_V(0);
        job = 0;
      }
      if (job < 0) {
        const unsigned ib = __builtin_amdgcn_s_getreg(7 | (31 << 11));
        if (((ib & 15u) | ((ib >> 18) & 48u)) == 0) { job = 0; asm volatile("" ::: "memory"); }
      }
      if (s == sstar) {
        const u32x4 xc0 = *(const u32x4*)(xsrc + (((4 * (j & 1) + h) ^ sw) * 16));
        const u32x4 xc1 = *(const u32x4*)(xsrc + (((4 * (j & 1) + 2 + h) ^ sw) * 16));
        WST16(rs, WS_XF + ((4 * (j >> 1) + 2 * (j & 1)) * 64 + tile) * 1024 + lane * 16, xc0);
        WST16(rs, WS_XF + ((4 * (j >> 1) + 2 * (j & 1) + 1) * 64 + tile) * 1024 + lane * 16, xc1);
      }
      if (job == 0) {
        const int c = 0, u = u0;
        if (u < 960) {
          const bool is1 = u < 704;
          const int uu = is1 ? u : u - 704;
          const int kb = is1 ? uu / 22 : uu >> 3, pr = is1 ? uu % 22 : uu & 7;
          const char* pp = sp + c * 2048 + ((is1 ? 8 : 4) * h + q4) * 128 + grp * 32 + p4 * 8;
          const int hi_off = is1 ? 512 : 1024;
#pragma unroll
          for (int n = 0; n < 2; ++n) {
            const s16x4 lo = ldtr(pp + n * 64), hi = ldtr(pp + n * 64 + hi_off);
            const bf16x8 f = __builtin_shufflevector(lo, hi, 0, 1, 2, 3, 4, 5, 6, 7);
            const int blk = 2 * pr + n;
            const unsigned dst = is1 ? WS_W1P + (kb * 43 + blk) * 1024 : WS_WOP + (kb * 16 + blk) * 1024;
            if (!is1 || blk < 43) WST16(rs, dst + lane * 16, f);
          }
        }
      }
      if (job == 1) {
        const u32x4 gg = *(const u32x4*)(lds + K1_SIDE + 24576 + m * 1024 + lane * 16);
#pragma unroll
        for (int t = 0; t < 2; ++t) {
          const u32x4 gx = *(const u32x4*)(sp + 4096 + t * 1024 + lane * 16);
          float sm = 0.f;
#pragma unroll
          for (int e = 0; e < 4; ++e) {
            sm += rbf(bf_lo(gx[e]) * bf_lo(gg[e]));
            sm += rbf(bf_hi(gx[e]) * bf_hi(gg[e]));
          }
          sm = row16_sum(sm);
          sm = (rdl(sm, 0) + rdl(sm, 16)) + (rdl(sm, 32) + rdl(sm, 48));
          sm = rbf(sm);
          const float g = rbf(__builtin_amdgcn_rcpf(1.f + __builtin_amdgcn_exp2f(-sm * 1.4426950408889634f)));
          if (lane == 0) ((float*)(ws + WS_GATE))[tok + t] = g;
        }
      }
      if (job >= 0) ++job;
    }
    return;
  }
  if (w >= 4) {
    const unsigned xo0 = (unsigned)((lane >> 3) * D_ + (((lane & 7) ^ ((lane >> 4) & 7)) * 8)) * 2u;
    const unsigned xo1 = (unsigned)((8 + (lane >> 3)) * D_ + (((lane & 7) ^ ((4 + (lane >> 4)) & 7)) * 8)) * 2u;
    const unsigned wqo = (unsigned)((lane >> 2) * 1536 + (lane & 3) * 8) * 2u;
    int kq = kq0;
    const char* xb = (const char*)(x + (size_t)(rb * 128 + 32 * m) * D_ + 64 * kq);
    const char* wqb = (const char*)(wqkv + (size_t)(16 * m + 64 * kq) * 1536 + 64 * j);
    const unsigned vo_ = wqo + 2048 - 64 * j;
    unsigned xl = lb + m * 4096, wl = lb + 16384 + m * 1024;
#define STAGE()                                                                                   \
  do {                                                                                            \
    gldsa16(wqb, wqo, wl);                                                                        \
    gldsa16(wqb, wqo + 64, wl + 4096);                                                            \
    gldsa16(wqb, vo_, wl + 8192);                                                                 \
    gldsa16(xb, xo0, xl);                                                                         \
    gldsa16(xb, xo1, xl + 1024);                                                                  \
    gldsa16(xb + 16 * D_ * 2, xo0, xl + 2048);                                                    \
    gldsa16(xb + 16 * D_ * 2, xo1, xl + 3072);                                                    \
    kq = (kq + 1) & 7;                                                                            \
    { const int dk_ = kq ? 128 : -896; xb += dk_; wqb += dk_ * 1536; }                            \
    const bool wrap_ = xl + K1_ST >= lb + 4 * K1_ST;                                              \
    xl += wrap_ ? 0u - 3u * K1_ST : K1_ST; wl += wrap_ ? 0u - 3u * K1_ST : K1_ST;                 \
  } while (0)
    STAGE();
    STAGE();
    WAIT_V(7); BAR();
    STAGE();
    WAIT_V(7); BAR();
    STAGE();
#pragma unroll 1
    for (int kt = 2; kt < 6; ++kt) {
      STAGE();
      WAIT_V(14); BAR();
    }
#undef STAGE
    WAIT_V(7); BAR();
    WAIT_V(0); BAR();
    return;
  }

  f32x16 acc[3];
#pragma unroll
  for (int b = 0; b < 3; ++b)
#pragma unroll
    for (int i = 0; i < 16; ++i) acc[b][i] = 0.f;
  {
    const unsigned xr0 = lb + m * 4096 + r * 128;
    unsigned xa0 = xr0 + ((0 + h) ^ sw) * 16, xa1 = xr0 + ((2 + h) ^ sw) * 16, xa2 = xr0 + ((4 + h) ^ sw) * 16,
             xa3 = xr0 + ((6 + h) ^ sw) * 16;
    unsigned wa = lb + 16384 + (8 * h + q4) * 64 + grp * 32 + p4 * 8;
    unsigned kt = 0, t0;
    asm volatile(K1_LOOP_ASM
                 : "+{v[0:15]}"(acc[0]), "+{v[16:31]}"(acc[1]), "+{v[32:47]}"(acc[2]), "+{v80}"(xa0),
                   "+{v81}"(xa1), "+{v82}"(xa2), "+{v83}"(xa3), "+{v84}"(wa), [kt] "+s"(kt), [t0] "=&s"(t0)
                 : [cst] "s"((unsigned)K1_ST), [cmw] "s"(0u - 3u * K1_ST)
                 : "memory", "scc", "v48", "v49", "v50", "v51", "v52", "v53", "v54", "v55", "v56", "v57",
                   "v58", "v59", "v60", "v61", "v62", "v63", "v64", "v65", "v66", "v67", "v68", "v69", "v70",
                   "v71", "v72", "v73", "v74", "v75", "v76", "v77", "v78", "v79");
  }
#pragma unroll
  for (int b = 0; b < 2; ++b) {
    const int c0 = 64 * j + 32 * b;
    const bool isq = c0 < 512;
    const float sc = isq ? 0.36067376022224085f : 1.0f;
    const int head0 = (c0 & 511) >> 4;
    const unsigned dst = isq ? WS_Q : WS_K;
#pragma unroll
    for (int hh = 0; hh < 2; ++hh) {
      u32x4 o;
#pragma unroll
      for (int e = 0; e < 4; ++e) o[e] = pk2(acc[b][8 * hh + 2 * e] * sc, acc[b][8 * hh + 2 * e + 1] * sc);
      WST16(rs, dst + (((head0 + hh) * 64 + tile) * 64 + lane) * 16, o);
      if (!isq) {
        float ss = 0.f;
#pragma unroll
        for (int e = 0; e < 4; ++e) {
          const float a = bf_lo(o[e]), c = bf_hi(o[e]);
          ss = __builtin_fmaf(a, a, ss);
          ss = __builtin_fmaf(c, c, ss);
        }
        ss = row16_max(add_half(ss));
        ss = fmaxf(rdl(ss, 0), rdl(ss, 16));
        if (lane == 0) ((float*)(ws + WS_KMAX))[(head0 + hh) * 64 + tile] = ss;
      }
    }
  }
  {
    const int head = 2 * j + (r >> 4), d = r & 15;
#pragma unroll
    for (int s = 0; s < 2; ++s) {
      u32x4 o;
#pragma unroll
      for (int e = 0; e < 4; ++e) o[e] = pk2(acc[2][8 * s + 2 * e], acc[2][8 * s + 2 * e + 1]);
      WST16(rs, WS_V + (head * 64 + tile) * 1024 + s * 512 + h * 256 + d * 16, o);
    }
  }
}

#define ATTN_LOOP_ASM \
  "1:\n\t" \
  "v_exp_f32 v32, v32\n\t" \
  "v_exp_f32 v33, v33\n\t" \
  "v_exp_f32 v34, v34\n\t" \
  "v_cvt_pk_bf16_f32 v88, v32, v33\n\t" \
  "v_mfma_f32_32x32x16_bf16 v[0:15], v[84:87], v[92:95], v[0:15]\n\t" \
  "s_mov_b64 exec, %[mask]\n\t" \
  "ds_read_b128 v[80:83], v97 offset:1024\n\t" \
  "ds_read_b128 v[84:87], v97 offset:1536\n\t" \
  "s_mov_b64 exec, -1\n\t" \
  "v_exp_f32 v35, v35\n\t" \
  "v_exp_f32 v36, v36\n\t" \
  "v_cvt_pk_bf16_f32 v89, v34, v35\n\t" \
  "v_exp_f32 v37, v37\n\t" \
  "v_exp_f32 v38, v38\n\t" \
  "v_cvt_pk_bf16_f32 v90, v36, v37\n\t" \
  "v_exp_f32 v39, v39\n\t" \
  "v_exp_f32 v40, v40\n\t" \
  "v_cvt_pk_bf16_f32 v91, v38, v39\n\t" \
  "v_exp_f32 v41, v41\n\t" \
  "v_exp_f32 v42, v42\n\t" \
  "v_mfma_f32_32x32x16_bf16 v[0:15], v[72:75], v[88:91], v[0:15]\n\t" \
  "v_cvt_pk_bf16_f32 v92, v40, v41\n\t" \
  "v_exp_f32 v43, v43\n\t" \
  "v_exp_f32 v44, v44\n\t" \
  "v_cvt_pk_bf16_f32 v93, v42, v43\n\t" \
  "v_exp_f32 v45, v45\n\t" \
  "v_exp_f32 v46, v46\n\t" \
  "v_cvt_pk_bf16_f32 v94, v44, v45\n\t" \
  "v_exp_f32 v47, v47\n\t" \
  "s_waitcnt lgkmcnt(0)\n\t" \
  "v_cvt_pk_bf16_f32 v95, v46, v47\n\t" \
  "v_mfma_f32_32x32x16_bf16 v[32:47], v[68:71], v[64:67], v[16:31]\n\t" \
  "ds_read_b128 v[68:71], v96 offset:3072\n\t" \
  "v_exp_f32 v48, v48\n\t" \
  "v_exp_f32 v49, v49\n\t" \
  "v_exp_f32 v50, v50\n\t" \
  "v_cvt_pk_bf16_f32 v88, v48, v49\n\t" \
  "v_mfma_f32_32x32x16_bf16 v[0:15], v[76:79], v[92:95], v[0:15]\n\t" \
  "s_mov_b64 exec, %[mask]\n\t" \
  "ds_read_b128 v[72:75], v97 offset:2048\n\t" \
  "ds_read_b128 v[76:79], v97 offset:2560\n\t" \
  "s_mov_b64 exec, -1\n\t" \
  "v_exp_f32 v51, v51\n\t" \
  "v_exp_f32 v52, v52\n\t" \
  "v_cvt_pk_bf16_f32 v89, v50, v51\n\t" \
  "v_exp_f32 v53, v53\n\t" \
  "v_exp_f32 v54, v54\n\t" \
  "v_cvt_pk_bf16_f32 v90, v52, v53\n\t" \
  "v_exp_f32 v55, v55\n\t" \
  "v_exp_f32 v56, v56\n\t" \
  "v_cvt_pk_bf16_f32 v91, v54, v55\n\t" \
  "v_exp_f32 v57, v57\n\t" \
  "v_exp_f32 v58, v58\n\t" \
  "v_mfma_f32_32x32x16_bf16 v[0:15], v[80:83], v[88:91], v[0:15]\n\t" \
  "v_cvt_pk_bf16_f32 v92, v56, v57\n\t" \
  "v_exp_f32 v59, v59\n\t" \
  "v_exp_f32 v60, v60\n\t" \
  "v_cvt_pk_bf16_f32 v93, v58, v59\n\t" \
  "v_exp_f32 v61, v61\n\t" \
  "v_exp_f32 v62, v62\n\t" \
  "v_cvt_pk_bf16_f32 v94, v60, v61\n\t" \
  "v_exp_f32 v63, v63\n\t" \
  "s_waitcnt lgkmcnt(0)\n\t" \
  "v_cvt_pk_bf16_f32 v95, v62, v63\n\t" \
  "v_mfma_f32_32x32x16_bf16 v[48:63], v[68:71], v[64:67], v[16:31]\n\t" \
  "ds_read_b128 v[68:71], v96 offset:4096\n\t" \
  "v_exp_f32 v32, v32\n\t" \
  "v_exp_f32 v33, v33\n\t" \
  "v_exp_f32 v34, v34\n\t" \
  "v_cvt_pk_bf16_f32 v88, v32, v33\n\t" \
  "v_mfma_f32_32x32x16_bf16 v[0:15], v[84:87], v[92:95], v[0:15]\n\t" \
  "s_mov_b64 exec, %[mask]\n\t" \
  "ds_read_b128 v[80:83], v97 offset:3072\n\t" \
  "ds_read_b128 v[84:87], v97 offset:3584\n\t" \
  "s_mov_b64 exec, -1\n\t" \
  "v_exp_f32 v35, v35\n\t" \
  "v_exp_f32 v36, v36\n\t" \
  "v_cvt_pk_bf16_f32 v89, v34, v35\n\t" \
  "v_exp_f32 v37, v37\n\t" \
  "v_exp_f32 v38, v38\n\t" \
  "v_cvt_pk_bf16_f32 v90, v36, v37\n\t" \
  "v_exp_f32 v39, v39\n\t" \
  "v_exp_f32 v40, v40\n\t" \
  "v_cvt_pk_bf16_f32 v91, v38, v39\n\t" \
  "v_exp_f32 v41, v41\n\t" \
  "v_exp_f32 v42, v42\n\t" \
  "v_mfma_f32_32x32x16_bf16 v[0:15], v[72:75], v[88:91], v[0:15]\n\t" \
  "v_cvt_pk_bf16_f32 v92, v40, v41\n\t" \
  "v_exp_f32 v43, v43\n\t" \
  "v_exp_f32 v44, v44\n\t" \
  "v_cvt_pk_bf16_f32 v93, v42, v43\n\t" \
  "v_exp_f32 v45, v45\n\t" \
  "v_exp_f32 v46, v46\n\t" \
  "v_cvt_pk_bf16_f32 v94, v44, v45\n\t" \
  "v_exp_f32 v47, v47\n\t" \
  "s_waitcnt lgkmcnt(0)\n\t" \
  "v_cvt_pk_bf16_f32 v95, v46, v47\n\t" \
  "v_mfma_f32_32x32x16_bf16 v[32:47], v[68:71], v[64:67], v[16:31]\n\t" \
  "ds_read_b128 v[68:71], v96 offset:5120\n\t" \
  "v_exp_f32 v48, v48\n\t" \
  "v_exp_f32 v49, v49\n\t" \
  "v_exp_f32 v50, v50\n\t" \
  "v_cvt_pk_bf16_f32 v88, v48, v49\n\t" \
  "v_mfma_f32_32x32x16_bf16 v[0:15], v[76:79], v[92:95], v[0:15]\n\t" \
  "s_mov_b64 exec, %[mask]\n\t" \
  "ds_read_b128 v[72:75], v97 offset:4096\n\t" \
  "ds_read_b128 v[76:79], v97 offset:4608\n\t" \
  "s_mov_b64 exec, -1\n\t" \
  "v_exp_f32 v51, v51\n\t" \
  "v_exp_f32 v52, v52\n\t" \
  "v_cvt_pk_bf16_f32 v89, v50, v51\n\t" \
  "v_exp_f32 v53, v53\n\t" \
  "v_exp_f32 v54, v54\n\t" \
  "v_cvt_pk_bf16_f32 v90, v52, v53\n\t" \
  "v_exp_f32 v55, v55\n\t" \
  "v_exp_f32 v56, v56\n\t" \
  "v_cvt_pk_bf16_f32 v91, v54, v55\n\t" \
  "v_exp_f32 v57, v57\n\t" \
  "v_exp_f32 v58, v58\n\t" \
  "v_mfma_f32_32x32x16_bf16 v[0:15], v[80:83], v[88:91], v[0:15]\n\t" \
  "v_cvt_pk_bf16_f32 v92, v56, v57\n\t" \
  "v_exp_f32 v59, v59\n\t" \
  "v_exp_f32 v60, v60\n\t" \
  "v_cvt_pk_bf16_f32 v93, v58, v59\n\t" \
  "v_exp_f32 v61, v61\n\t" \
  "v_exp_f32 v62, v62\n\t" \
  "v_cvt_pk_bf16_f32 v94, v60, v61\n\t" \
  "v_exp_f32 v63, v63\n\t" \
  "s_waitcnt lgkmcnt(0)\n\t" \
  "v_cvt_pk_bf16_f32 v95, v62, v63\n\t" \
  "v_mfma_f32_32x32x16_bf16 v[48:63], v[68:71], v[64:67], v[16:31]\n\t" \
  "ds_read_b128 v[68:71], v96 offset:6144\n\t" \
  "v_add_u32 v96, 0x1000, v96\n\t" \
  "v_add_u32 v97, 0x1000, v97\n\t" \
  "s_sub_u32 %[cnt], %[cnt], 1\n\t" \
  "s_and_b32 %[tmp], %[cnt], 13\n\t" \
  "s_cmp_lg_u32 %[tmp], 12\n\t" \
  "s_cbranch_scc1 2f\n\t" \
  "3:\n\t" \
  "ds_read_b32 v98, v99\n\t" \
  "s_waitcnt lgkmcnt(0)\n\t" \
  "v_readfirstlane_b32 %[tmp], v98\n\t" \
  "s_cmp_ge_u32 %[tmp], 4\n\t" \
  "s_cbranch_scc1 4f\n\t" \
  "s_sleep 1\n\t" \
  "s_branch 3b\n\t" \
  "4:\n\t" \
  "v_add_u32 v99, 4, v99\n\t" \
  "2:\n\t" \
  "s_cmp_lg_u32 %[cnt], 7\n\t" \
  "s_cbranch_scc1 5f\n\t" \
  "v_mov_b32 v98, 1\n\t" \
  "s_mov_b64 exec, 1\n\t" \
  "ds_add_u32 v99, v98\n\t" \
  "s_mov_b64 exec, -1\n\t" \
  "5:\n\t" \
  "s_cmp_lg_u32 %[cnt], 15\n\t" \
  "s_cbranch_scc1 6f\n\t" \
  "v_mov_b32 v98, 1\n\t" \
  "s_waitcnt vmcnt(0)\n\t" \
  "s_mov_b64 exec, 1\n\t" \
  "ds_add_u32 v100, v98\n\t" \
  "s_mov_b64 exec, -1\n\t" \
  "6:\n\t" \
  "s_cmp_lg_u32 %[cnt], 0\n\t" \
  "s_cbranch_scc1 1b\n\t" \
  "s_waitcnt lgkmcnt(0)\n\t" \
  "s_nop 15\n\t" \
  ""

constexpr int L2_FLAG = 131072;
__global__ __launch_bounds__(768) void k2(char* __restrict__ ws, const u16* __restrict__ b1,
                                          const u16* __restrict__ w2, u16* __restrict__ out) {
  __shared__ __attribute__((aligned(16))) char lds[L2_FLAG + 256 + 8192];
  const int bid = blockIdx.x, xcd = bid & 7, idx = bid >> 3;
  const int head = xcd * 4 + (idx & 3), qb = idx >> 2;
  const int tp = xcd >> 1, cp = (xcd & 1) * 4 + (idx & 3);
  const int T0 = qb * 8 + 2 * tp;
  const int tid = threadIdx.x, lane = tid & 63;
  const int w = __builtin_amdgcn_readfirstlane(tid >> 6);
  const int r = lane & 31, h = lane >> 5;
  const unsigned lb = (unsigned)(size_t)(LAS char*)lds;
  const unsigned l16 = lane * 16;
  const auto rs = WS_RSRC(ws);
  char* cnt = ws + WS_CNT;
  char* cntF = cnt + (8 + qb * 4 + tp) * 128;
  if (w >= 8) {
    const int L = w - 8;
    {
      const char* ksrc = ws + WS_K + (size_t)(head * 64 + L) * 1024;
      const char* vsrc = ws + WS_V + (size_t)(head * 64 + L) * 1024;
      const unsigned kdst = lb + L * 1024, vdst = lb + 65536 + L * 1024;
#pragma unroll
      for (int c = 0; c < 3; ++c) { gldsa16(ksrc + c * 4096, l16, kdst + c * 4096); gldsa16(vsrc + c * 4096, l16, vdst + c * 4096); }
      if (tid >= 512 && tid < 520) ((volatile LAS unsigned*)(lds + L2_FLAG))[tid - 512] = 0;
      WAIT_V(0); BAR();
#pragma unroll 1
      for (int c = 3; c < 16; ++c) {
        gldsa16(ksrc + c * 4096, l16, kdst + c * 4096); gldsa16(vsrc + c * 4096, l16, vdst + c * 4096);
      }
    }
    const int pu = bid * 4 + L, puc = min(pu, 687);
    {
      const u16* src = w2 + (size_t)(puc >> 3) * 16 * D_ + (puc & 7) * 64;
      const unsigned po = (unsigned)((lane >> 3) * D_ + (lane & 7) * 8) * 2u;
      gldsa16(src, po, lb + L2_FLAG + 256 + L * 2048);
      gldsa16(src + 8 * D_, po, lb + L2_FLAG + 256 + L * 2048 + 1024);
    }
    {
      const int T = T0 + (L & 1);
      const int f0 = cp + 24 * (L >> 1);
      const int fbc2 = min(f0 + 16, 42);
      const auto rx = __builtin_amdgcn_make_buffer_rsrc((void*)(ws + WS_XF + T * 1024), 0, 31 * 65536 + 1024, 0x00020000);
      const auto rw = __builtin_amdgcn_make_buffer_rsrc((void*)(ws + WS_W1P), 0, 32 * 43 * 1024, 0x00020000);
      const unsigned wv0 = l16 + f0 * 1024, wv1 = l16 + (f0 + 8) * 1024, wv2 = l16 + fbc2 * 1024;
      constexpr int PD = 6;
      u32x4 fx[PD], fw[PD][3];
#pragma unroll
      for (int i = 0; i < PD; ++i) {
        fx[i] = BLD(rx, l16, i * 65536, 0);
        fw[i][0] = BLD(rw, wv0, i * 44032, 0); fw[i][1] = BLD(rw, wv1, i * 44032, 0); fw[i][2] = BLD(rw, wv2, i * 44032, 0);
      }
      WAIT_V(40);
      add_lds(lds + L2_FLAG, lane);
      WAIT_V(24);
      add_lds(lds + L2_FLAG + 4, lane);
      {
        const char* pp = lds + L2_FLAG + 256 + L * 2048 + (4 * h + ((lane & 15) >> 2)) * 128 + ((lane >> 4) & 1) * 32 + (lane & 3) * 8;
#pragma unroll
        for (int n = 0; n < 2; ++n) {
          const s16x4 lo = ldtr(pp + n * 64), hi = ldtr(pp + n * 64 + 1024);
          const bf16x8 f = __builtin_shufflevector(lo, hi, 0, 1, 2, 3, 4, 5, 6, 7);
          if (pu < 688) WST16(rs, WS_W2P + (pu * 2 + n) * 1024 + l16, f);
        }
      }
      __builtin_amdgcn_s_setprio(FFN1_PRIO);
      f32x16 a[3];
#pragma unroll
      for (int g = 0; g < 4; ++g) {
        const u32x2 b0 = *(const u32x2*)(b1 + 32 * f0 + 8 * g + 4 * h);
        const u32x2 b1_ = *(const u32x2*)(b1 + 32 * (f0 + 8) + 8 * g + 4 * h);
        const u32x2 b2 = *(const u32x2*)(b1 + 32 * fbc2 + 8 * g + 4 * h);
        a[0][4 * g] = bf_lo(b0[0]); a[0][4 * g + 1] = bf_hi(b0[0]); a[0][4 * g + 2] = bf_lo(b0[1]); a[0][4 * g + 3] = bf_hi(b0[1]);
        a[1][4 * g] = bf_lo(b1_[0]); a[1][4 * g + 1] = bf_hi(b1_[0]); a[1][4 * g + 2] = bf_lo(b1_[1]); a[1][4 * g + 3] = bf_hi(b1_[1]);
        a[2][4 * g] = bf_lo(b2[0]); a[2][4 * g + 1] = bf_hi(b2[0]); a[2][4 * g + 2] = bf_lo(b2[1]); a[2][4 * g + 3] = bf_hi(b2[1]);
      }
      __builtin_amdgcn_sched_barrier(0);
      WAIT_V(0);
#pragma unroll 1
      for (int k0 = PD; k0 < 12 + PD; k0 += PD) {
#pragma unroll
        for (int i = 0; i < PD; ++i) {
          a[0] = MFMAU(fw[i][0], fx[i], a[0]);
          a[1] = MFMAU(fw[i][1], fx[i], a[1]);
          a[2] = MFMAU(fw[i][2], fx[i], a[2]);
          fx[i] = BLD(rx, l16, (k0 + i) * 65536, 0);
          fw[i][0] = BLD(rw, wv0, (k0 + i) * 44032, 0); fw[i][1] = BLD(rw, wv1, (k0 + i) * 44032, 0);
          fw[i][2] = BLD(rw, wv2, (k0 + i) * 44032, 0);
          __builtin_amdgcn_sched_barrier(0);
        }
      }
      wait_lds(lds + L2_FLAG + 24, 8);
      if (add_lds(lds + L2_FLAG + 16, lane) == 3 && lane == 0) __hip_atomic_fetch_add((GAS unsigned*)(cnt + xcd * 128), 1u, RLX_AGENT);
      __builtin_amdgcn_sched_barrier(0);
#pragma unroll 1
      for (int k0 = 12 + PD; k0 < 30 + PD; k0 += PD) {
#pragma unroll
        for (int i = 0; i < PD; ++i) {
          a[0] = MFMAU(fw[i][0], fx[i], a[0]);
          a[1] = MFMAU(fw[i][1], fx[i], a[1]);
          a[2] = MFMAU(fw[i][2], fx[i], a[2]);
          fx[i] = BLD(rx, l16, (k0 + i) * 65536, 0);
          fw[i][0] = BLD(rw, wv0, (k0 + i) * 44032, 0); fw[i][1] = BLD(rw, wv1, (k0 + i) * 44032, 0);
          fw[i][2] = BLD(rw, wv2, (k0 + i) * 44032, 0);
          __builtin_amdgcn_sched_barrier(0);
        }
      }
#pragma unroll
      for (int i = 0; i < 2; ++i) {
        a[0] = MFMAU(fw[i][0], fx[i], a[0]);
        a[1] = MFMAU(fw[i][1], fx[i], a[1]);
        a[2] = MFMAU(fw[i][2], fx[i], a[2]);
      }
      const float g1 = rbf(1.f - ((const float*)(ws + WS_GATE))[T * 32 + r]);
      __builtin_amdgcn_s_setprio(MISH_PRIO);
#pragma unroll
      for (int b = 0; b < 3; ++b) {
        if (f0 + 8 * b < 43) {
#pragma unroll
          for (int hh = 0; hh < 2; ++hh) {
            u32x4 o;
#pragma unroll
            for (int gq = 0; gq < 2; ++gq) {
              const int g = 2 * hh + gq;
              const float h0 = a[b][4 * g + 0], h1 = a[b][4 * g + 1], h2 = a[b][4 * g + 2], h3 = a[b][4 * g + 3];
              o[2 * gq] = pk2(mish_g(h0, g1), mish_g(h1, g1));
              o[2 * gq + 1] = pk2(mish_g(h2, g1), mish_g(h3, g1));
            }
            WST16(rs, WS_ACT + ((2 * (f0 + 8 * b) + hh) * 64 + T) * 1024 + l16, o);
          }
        }
      }
      WAIT_V(0);
      if (add_lds(lds + L2_FLAG + 12, lane) == 3 && lane == 0) __hip_atomic_fetch_add((GAS unsigned*)cntF, 1u, RLX_AGENT);
    }
    {
      const int klast = min(22 * L + 21, 85);
      const auto ra = __builtin_amdgcn_make_buffer_rsrc((void*)(ws + WS_ACT + T0 * 1024), 0, klast * 65536 + 2048, 0x00020000);
      const auto rw = __builtin_amdgcn_make_buffer_rsrc((void*)(ws + WS_W2P + cp * 2048), 0, klast * 16384 + 2048, 0x00020000);
      constexpr int PD = 5;
      u32x4 fa[PD][2], fw[PD][2];
      const int kb0 = 22 * L;
      if (L == 0) {
        char* pw = lane < 8 ? cnt + lane * 128 : cntF;
        const unsigned want = lane < 8 ? 32u : 8u;
        wait_global(cntF, 8);
        for (unsigned spins = 0; spins < (1u << 17); ++spins) {
          const unsigned v = __hip_atomic_load((GAS unsigned*)pw, RLX_AGENT);
          if (__all(v >= want)) break;
          __builtin_amdgcn_s_sleep(2);
        }
        asm volatile("" ::: "memory");
        add_lds(lds + L2_FLAG + 20, lane);
      } else {
        wait_lds(lds + L2_FLAG + 20, 1);
      }
      __builtin_amdgcn_s_setprio(FFN2_PRIO);
#pragma unroll
      for (int i = 0; i < PD; ++i) {
        fa[i][0] = BLD(ra, l16, (kb0 + i) * 65536, HO_AUX); fa[i][1] = BLD(ra, l16 + 1024, (kb0 + i) * 65536, HO_AUX);
        fw[i][0] = BLD(rw, l16, (kb0 + i) * 16384, HO_AUX); fw[i][1] = BLD(rw, l16 + 1024, (kb0 + i) * 16384, HO_AUX);
        __builtin_amdgcn_sched_barrier(0);
      }
      f32x16 b[4];
#pragma unroll
      for (int t = 0; t < 4; ++t)
#pragma unroll
        for (int i = 0; i < 16; ++i) b[t][i] = 0.f;
      __builtin_amdgcn_sched_barrier(0);
#pragma unroll 1
      for (int k0 = PD; k0 < 20 + PD; k0 += PD) {
#pragma unroll
        for (int i = 0; i < PD; ++i) {
#pragma unroll
          for (int t = 0; t < 4; ++t) b[t] = MFMAU(fw[i][t >> 1], fa[i][t & 1], b[t]);
          const int kn = kb0 + k0 + i;
          fa[i][0] = BLD(ra, l16, kn * 65536, HO_AUX); fa[i][1] = BLD(ra, l16 + 1024, kn * 65536, HO_AUX);
          fw[i][0] = BLD(rw, l16, kn * 16384, HO_AUX); fw[i][1] = BLD(rw, l16 + 1024, kn * 16384, HO_AUX);
          __builtin_amdgcn_sched_barrier(0);
        }
      }
#pragma unroll
      for (int i = 0; i < 2; ++i)
#pragma unroll
        for (int t = 0; t < 4; ++t) b[t] = MFMAU(fw[i][t >> 1], fa[i][t & 1], b[t]);
      WAIT_V(0);
      BAR();
      char* bs = lds + (L >> 1) * 65536 + 32768 + (L & 1) * 16384 + l16;
#pragma unroll
      for (int t = 0; t < 4; ++t)
#pragma unroll
        for (int q = 0; q < 4; ++q) {
          f32x4 v = {b[t][4 * q], b[t][4 * q + 1], b[t][4 * q + 2], b[t][4 * q + 3]};
          *(f32x4*)(bs + (t & 1) * 8192 + (t >> 1) * 4096 + q * 1024) = v;
        }
      WAIT_V(0);
      BAR();
      BAR();
    }
    return;
  }

  const int qt = qb * 8 + w;
  bf16x8 qf = *(const bf16x8*)(ws + WS_Q + (size_t)(head * 64 + qt) * 1024 + l16);
  float km = ((const float*)(ws + WS_KMAX))[head * 64 + lane];
  const float gq = ((const float*)(ws + WS_GATE))[qt * 32 + r];
  km = row16_max(km);
  km = fmaxf(fmaxf(rdl(km, 0), rdl(km, 16)), fmaxf(rdl(km, 32), rdl(km, 48)));
  float qs = 0.f;
  {
    const u32x4 qu = __builtin_bit_cast(u32x4, qf);
#pragma unroll
    for (int e = 0; e < 4; ++e) {
      const float a = bf_lo(qu[e]), c = bf_hi(qu[e]);
      qs = __builtin_fmaf(a, a, qs);
      qs = __builtin_fmaf(c, c, qs);
    }
    qs = add_half(qs);
  }
  float mb = __builtin_amdgcn_sqrtf(qs * km) * 1.0001f;
  asm volatile("" : "+v"(mb) : "v"(gq) : "memory");
  f32x16 negm, O;
#pragma unroll
  for (int i = 0; i < 16; ++i) { negm[i] = -mb; O[i] = 0.f; }

  const char* kp = lds + lane * 16;
  const char* vp = lds + 65536 + h * 256 + (lane & 15) * 16;
  const bool vact = (lane & 16) == 0;
  const short one = ((lane & 27) == 16) ? (short)0x3F80 : (short)0;
  bf16x8 va0, va1, vb0, vb1;
#pragma unroll
  for (int i = 0; i < 8; ++i) { va0[i] = one; va1[i] = one; vb0[i] = one; vb1[i] = one; }
  u32x4 p0 = {0, 0, 0, 0}, p1 = {0, 0, 0, 0};
  unsigned kaddr = lb + l16, vaddr = lb + 65536 + h * 256 + (lane & 15) * 16;
  const unsigned long long vmask = 0x0000FFFF0000FFFFull;

  {
    const u32x4 ones = {0xFFFFFFFFu, 0xFFFFFFFFu, 0xFFFFFFFFu, 0xFFFFFFFFu};
    WST16(rs, WS_O + (head * 64 + qt) * 1024 + l16, ones);
  }
  BAR();
  f32x16 Sa = MFMA(*(const bf16x8*)(kp), qf, negm);
  f32x16 Sb = MFMA(*(const bf16x8*)(kp + 1024), qf, negm);
  bf16x8 kf = *(const bf16x8*)(kp + 2048);
  if (vact) { va0 = *(const bf16x8*)(vp); va1 = *(const bf16x8*)(vp + 512); }
  {
    unsigned cnt = 16, tmp, fa_ = lb + L2_FLAG;
    asm volatile("s_nop 15\n\t" ATTN_LOOP_ASM
                 : "+{v[0:15]}"(O), "+{v[32:47]}"(Sa), "+{v[48:63]}"(Sb), "+{v[68:71]}"(kf),
                   "+{v[72:75]}"(va0), "+{v[76:79]}"(va1), "+{v[80:83]}"(vb0), "+{v[84:87]}"(vb1),
                   "+{v[88:91]}"(p0), "+{v[92:95]}"(p1), "+{v96}"(kaddr), "+{v97}"(vaddr), [cnt] "+s"(cnt),
                   [tmp] "=&s"(tmp), "+{v99}"(fa_)
                 : "{v[16:31]}"(negm), "{v[64:67]}"(qf), [mask] "s"(vmask), "{v100}"(lb + L2_FLAG + 24)
                 : "memory", "scc", "v98");
  }
  O = MFMA(vb1, __builtin_bit_cast(bf16x8, p1), O);

  float l = O[8];
  if (__any(!(l >= 8.271806125530277e-25f))) {
    float m = -INFINITY;
    f32x16 zero;
#pragma unroll
    for (int i = 0; i < 16; ++i) { zero[i] = 0.f; O[i] = 0.f; }
    const char* kg = ws + WS_K + (size_t)head * 65536 + l16;
    const char* vg = ws + WS_V + (size_t)head * 65536 + h * 256 + (lane & 15) * 16;
#pragma unroll 1
    for (int t = 0; t < 64; ++t) {
      f32x16 Sx = MFMA(*(const bf16x8*)(kg + t * 1024), qf, zero);
      float mx = Sx[0];
#pragma unroll
      for (int i = 1; i < 16; ++i) mx = fmaxf(mx, Sx[i]);
      mx = max_half(mx);
      const float mn = fmaxf(m, mx);
      const float alpha = __builtin_amdgcn_exp2f(m - mn);
      m = mn;
#pragma unroll
      for (int i = 0; i < 16; ++i) { O[i] *= alpha; Sx[i] -= mn; }
      if (vact) { va0 = *(const bf16x8*)(vg + t * 1024); va1 = *(const bf16x8*)(vg + t * 1024 + 512); }
#pragma unroll
      for (int e = 0; e < 4; ++e) {
        p0[e] = pk2(__builtin_amdgcn_exp2f(Sx[2 * e]), __builtin_amdgcn_exp2f(Sx[2 * e + 1]));
        p1[e] = pk2(__builtin_amdgcn_exp2f(Sx[8 + 2 * e]), __builtin_amdgcn_exp2f(Sx[8 + 2 * e + 1]));
      }
      O = MFMAU(va0, p0, O);
      O = MFMAU(va1, p1, O);
    }
    l = O[8];
  }
  {
    const float rl = gq / l;
    u32x4 o;
#pragma unroll
    for (int e = 0; e < 4; ++e) o[e] = pk2(O[2 * e] * rl, O[2 * e + 1] * rl);
    WST16(rs, WS_O + (head * 64 + qt) * 1024 + l16, o);
  }
  if (w < 4) {
    wait_lds(lds + L2_FLAG + 8, 8);
    const char* wsrc = ws + WS_WOP + cp * 2048 + (size_t)w * 8 * 16384;
    const unsigned wdst = lb + (w >> 1) * 65536 + (w & 1) * 16384;
#pragma unroll 1
    for (int i = 0; i < 8; ++i) {
      gldsa16(wsrc + i * 16384, l16, wdst + i * 2048);
      gldsa16(wsrc + i * 16384, l16 + 1024, wdst + i * 2048 + 1024);
    }
    WAIT_V(0);
  }
  BAR();
  {
    const int kq = w >> 1;
    u32x4 fo[8];
#pragma unroll
    for (int i = 0; i < 8; ++i) fo[i] = BLD(rs, l16, WS_O + ((kq * 8 + i) * 64 + T0 + (w & 1)) * 1024, HO_AUX);
    const char* wp = lds + (kq >> 1) * 65536 + (kq & 1) * 16384 + l16;
    char* pp = lds + (kq >> 1) * 65536 + 32768 + (kq & 1) * 16384 + (w & 1) * 8192 + l16;
    BAR();
    f32x16 a0, a1;
#pragma unroll
    for (int q = 0; q < 4; ++q) {
      const f32x4 v0 = *(const f32x4*)(pp + q * 1024), v1 = *(const f32x4*)(pp + 4096 + q * 1024);
#pragma unroll
      for (int e = 0; e < 4; ++e) { a0[4 * q + e] = v0[e]; a1[4 * q + e] = v1[e]; }
    }
#define INCOMPLETE(v) __any(max(max(v[0], v[1]), max(v[2], v[3])) == 0xFFFFFFFFu)
#pragma unroll
    for (int i = 0; i < 8; ++i) {
      if (INCOMPLETE(fo[i])) {
#pragma unroll 1
        for (unsigned spins = 0; spins < (1u << 18); ++spins) {
          asm volatile("" ::: "memory");
#pragma unroll
          for (int i2 = i; i2 < 8; ++i2)
            if (i2 == i || INCOMPLETE(fo[i2]))
              fo[i2] = BLD(rs, l16, WS_O + ((kq * 8 + i2) * 64 + T0 + (w & 1)) * 1024, HO_AUX);
          if (!INCOMPLETE(fo[i])) break;
        }
      }
      a0 = MFMAU(*(const u32x4*)(wp + i * 2048), fo[i], a0);
      a1 = MFMAU(*(const u32x4*)(wp + i * 2048 + 1024), fo[i], a1);
    }
#pragma unroll
    for (int q = 0; q < 4; ++q) {
      f32x4 v0 = {a0[4 * q], a0[4 * q + 1], a0[4 * q + 2], a0[4 * q + 3]};
      f32x4 v1 = {a1[4 * q], a1[4 * q + 1], a1[4 * q + 2], a1[4 * q + 3]};
      *(f32x4*)(pp + q * 1024) = v0;
      *(f32x4*)(pp + 4096 + q * 1024) = v1;
    }
  }
  WAIT_V(0);
  BAR();
  {
    const int t = w & 3, tt = t & 1, n = t >> 1;
    const int tok = (T0 + tt) * 32 + r;
#pragma unroll
    for (int e = 0; e < 2; ++e) {
      const int q = 2 * (w >> 2) + e;
      const char* sp = lds + 32768 + tt * 8192 + n * 4096 + q * 1024 + l16;
      const f32x4 s = (*(const f32x4*)(sp) + *(const f32x4*)(sp + 16384)) +
                      (*(const f32x4*)(sp + 65536) + *(const f32x4*)(sp + 65536 + 16384));
      u32x2 o = {pk2(s[0], s[1]), pk2(s[2], s[3])};
      *(u32x2*)(out + (size_t)tok * D_ + (2 * cp + n) * 32 + 8 * q + 4 * h) = o;
    }
  }
}

extern "C" void kernel_launch(void* const* d_in, const int* in_sizes, int n_in, void* d_out, int out_size,
                              void* d_ws, size_t ws_size, hipStream_t stream) {
  const u16* x = (const u16*)d_in[0];
  const u16* wqkv = (const u16*)d_in[1];
  const u16* wo = (const u16*)d_in[2];
  const u16* w1 = (const u16*)d_in[4];
  const u16* b1 = (const u16*)d_in[5];
  const u16* w2 = (const u16*)d_in[6];
  const u16* gw = (const u16*)d_in[7];
  char* ws = (char*)d_ws;
  k1<<<256, 768, 0, stream>>>(x, wqkv, wo, w1, gw, ws);
  k2<<<256, 768, 0, stream>>>(ws, b1, w2, (u16*)d_out);
}
```

```cpp
#include <hip/hip_runtime.h>
#include <hip/hip_bf16.h>
#include <stdint.h>

typedef short bf16x8 __attribute__((ext_vector_type(8)));
typedef short s16x4 __attribute__((ext_vector_type(4)));
typedef float f32x16 __attribute__((ext_vector_type(16)));
typedef float f32x4 __attribute__((ext_vector_type(4)));
typedef float f32x2 __attribute__((ext_vector_type(2)));
typedef __bf16 bf16x2_t __attribute__((ext_vector_type(2)));
typedef unsigned u32x4 __attribute__((ext_vector_type(4)));
typedef unsigned u32x2 __attribute__((ext_vector_type(2)));
typedef unsigned short u16;
#define DI __device__ __forceinline__
#define MFMA(a, b, c) __builtin_amdgcn_mfma_f32_32x32x16_bf16((a), (b), (c), 0, 0, 0)
#define MFMAU(a, b, c) MFMA(__builtin_bit_cast(bf16x8, a), __builtin_bit_cast(bf16x8, b), c)
#define LAS __attribute__((address_space(3)))
#define GAS __attribute__((address_space(1)))

#define KSTAG ((idx + (idx >> 3) * 2) & 7)
#define HO_AUX 16
#define FFN1_PRIO 1
#define MISH_PRIO 1
#define FFN2_PRIO 0
#define SIDE_DELAY 8
constexpr int D_ = 512, F_ = 1376;
constexpr unsigned WS_Q = 0, WS_K = 2u << 20, WS_V = 4u << 20, WS_ACT = 6u << 20, WS_O = 12u << 20, WS_W2P = 14u << 20,
                   WS_WOP = 16u << 20, WS_GATE = 17u << 20, WS_KMAX = (17u << 20) + 16384, WS_CNT = (17u << 20) + 32768,
                   WS_W1P = 18u << 20, WS_XF = 20u << 20;

DI unsigned pk2(float a, float b) {
  f32x2 v = {a, b};
  bf16x2_t r = __builtin_convertvector(v, bf16x2_t);
  return __builtin_bit_cast(unsigned, r);
}
DI float bf_lo(unsigned u) { return __uint_as_float(u << 16); }
DI float bf_hi(unsigned u) { return __uint_as_float(u & 0xffff0000u); }
DI float rbf(float x) { return bf_lo(pk2(x, 0.f)); }
DI void gldsa16(const void* sbase, unsigned voff, unsigned dst) {
  unsigned keep;
  asm volatile("s_mov_b32 %0, m0\n\ts_mov_b32 m0, %3\n\ts_nop 0\n\tglobal_load_lds_dwordx4 %1, %2\n\ts_mov_b32 m0, %0"
               : "=&s"(keep) : "v"(voff), "s"(sbase), "s"(dst) : "memory");
}
DI s16x4 ldtr(const char* l) {
  typedef short v4i16_t __attribute__((ext_vector_type(4)));
  return __builtin_amdgcn_ds_read_tr16_b64_v4i16((LAS v4i16_t*)l);
}
#define WAIT_V(n) asm volatile("s_waitcnt vmcnt(" #n ") lgkmcnt(0)" ::: "memory")
#define BAR() do { asm volatile("" ::: "memory"); __builtin_amdgcn_s_barrier(); asm volatile("" ::: "memory"); } while (0)
template <int CTRL> DI float dppf(float v) {
  return __int_as_float(__builtin_amdgcn_update_dpp(0, __float_as_int(v), CTRL, 0xF, 0xF, true));
}
DI float rdl(float v, int l) { return __int_as_float(__builtin_amdgcn_readlane(__float_as_int(v), l)); }
DI float row16_max(float v) {
  v = fmaxf(v, dppf<0xB1>(v)); v = fmaxf(v, dppf<0x4E>(v)); v = fmaxf(v, dppf<0x141>(v)); v = fmaxf(v, dppf<0x140>(v));
  return v;
}
DI float row16_sum(float v) {
  v += dppf<0xB1>(v); v += dppf<0x4E>(v); v += dppf<0x141>(v); v += dppf<0x140>(v);
  return v;
}
DI float add_half(float v) {
  auto rr = __builtin_amdgcn_permlane32_swap(__float_as_uint(v), __float_as_uint(v), false, false);
  return __uint_as_float(rr[0]) + __uint_as_float(rr[1]);
}
DI float max_half(float v) {
  auto rr = __builtin_amdgcn_permlane32_swap(__float_as_uint(v), __float_as_uint(v), false, false);
  return fmaxf(__uint_as_float(rr[0]), __uint_as_float(rr[1]));
}
#define WS_RSRC(ws) __builtin_amdgcn_make_buffer_rsrc((void*)(ws), 0, 32u << 20, 0x00020000)
#define WST16(rs, off, v) __builtin_amdgcn_raw_buffer_store_b128(__builtin_bit_cast(u32x4, v), rs, (unsigned)(off), 0, 16)
#define BLD(rs, voff, soff, aux) __builtin_amdgcn_raw_buffer_load_b128(rs, voff, soff, aux)
#define RLX_AGENT __ATOMIC_RELAXED, __HIP_MEMORY_SCOPE_AGENT
#define RLX_WG __ATOMIC_RELAXED, __HIP_MEMORY_SCOPE_WORKGROUP

DI float mish_g(float h, float g) {
  const float w = __builtin_amdgcn_exp2f(h * 1.4426950408889634f);
  const float d = __builtin_fmaf(w, w + 2.f, 2.f);
  const float hg = h * g;
  return __builtin_fmaf(-2.f * hg, __builtin_amdgcn_rcpf(d), hg);
}
DI void wait_global(char* p, unsigned want) {
  for (unsigned spins = 0; spins < (1u << 17); ++spins) {
    if ((unsigned)__builtin_amdgcn_readfirstlane(__hip_atomic_load((GAS unsigned*)p, RLX_AGENT)) >= want) break;
    __builtin_amdgcn_s_sleep(2);
  }
  asm volatile("" ::: "memory");
}
DI void wait_lds(char* p, unsigned want) {
  while ((unsigned)__builtin_amdgcn_readfirstlane(__hip_atomic_load((LAS unsigned*)p, RLX_WG)) < want)
    __builtin_amdgcn_s_sleep(1);
  asm volatile("" ::: "memory");
}
DI unsigned add_lds(char* p, int lane) {
  unsigned old = 0;
  if (lane == 0) old = __hip_atomic_fetch_add((LAS unsigned*)p, 1u, RLX_WG);
  return (unsigned)__builtin_amdgcn_readfirstlane(old);
}

#define K1_LOOP_ASM \
  "s_barrier\n\t" \
  "ds_read_b128 v[48:51], v80\n\t" \
  "ds_read_b64_tr_b16 v[52:53], v84 offset:0\n\t" \
  "ds_read_b64_tr_b16 v[54:55], v84 offset:256\n\t" \
  "ds_read_b64_tr_b16 v[56:57], v84 offset:4096\n\t" \
  "ds_read_b64_tr_b16 v[58:59], v84 offset:4352\n\t" \
  "ds_read_b64_tr_b16 v[60:61], v84 offset:8192\n\t" \
  "ds_read_b64_tr_b16 v[62:63], v84 offset:8448\n\t" \
  "1:\n\t" \
  "ds_read_b128 v[64:67], v81\n\t" \
  "ds_read_b64_tr_b16 v[68:69], v84 offset:1024\n\t" \
  "ds_read_b64_tr_b16 v[70:71], v84 offset:1280\n\t" \
  "ds_read_b64_tr_b16 v[72:73], v84 offset:5120\n\t" \
  "ds_read_b64_tr_b16 v[74:75], v84 offset:5376\n\t" \
  "ds_read_b64_tr_b16 v[76:77], v84 offset:9216\n\t" \
  "ds_read_b64_tr_b16 v[78:79], v84 offset:9472\n\t" \
  "s_waitcnt lgkmcnt(7)\n\t" \
  "v_mfma_f32_32x32x16_bf16 v[0:15], v[52:55], v[48:51], v[0:15]\n\t" \
  "v_mfma_f32_32x32x16_bf16 v[16:31], v[56:59], v[48:51], v[16:31]\n\t" \
  "v_mfma_f32_32x32x16_bf16 v[32:47], v[48:51], v[60:63], v[32:47]\n\t" \
  "ds_read_b128 v[48:51], v82\n\t" \
  "ds_read_b64_tr_b16 v[52:53], v84 offset:2048\n\t" \
  "ds_read_b64_tr_b16 v[54:55], v84 offset:2304\n\t" \
  "ds_read_b64_tr_b16 v[56:57], v84 offset:6144\n\t" \
  "ds_read_b64_tr_b16 v[58:59], v84 offset:6400\n\t" \
  "ds_read_b64_tr_b16 v[60:61], v84 offset:10240\n\t" \
  "ds_read_b64_tr_b16 v[62:63], v84 offset:10496\n\t" \
  "s_waitcnt lgkmcnt(7)\n\t" \
  "v_mfma_f32_32x32x16_bf16 v[0:15], v[68:71], v[64:67], v[0:15]\n\t" \
  "v_mfma_f32_32x32x16_bf16 v[16:31], v[72:75], v[64:67], v[16:31]\n\t" \
  "v_mfma_f32_32x32x16_bf16 v[32:47], v[64:67], v[76:79], v[32:47]\n\t" \
  "ds_read_b128 v[64:67], v83\n\t" \
  "ds_read_b64_tr_b16 v[68:69], v84 offset:3072\n\t" \
  "ds_read_b64_tr_b16 v[70:71], v84 offset:3328\n\t" \
  "ds_read_b64_tr_b16 v[72:73], v84 offset:7168\n\t" \
  "ds_read_b64_tr_b16 v[74:75], v84 offset:7424\n\t" \
  "ds_read_b64_tr_b16 v[76:77], v84 offset:11264\n\t" \
  "ds_read_b64_tr_b16 v[78:79], v84 offset:11520\n\t" \
  "s_waitcnt lgkmcnt(7)\n\t" \
  "v_mfma_f32_32x32x16_bf16 v[0:15], v[52:55], v[48:51], v[0:15]\n\t" \
  "v_mfma_f32_32x32x16_bf16 v[16:31], v[56:59], v[48:51], v[16:31]\n\t" \
  "v_mfma_f32_32x32x16_bf16 v[32:47], v[48:51], v[60:63], v[32:47]\n\t" \
  "s_cmp_eq_u32 %[kt], 7\n\t" \
  "s_cbranch_scc1 9f\n\t" \
  "s_add_u32 %[kt], %[kt], 1\n\t" \
  "s_and_b32 %[t0], %[kt], 3\n\t" \
  "s_cmp_eq_u32 %[t0], 0\n\t" \
  "s_cselect_b32 %[t0], %[cmw], %[cst]\n\t" \
  "v_add_u32 v80, %[t0], v80\n\t" \
  "v_add_u32 v81, %[t0], v81\n\t" \
  "v_add_u32 v82, %[t0], v82\n\t" \
  "v_add_u32 v83, %[t0], v83\n\t" \
  "v_add_u32 v84, %[t0], v84\n\t" \
  "s_waitcnt lgkmcnt(0)\n\t" \
  "s_barrier\n\t" \
  "ds_read_b128 v[48:51], v80\n\t" \
  "ds_read_b64_tr_b16 v[52:53], v84 offset:0\n\t" \
  "ds_read_b64_tr_b16 v[54:55], v84 offset:256\n\t" \
  "ds_read_b64_tr_b16 v[56:57], v84 offset:4096\n\t" \
  "ds_read_b64_tr_b16 v[58:59], v84 offset:4352\n\t" \
  "ds_read_b64_tr_b16 v[60:61], v84 offset:8192\n\t" \
  "ds_read_b64_tr_b16 v[62:63], v84 offset:8448\n\t" \
  "v_mfma_f32_32x32x16_bf16 v[0:15], v[68:71], v[64:67], v[0:15]\n\t" \
  "v_mfma_f32_32x32x16_bf16 v[16:31], v[72:75], v[64:67], v[16:31]\n\t" \
  "v_mfma_f32_32x32x16_bf16 v[32:47], v[64:67], v[76:79], v[32:47]\n\t" \
  "s_branch 1b\n\t" \
  "9:\n\t" \
  "s_waitcnt lgkmcnt(0)\n\t" \
  "v_mfma_f32_32x32x16_bf16 v[0:15], v[68:71], v[64:67], v[0:15]\n\t" \
  "v_mfma_f32_32x32x16_bf16 v[16:31], v[72:75], v[64:67], v[16:31]\n\t" \
  "v_mfma_f32_32x32x16_bf16 v[32:47], v[64:67], v[76:79], v[32:47]\n\t" \
  "s_nop 15\n\t" \
  ""

constexpr int K1_ST = 28672, K1_SIDE = 4 * K1_ST;
__global__ __launch_bounds__(768) void k1(const u16* __restrict__ x, const u16* __restrict__ wqkv,
                                          const u16* __restrict__ wo, const u16* __restrict__ w1,
                                          const u16* __restrict__ gw,
                                          char* __restrict__ ws) {
  __shared__ __attribute__((aligned(16))) char lds[K1_SIDE + 4 * 6144 + 4096];
  const int bid = blockIdx.x, xcd = bid & 7, idx = bid >> 3;
  const int rb = (xcd & 1) * 8 + (idx & 7), j = (xcd >> 1) * 4 + (idx >> 3);
  const int tid = threadIdx.x, lane = tid & 63;
  const int w = __builtin_amdgcn_readfirstlane(tid >> 6);
  const int r = lane & 31, h = lane >> 5, m = w & 3;
  const unsigned lb = (unsigned)(size_t)(LAS char*)lds;
  const int tile = rb * 4 + m;
  const int q4 = (lane & 15) >> 2, p4 = lane & 3, grp = (lane >> 4) & 1;
  const int sw = (r >> 1) & 7;
  const auto rs = WS_RSRC(ws);
  const int u0 = bid * 4 + m;
  const int tok = rb * 128 + j * 8 + 2 * m;
  const int kq0 = KSTAG;
  if (w >= 8) {
    __builtin_amdgcn_s_sleep(SIDE_DELAY);
    const unsigned sl = lb + K1_SIDE + m * 6144;
    {
      const int u = min(u0, 959);
      const bool is1 = u < 704;
      const int uu = is1 ? u : u - 704;
      const int kb = is1 ? uu / 22 : uu >> 3, pr = is1 ? uu % 22 : uu & 7;
      const int ld = is1 ? F_ : D_;
      const u16* src = is1 ? w1 + (size_t)kb * 16 * F_ : wo + (size_t)kb * 16 * D_;
      const unsigned po = (unsigned)((lane >> 3) * ld + min(64 * pr + (lane & 7) * 8, ld - 8)) * 2u;
      gldsa16(src, po, sl);
      gldsa16(src + 8 * ld, po, sl + 1024);
    }
    gldsa16(x + (size_t)tok * D_, lane * 16, sl + 4096);
    gldsa16(x + (size_t)tok * D_, lane * 16 + 1024, sl + 5120);
    gldsa16(gw, lane * 16, lb + K1_SIDE + 24576 + m * 1024);
    if (bid == 0 && w == 8 && lane < 40) ((unsigned*)(ws + WS_CNT))[lane * 32] = 0;
    const int sstar = ((j >> 1) - kq0) & 7;
    const char* xsrc = lds + (sstar & 3) * K1_ST + m * 4096 + r * 128;
    const char* sp = lds + K1_SIDE + m * 6144;
    int job = -1;
#pragma unroll 1
    for (int s = 0; s < 8 || job < 2; ++s) {
      if (s < 8) {
        asm volatile("s_waitcnt lgkmcnt(0)" ::: "memory");
        BAR();
      } else if (job < 0) {
        WAIT_V(0);
        job = 0;
      }
      if (job < 0) {
        const unsigned ib = __builtin_amdgcn_s_getreg(7 | (31 << 11));
        if (((ib & 15u) | ((ib >> 18) & 48u)) == 0) { job = 0; asm volatile("" ::: "memory"); }
      }
      if (s == sstar) {
        const u32x4 xc0 = *(const u32x4*)(xsrc + (((4 * (j & 1) + h) ^ sw) * 16));
        const u32x4 xc1 = *(const u32x4*)(xsrc + (((4 * (j & 1) + 2 + h) ^ sw) * 16));
        WST16(rs, WS_XF + ((4 * (j >> 1) + 2 * (j & 1)) * 64 + tile) * 1024 + lane * 16, xc0);
        WST16(rs, WS_XF + ((4 * (j >> 1) + 2 * (j & 1) + 1) * 64 + tile) * 1024 + lane * 16, xc1);
      }
      if (job == 0) {
        const int c = 0, u = u0;
        if (u < 960) {
          const bool is1 = u < 704;
          const int uu = is1 ? u : u - 704;
          const int kb = is1 ? uu / 22 : uu >> 3, pr = is1 ? uu % 22 : uu & 7;
          const char* pp = sp + c * 2048 + ((is1 ? 8 : 4) * h + q4) * 128 + grp * 32 + p4 * 8;
          const int hi_off = is1 ? 512 : 1024;
#pragma unroll
          for (int n = 0; n < 2; ++n) {
            const s16x4 lo = ldtr(pp + n * 64), hi = ldtr(pp + n * 64 + hi_off);
            const bf16x8 f = __builtin_shufflevector(lo, hi, 0, 1, 2, 3, 4, 5, 6, 7);
            const int blk = 2 * pr + n;
            const unsigned dst = is1 ? WS_W1P + (kb * 43 + blk) * 1024 : WS_WOP + (kb * 16 + blk) * 1024;
            if (!is1 || blk < 43) WST16(rs, dst + lane * 16, f);
          }
        }
      }
      if (job == 1) {
        const u32x4 gg = *(const u32x4*)(lds + K1_SIDE + 24576 + m * 1024 + lane * 16);
#pragma unroll
        for (int t = 0; t < 2; ++t) {
          const u32x4 gx = *(const u32x4*)(sp + 4096 + t * 1024 + lane * 16);
          float sm = 0.f;
#pragma unroll
          for (int e = 0; e < 4; ++e) {
            sm += rbf(bf_lo(gx[e]) * bf_lo(gg[e]));
            sm += rbf(bf_hi(gx[e]) * bf_hi(gg[e]));
          }
          sm = row16_sum(sm);
          sm = (rdl(sm, 0) + rdl(sm, 16)) + (rdl(sm, 32) + rdl(sm, 48));
          sm = rbf(sm);
          const float g = rbf(__builtin_amdgcn_rcpf(1.f + __builtin_amdgcn_exp2f(-sm * 1.4426950408889634f)));
          if (lane == 0) ((float*)(ws + WS_GATE))[tok + t] = g;
        }
      }
      if (job >= 0) ++job;
    }
    return;
  }
  if (w >= 4) {
    const unsigned xo0 = (unsigned)((lane >> 3) * D_ + (((lane & 7) ^ ((lane >> 4) & 7)) * 8)) * 2u;
    const unsigned xo1 = (unsigned)((8 + (lane >> 3)) * D_ + (((lane & 7) ^ ((4 + (lane >> 4)) & 7)) * 8)) * 2u;
    const unsigned wqo = (unsigned)((lane >> 2) * 1536 + (lane & 3) * 8) * 2u;
    int kq = kq0;
    const char* xb = (const char*)(x + (size_t)(rb * 128 + 32 * m) * D_ + 64 * kq);
    const char* wqb = (const char*)(wqkv + (size_t)(16 * m + 64 * kq) * 1536 + 64 * j);
    const unsigned vo_ = wqo + 2048 - 64 * j;
    unsigned xl = lb + m * 4096, wl = lb + 16384 + m * 1024;
#define STAGE()                                                                                   \
  do {                                                                                            \
    gldsa16(wqb, wqo, wl);                                                                        \
    gldsa16(wqb, wqo + 64, wl + 4096);                                                            \
    gldsa16(wqb, vo_, wl + 8192);                                                                 \
    gldsa16(xb, xo0, xl);                                                                         \
    gldsa16(xb, xo1, xl + 1024);                                                                  \
    gldsa16(xb + 16 * D_ * 2, xo0, xl + 2048);                                                    \
    gldsa16(xb + 16 * D_ * 2, xo1, xl + 3072);                                                    \
    kq = (kq + 1) & 7;                                                                            \
    { const int dk_ = kq ? 128 : -896; xb += dk_; wqb += dk_ * 1536; }                            \
    const bool wrap_ = xl + K1_ST >= lb + 4 * K1_ST;                                              \
    xl += wrap_ ? 0u - 3u * K1_ST : K1_ST; wl += wrap_ ? 0u - 3u * K1_ST : K1_ST;                 \
  } while (0)
    STAGE();
    STAGE();
    WAIT_V(7); BAR();
    STAGE();
    WAIT_V(7); BAR();
    STAGE();
#pragma unroll 1
    for (int kt = 2; kt < 6; ++kt) {
      STAGE();
      WAIT_V(14); BAR();
    }
#undef STAGE
    WAIT_V(7); BAR();
    WAIT_V(0); BAR();
    return;
  }

  f32x16 acc[3];
#pragma unroll
  for (int b = 0; b < 3; ++b)
#pragma unroll
    for (int i = 0; i < 16; ++i) acc[b][i] = 0.f;
  {
    const unsigned xr0 = lb + m * 4096 + r * 128;
    unsigned xa0 = xr0 + ((0 + h) ^ sw) * 16, xa1 = xr0 + ((2 + h) ^ sw) * 16, xa2 = xr0 + ((4 + h) ^ sw) * 16,
             xa3 = xr0 + ((6 + h) ^ sw) * 16;
    unsigned wa = lb + 16384 + (8 * h + q4) * 64 + grp * 32 + p4 * 8;
    unsigned kt = 0, t0;
    asm volatile(K1_LOOP_ASM
                 : "+{v[0:15]}"(acc[0]), "+{v[16:31]}"(acc[1]), "+{v[32:47]}"(acc[2]), "+{v80}"(xa0),
                   "+{v81}"(xa1), "+{v82}"(xa2), "+{v83}"(xa3), "+{v84}"(wa), [kt] "+s"(kt), [t0] "=&s"(t0)
                 : [cst] "s"((unsigned)K1_ST), [cmw] "s"(0u - 3u * K1_ST)
                 : "memory", "scc", "v48", "v49", "v50", "v51", "v52", "v53", "v54", "v55", "v56", "v57",
                   "v58", "v59", "v60", "v61", "v62", "v63", "v64", "v65", "v66", "v67", "v68", "v69", "v70",
                   "v71", "v72", "v73", "v74", "v75", "v76", "v77", "v78", "v79");
  }
#pragma unroll
  for (int b = 0; b < 2; ++b) {
    const int c0 = 64 * j + 32 * b;
    const bool isq = c0 < 512;
    const float sc = isq ? 0.36067376022224085f : 1.0f;
    const int head0 = (c0 & 511) >> 4;
    const unsigned dst = isq ? WS_Q : WS_K;
#pragma unroll
    for (int hh = 0; hh < 2; ++hh) {
      u32x4 o;
#pragma unroll
      for (int e = 0; e < 4; ++e) o[e] = pk2(acc[b][8 * hh + 2 * e] * sc, acc[b][8 * hh + 2 * e + 1] * sc);
      WST16(rs, dst + (((head0 + hh) * 64 + tile) * 64 + lane) * 16, o);
      if (!isq) {
        float ss = 0.f;
#pragma unroll
        for (int e = 0; e < 4; ++e) {
          const float a = bf_lo(o[e]), c = bf_hi(o[e]);
          ss = __builtin_fmaf(a, a, ss);
          ss = __builtin_fmaf(c, c, ss);
        }
        ss = row16_max(add_half(ss));
        ss = fmaxf(rdl(ss, 0), rdl(ss, 16));
        if (lane == 0) ((float*)(ws + WS_KMAX))[(head0 + hh) * 64 + tile] = ss;
      }
    }
  }
  {
    const int head = 2 * j + (r >> 4), d = r & 15;
#pragma unroll
    for (int s = 0; s < 2; ++s) {
      u32x4 o;
#pragma unroll
      for (int e = 0; e < 4; ++e) o[e] = pk2(acc[2][8 * s + 2 * e], acc[2][8 * s + 2 * e + 1]);
      WST16(rs, WS_V + (head * 64 + tile) * 1024 + s * 512 + h * 256 + d * 16, o);
    }
  }
}

#define ATTN_LOOP_ASM \
  "1:\n\t" \
  "v_exp_f32 v32, v32\n\t" \
  "v_exp_f32 v33, v33\n\t" \
  "v_exp_f32 v34, v34\n\t" \
  "v_cvt_pk_bf16_f32 v88, v32, v33\n\t" \
  "v_mfma_f32_32x32x16_bf16 v[0:15], v[84:87], v[92:95], v[0:15]\n\t" \
  "s_mov_b64 exec, %[mask]\n\t" \
  "ds_read_b128 v[80:83], v97 offset:1024\n\t" \
  "ds_read_b128 v[84:87], v97 offset:1536\n\t" \
  "s_mov_b64 exec, -1\n\t" \
  "v_exp_f32 v35, v35\n\t" \
  "v_exp_f32 v36, v36\n\t" \
  "v_cvt_pk_bf16_f32 v89, v34, v35\n\t" \
  "v_exp_f32 v37, v37\n\t" \
  "v_exp_f32 v38, v38\n\t" \
  "v_cvt_pk_bf16_f32 v90, v36, v37\n\t" \
  "v_exp_f32 v39, v39\n\t" \
  "v_exp_f32 v40, v40\n\t" \
  "v_cvt_pk_bf16_f32 v91, v38, v39\n\t" \
  "v_exp_f32 v41, v41\n\t" \
  "v_exp_f32 v42, v42\n\t" \
  "v_mfma_f32_32x32x16_bf16 v[0:15], v[72:75], v[88:91], v[0:15]\n\t" \
  "v_cvt_pk_bf16_f32 v92, v40, v41\n\t" \
  "v_exp_f32 v43, v43\n\t" \
  "v_exp_f32 v44, v44\n\t" \
  "v_cvt_pk_bf16_f32 v93, v42, v43\n\t" \
  "v_exp_f32 v45, v45\n\t" \
  "v_exp_f32 v46, v46\n\t" \
  "v_cvt_pk_bf16_f32 v94, v44, v45\n\t" \
  "v_exp_f32 v47, v47\n\t" \
  "s_waitcnt lgkmcnt(0)\n\t" \
  "v_cvt_pk_bf16_f32 v95, v46, v47\n\t" \
  "v_mfma_f32_32x32x16_bf16 v[32:47], v[68:71], v[64:67], v[16:31]\n\t" \
  "ds_read_b128 v[68:71], v96 offset:3072\n\t" \
  "v_exp_f32 v48, v48\n\t" \
  "v_exp_f32 v49, v49\n\t" \
  "v_exp_f32 v50, v50\n\t" \
  "v_cvt_pk_bf16_f32 v88, v48, v49\n\t" \
  "v_mfma_f32_32x32x16_bf16 v[0:15], v[76:79], v[92:95], v[0:15]\n\t" \
  "s_mov_b64 exec, %[mask]\n\t" \
  "ds_read_b128 v[72:75], v97 offset:2048\n\t" \
  "ds_read_b128 v[76:79], v97 offset:2560\n\t" \
  "s_mov_b64 exec, -1\n\t" \
  "v_exp_f32 v51, v51\n\t" \
  "v_exp_f32 v52, v52\n\t" \
  "v_cvt_pk_bf16_f32 v89, v50, v51\n\t" \
  "v_exp_f32 v53, v53\n\t" \
  "v_exp_f32 v54, v54\n\t" \
  "v_cvt_pk_bf16_f32 v90, v52, v53\n\t" \
  "v_exp_f32 v55, v55\n\t" \
  "v_exp_f32 v56, v56\n\t" \
  "v_cvt_pk_bf16_f32 v91, v54, v55\n\t" \
  "v_exp_f32 v57, v57\n\t" \
  "v_exp_f32 v58, v58\n\t" \
  "v_mfma_f32_32x32x16_bf16 v[0:15], v[80:83], v[88:91], v[0:15]\n\t" \
  "v_cvt_pk_bf16_f32 v92, v56, v57\n\t" \
  "v_exp_f32 v59, v59\n\t" \
  "v_exp_f32 v60, v60\n\t" \
  "v_cvt_pk_bf16_f32 v93, v58, v59\n\t" \
  "v_exp_f32 v61, v61\n\t" \
  "v_exp_f32 v62, v62\n\t" \
  "v_cvt_pk_bf16_f32 v94, v60, v61\n\t" \
  "v_exp_f32 v63, v63\n\t" \
  "s_waitcnt lgkmcnt(0)\n\t" \
  "v_cvt_pk_bf16_f32 v95, v62, v63\n\t" \
  "v_mfma_f32_32x32x16_bf16 v[48:63], v[68:71], v[64:67], v[16:31]\n\t" \
  "ds_read_b128 v[68:71], v96 offset:4096\n\t" \
  "v_exp_f32 v32, v32\n\t" \
  "v_exp_f32 v33, v33\n\t" \
  "v_exp_f32 v34, v34\n\t" \
  "v_cvt_pk_bf16_f32 v88, v32, v33\n\t" \
  "v_mfma_f32_32x32x16_bf16 v[0:15], v[84:87], v[92:95], v[0:15]\n\t" \
  "s_mov_b64 exec, %[mask]\n\t" \
  "ds_read_b128 v[80:83], v97 offset:3072\n\t" \
  "ds_read_b128 v[84:87], v97 offset:3584\n\t" \
  "s_mov_b64 exec, -1\n\t" \
  "v_exp_f32 v35, v35\n\t" \
  "v_exp_f32 v36, v36\n\t" \
  "v_cvt_pk_bf16_f32 v89, v34, v35\n\t" \
  "v_exp_f32 v37, v37\n\t" \
  "v_exp_f32 v38, v38\n\t" \
  "v_cvt_pk_bf16_f32 v90, v36, v37\n\t" \
  "v_exp_f32 v39, v39\n\t" \
  "v_exp_f32 v40, v40\n\t" \
  "v_cvt_pk_bf16_f32 v91, v38, v39\n\t" \
  "v_exp_f32 v41, v41\n\t" \
  "v_exp_f32 v42, v42\n\t" \
  "v_mfma_f32_32x32x16_bf16 v[0:15], v[72:75], v[88:91], v[0:15]\n\t" \
  "v_cvt_pk_bf16_f32 v92, v40, v41\n\t" \
  "v_exp_f32 v43, v43\n\t" \
  "v_exp_f32 v44, v44\n\t" \
  "v_cvt_pk_bf16_f32 v93, v42, v43\n\t" \
  "v_exp_f32 v45, v45\n\t" \
  "v_exp_f32 v46, v46\n\t" \
  "v_cvt_pk_bf16_f32 v94, v44, v45\n\t" \
  "v_exp_f32 v47, v47\n\t" \
  "s_waitcnt lgkmcnt(0)\n\t" \
  "v_cvt_pk_bf16_f32 v95, v46, v47\n\t" \
  "v_mfma_f32_32x32x16_bf16 v[32:47], v[68:71], v[64:67], v[16:31]\n\t" \
  "ds_read_b128 v[68:71], v96 offset:5120\n\t" \
  "v_exp_f32 v48, v48\n\t" \
  "v_exp_f32 v49, v49\n\t" \
  "v_exp_f32 v50, v50\n\t" \
  "v_cvt_pk_bf16_f32 v88, v48, v49\n\t" \
  "v_mfma_f32_32x32x16_bf16 v[0:15], v[76:79], v[92:95], v[0:15]\n\t" \
  "s_mov_b64 exec, %[mask]\n\t" \
  "ds_read_b128 v[72:75], v97 offset:4096\n\t" \
  "ds_read_b128 v[76:79], v97 offset:4608\n\t" \
  "s_mov_b64 exec, -1\n\t" \
  "v_exp_f32 v51, v51\n\t" \
  "v_exp_f32 v52, v52\n\t" \
  "v_cvt_pk_bf16_f32 v89, v50, v51\n\t" \
  "v_exp_f32 v53, v53\n\t" \
  "v_exp_f32 v54, v54\n\t" \
  "v_cvt_pk_bf16_f32 v90, v52, v53\n\t" \
  "v_exp_f32 v55, v55\n\t" \
  "v_exp_f32 v56, v56\n\t" \
  "v_cvt_pk_bf16_f32 v91, v54, v55\n\t" \
  "v_exp_f32 v57, v57\n\t" \
  "v_exp_f32 v58, v58\n\t" \
  "v_mfma_f32_32x32x16_bf16 v[0:15], v[80:83], v[88:91], v[0:15]\n\t" \
  "v_cvt_pk_bf16_f32 v92, v56, v57\n\t" \
  "v_exp_f32 v59, v59\n\t" \
  "v_exp_f32 v60, v60\n\t" \
  "v_cvt_pk_bf16_f32 v93, v58, v59\n\t" \
  "v_exp_f32 v61, v61\n\t" \
  "v_exp_f32 v62, v62\n\t" \
  "v_cvt_pk_bf16_f32 v94, v60, v61\n\t" \
  "v_exp_f32 v63, v63\n\t" \
  "s_waitcnt lgkmcnt(0)\n\t" \
  "v_cvt_pk_bf16_f32 v95, v62, v63\n\t" \
  "v_mfma_f32_32x32x16_bf16 v[48:63], v[68:71], v[64:67], v[16:31]\n\t" \
  "ds_read_b128 v[68:71], v96 offset:6144\n\t" \
  "v_add_u32 v96, 0x1000, v96\n\t" \
  "v_add_u32 v97, 0x1000, v97\n\t" \
  "s_sub_u32 %[cnt], %[cnt], 1\n\t" \
  "s_and_b32 %[tmp], %[cnt], 13\n\t" \
  "s_cmp_lg_u32 %[tmp], 12\n\t" \
  "s_cbranch_scc1 2f\n\t" \
  "3:\n\t" \
  "ds_read_b32 v98, v99\n\t" \
  "s_waitcnt lgkmcnt(0)\n\t" \
  "v_readfirstlane_b32 %[tmp], v98\n\t" \
  "s_cmp_ge_u32 %[tmp], 4\n\t" \
  "s_cbranch_scc1 4f\n\t" \
  "s_sleep 1\n\t" \
  "s_branch 3b\n\t" \
  "4:\n\t" \
  "v_add_u32 v99, 4, v99\n\t" \
  "2:\n\t" \
  "s_cmp_lg_u32 %[cnt], 7\n\t" \
  "s_cbranch_scc1 5f\n\t" \
  "v_mov_b32 v98, 1\n\t" \
  "s_mov_b64 exec, 1\n\t" \
  "ds_add_u32 v99, v98\n\t" \
  "s_mov_b64 exec, -1\n\t" \
  "5:\n\t" \
  "s_cmp_lg_u32 %[cnt], 15\n\t" \
  "s_cbranch_scc1 6f\n\t" \
  "v_mov_b32 v98, 1\n\t" \
  "s_waitcnt vmcnt(0)\n\t" \
  "s_mov_b64 exec, 1\n\t" \
  "ds_add_u32 v100, v98\n\t" \
  "s_mov_b64 exec, -1\n\t" \
  "6:\n\t" \
  "s_cmp_lg_u32 %[cnt], 0\n\t" \
  "s_cbranch_scc1 1b\n\t" \
  "s_waitcnt lgkmcnt(0)\n\t" \
  "s_nop 15\n\t" \
  ""

constexpr int L2_FLAG = 131072;
__global__ __launch_bounds__(768) void k2(char* __restrict__ ws, const u16* __restrict__ b1,
                                          const u16* __restrict__ w2, u16* __restrict__ out) {
  __shared__ __attribute__((aligned(16))) char lds[L2_FLAG + 256 + 8192];
  const int bid = blockIdx.x, xcd = bid & 7, idx = bid >> 3;
  const int head = xcd * 4 + (idx & 3), qb = idx >> 2;
  const int tp = xcd >> 1, cp = (xcd & 1) * 4 + (idx & 3);
  const int T0 = qb * 8 + 2 * tp;
  const int tid = threadIdx.x, lane = tid & 63;
  const int w = __builtin_amdgcn_readfirstlane(tid >> 6);
  const int r = lane & 31, h = lane >> 5;
  const unsigned lb = (unsigned)(size_t)(LAS char*)lds;
  const unsigned l16 = lane * 16;
  const auto rs = WS_RSRC(ws);
  char* cnt = ws + WS_CNT;
  char* cntF = cnt + (8 + qb * 4 + tp) * 128;
  if (w >= 8) {
    const int L = w - 8;
    {
      const char* ksrc = ws + WS_K + (size_t)(head * 64 + L) * 1024;
      const char* vsrc = ws + WS_V + (size_t)(head * 64 + L) * 1024;
      const unsigned kdst = lb + L * 1024, vdst = lb + 65536 + L * 1024;
#pragma unroll
      for (int c = 0; c < 3; ++c) { gldsa16(ksrc + c * 4096, l16, kdst + c * 4096); gldsa16(vsrc + c * 4096, l16, vdst + c * 4096); }
      if (tid >= 512 && tid < 520) ((volatile LAS unsigned*)(lds + L2_FLAG))[tid - 512] = 0;
      WAIT_V(0); BAR();
#pragma unroll 1
      for (int c = 3; c < 16; ++c) {
        gldsa16(ksrc + c * 4096, l16, kdst + c * 4096); gldsa16(vsrc + c * 4096, l16, vdst + c * 4096);
      }
    }
    const int pu = bid * 4 + L, puc = min(pu, 687);
    {
      const u16* src = w2 + (size_t)(puc >> 3) * 16 * D_ + (puc & 7) * 64;
      const unsigned po = (unsigned)((lane >> 3) * D_ + (lane & 7) * 8) * 2u;
      gldsa16(src, po, lb + L2_FLAG + 256 + L * 2048);
      gldsa16(src + 8 * D_, po, lb + L2_FLAG + 256 + L * 2048 + 1024);
    }
    {
      const int T = T0 + (L & 1);
      const int f0 = cp + 24 * (L >> 1);
      const int fbc2 = min(f0 + 16, 42);
      const auto rx = __builtin_amdgcn_make_buffer_rsrc((void*)(ws + WS_XF + T * 1024), 0, 31 * 65536 + 1024, 0x00020000);
      const auto rw = __builtin_amdgcn_make_buffer_rsrc((void*)(ws + WS_W1P), 0, 32 * 43 * 1024, 0x00020000);
      const unsigned wv0 = l16 + f0 * 1024, wv1 = l16 + (f0 + 8) * 1024, wv2 = f0 + 16 < 43 ? l16 + (f0 + 16) * 1024 : 0x40000000u;
      constexpr int PD = 6;
      u32x4 fx[PD], fw[PD][3];
#pragma unroll
      for (int i = 0; i < PD; ++i) {
        fx[i] = BLD(rx, l16, i * 65536, 0);
        fw[i][0] = BLD(rw, wv0, i * 44032, 0); fw[i][1] = BLD(rw, wv1, i * 44032, 0); fw[i][2] = BLD(rw, wv2, i * 44032, 0);
      }
      WAIT_V(40);
      add_lds(lds + L2_FLAG, lane);
      WAIT_V(24);
      add_lds(lds + L2_FLAG + 4, lane);
      {
        const char* pp = lds + L2_FLAG + 256 + L * 2048 + (4 * h + ((lane & 15) >> 2)) * 128 + ((lane >> 4) & 1) * 32 + (lane & 3) * 8;
#pragma unroll
        for (int n = 0; n < 2; ++n) {
          const s16x4 lo = ldtr(pp + n * 64), hi = ldtr(pp + n * 64 + 1024);
          const bf16x8 f = __builtin_shufflevector(lo, hi, 0, 1, 2, 3, 4, 5, 6, 7);
          if (pu < 688) WST16(rs, WS_W2P + (pu * 2 + n) * 1024 + l16, f);
        }
      }
      __builtin_amdgcn_s_setprio(FFN1_PRIO);
      f32x16 a[3];
#pragma unroll
      for (int g = 0; g < 4; ++g) {
        const u32x2 b0 = *(const u32x2*)(b1 + 32 * f0 + 8 * g + 4 * h);
        const u32x2 b1_ = *(const u32x2*)(b1 + 32 * (f0 + 8) + 8 * g + 4 * h);
        const u32x2 b2 = *(const u32x2*)(b1 + 32 * fbc2 + 8 * g + 4 * h);
        a[0][4 * g] = bf_lo(b0[0]); a[0][4 * g + 1] = bf_hi(b0[0]); a[0][4 * g + 2] = bf_lo(b0[1]); a[0][4 * g + 3] = bf_hi(b0[1]);
        a[1][4 * g] = bf_lo(b1_[0]); a[1][4 * g + 1] = bf_hi(b1_[0]); a[1][4 * g + 2] = bf_lo(b1_[1]); a[1][4 * g + 3] = bf_hi(b1_[1]);
        a[2][4 * g] = bf_lo(b2[0]); a[2][4 * g + 1] = bf_hi(b2[0]); a[2][4 * g + 2] = bf_lo(b2[1]); a[2][4 * g + 3] = bf_hi(b2[1]);
      }
      __builtin_amdgcn_sched_barrier(0);
      WAIT_V(0);
#pragma unroll 1
      for (int k0 = PD; k0 < 12 + PD; k0 += PD) {
#pragma unroll
        for (int i = 0; i < PD; ++i) {
          a[0] = MFMAU(fw[i][0], fx[i], a[0]);
          a[1] = MFMAU(fw[i][1], fx[i], a[1]);
          a[2] = MFMAU(fw[i][2], fx[i], a[2]);
          fx[i] = BLD(rx, l16, (k0 + i) * 65536, 0);
          fw[i][0] = BLD(rw, wv0, (k0 + i) * 44032, 0); fw[i][1] = BLD(rw, wv1, (k0 + i) * 44032, 0);
          fw[i][2] = BLD(rw, wv2, (k0 + i) * 44032, 0);
          __builtin_amdgcn_sched_barrier(0);
        }
      }
      wait_lds(lds + L2_FLAG + 24, 8);
      if (add_lds(lds + L2_FLAG + 16, lane) == 3 && lane == 0) __hip_atomic_fetch_add((GAS unsigned*)(cnt + xcd * 128), 1u, RLX_AGENT);
      __builtin_amdgcn_sched_barrier(0);
#pragma unroll 1
      for (int k0 = 12 + PD; k0 < 30 + PD; k0 += PD) {
#pragma unroll
        for (int i = 0; i < PD; ++i) {
          a[0] = MFMAU(fw[i][0], fx[i], a[0]);
          a[1] = MFMAU(fw[i][1], fx[i], a[1]);
          a[2] = MFMAU(fw[i][2], fx[i], a[2]);
          fx[i] = BLD(rx, l16, (k0 + i) * 65536, 0);
          fw[i][0] = BLD(rw, wv0, (k0 + i) * 44032, 0); fw[i][1] = BLD(rw, wv1, (k0 + i) * 44032, 0);
          fw[i][2] = BLD(rw, wv2, (k0 + i) * 44032, 0);
          __builtin_amdgcn_sched_barrier(0);
        }
      }
#pragma unroll
      for (int i = 0; i < 2; ++i) {
        a[0] = MFMAU(fw[i][0], fx[i], a[0]);
        a[1] = MFMAU(fw[i][1], fx[i], a[1]);
        a[2] = MFMAU(fw[i][2], fx[i], a[2]);
      }
      const float g1 = rbf(1.f - ((const float*)(ws + WS_GATE))[T * 32 + r]);
      __builtin_amdgcn_s_setprio(MISH_PRIO);
#pragma unroll
      for (int b = 0; b < 3; ++b) {
        if (f0 + 8 * b < 43) {
#pragma unroll
          for (int hh = 0; hh < 2; ++hh) {
            u32x4 o;
#pragma unroll
            for (int gq = 0; gq < 2; ++gq) {
              const int g = 2 * hh + gq;
              const float h0 = a[b][4 * g + 0], h1 = a[b][4 * g + 1], h2 = a[b][4 * g + 2], h3 = a[b][4 * g + 3];
              o[2 * gq] = pk2(mish_g(h0, g1), mish_g(h1, g1));
              o[2 * gq + 1] = pk2(mish_g(h2, g1), mish_g(h3, g1));
            }
            WST16(rs, WS_ACT + ((2 * (f0 + 8 * b) + hh) * 64 + T) * 1024 + l16, o);
          }
        }
      }
      WAIT_V(0);
      if (add_lds(lds + L2_FLAG + 12, lane) == 3 && lane == 0) __hip_atomic_fetch_add((GAS unsigned*)cntF, 1u, RLX_AGENT);
    }
    {
      const int klast = min(22 * L + 21, 85);
      const auto ra = __builtin_amdgcn_make_buffer_rsrc((void*)(ws + WS_ACT + T0 * 1024), 0, klast * 65536 + 2048, 0x00020000);
      const auto rw = __builtin_amdgcn_make_buffer_rsrc((void*)(ws + WS_W2P + cp * 2048), 0, klast * 16384 + 2048, 0x00020000);
      constexpr int PD = 5;
      u32x4 fa[PD][2], fw[PD][2];
      const int kb0 = 22 * L;
      if (L == 0) {
        char* pw = lane < 8 ? cnt + lane * 128 : cntF;
        const unsigned want = lane < 8 ? 32u : 8u;
        wait_global(cntF, 8);
        for (unsigned spins = 0; spins < (1u << 17); ++spins) {
          const unsigned v = __hip_atomic_load((GAS unsigned*)pw, RLX_AGENT);
          if (__all(v >= want)) break;
          __builtin_amdgcn_s_sleep(2);
        }
        asm volatile("" ::: "memory");
        add_lds(lds + L2_FLAG + 20, lane);
      } else {
        wait_lds(lds + L2_FLAG + 20, 1);
      }
      __builtin_amdgcn_s_setprio(FFN2_PRIO);
#pragma unroll
      for (int i = 0; i < PD; ++i) {
        fa[i][0] = BLD(ra, l16, (kb0 + i) * 65536, HO_AUX); fa[i][1] = BLD(ra, l16 + 1024, (kb0 + i) * 65536, HO_AUX);
        fw[i][0] = BLD(rw, l16, (kb0 + i) * 16384, HO_AUX); fw[i][1] = BLD(rw, l16 + 1024, (kb0 + i) * 16384, HO_AUX);
        __builtin_amdgcn_sched_barrier(0);
      }
      f32x16 b[4];
#pragma unroll
      for (int t = 0; t < 4; ++t)
#pragma unroll
        for (int i = 0; i < 16; ++i) b[t][i] = 0.f;
      __builtin_amdgcn_sched_barrier(0);
#pragma unroll 1
      for (int k0 = PD; k0 < 20 + PD; k0 += PD) {
#pragma unroll
        for (int i = 0; i < PD; ++i) {
#pragma unroll
          for (int t = 0; t < 4; ++t) b[t] = MFMAU(fw[i][t >> 1], fa[i][t & 1], b[t]);
          const int kn = kb0 + k0 + i;
          fa[i][0] = BLD(ra, l16, kn * 65536, HO_AUX); fa[i][1] = BLD(ra, l16 + 1024, kn * 65536, HO_AUX);
          fw[i][0] = BLD(rw, l16, kn * 16384, HO_AUX); fw[i][1] = BLD(rw, l16 + 1024, kn * 16384, HO_AUX);
          __builtin_amdgcn_sched_barrier(0);
        }
      }
#pragma unroll
      for (int i = 0; i < 2; ++i)
#pragma unroll
        for (int t = 0; t < 4; ++t) b[t] = MFMAU(fw[i][t >> 1], fa[i][t & 1], b[t]);
      WAIT_V(0);
      BAR();
      char* bs = lds + (L >> 1) * 65536 + 32768 + (L & 1) * 16384 + l16;
#pragma unroll
      for (int t = 0; t < 4; ++t)
#pragma unroll
        for (int q = 0; q < 4; ++q) {
          f32x4 v = {b[t][4 * q], b[t][4 * q + 1], b[t][4 * q + 2], b[t][4 * q + 3]};
          *(f32x4*)(bs + (t & 1) * 8192 + (t >> 1) * 4096 + q * 1024) = v;
        }
      WAIT_V(0);
      BAR();
      BAR();
    }
    return;
  }

  const int qt = qb * 8 + w;
  bf16x8 qf = *(const bf16x8*)(ws + WS_Q + (size_t)(head * 64 + qt) * 1024 + l16);
  float km = ((const float*)(ws + WS_KMAX))[head * 64 + lane];
  const float gq = ((const float*)(ws + WS_GATE))[qt * 32 + r];
  km = row16_max(km);
  km = fmaxf(fmaxf(rdl(km, 0), rdl(km, 16)), fmaxf(rdl(km, 32), rdl(km, 48)));
  float qs = 0.f;
  {
    const u32x4 qu = __builtin_bit_cast(u32x4, qf);
#pragma unroll
    for (int e = 0; e < 4; ++e) {
      const float a = bf_lo(qu[e]), c = bf_hi(qu[e]);
      qs = __builtin_fmaf(a, a, qs);
      qs = __builtin_fmaf(c, c, qs);
    }
    qs = add_half(qs);
  }
  float mb = __builtin_amdgcn_sqrtf(qs * km) * 1.0001f;
  asm volatile("" : "+v"(mb) : "v"(gq) : "memory");
  f32x16 negm, O;
#pragma unroll
  for (int i = 0; i < 16; ++i) { negm[i] = -mb; O[i] = 0.f; }

  const char* kp = lds + lane * 16;
  const char* vp = lds + 65536 + h * 256 + (lane & 15) * 16;
  const bool vact = (lane & 16) == 0;
  const short one = ((lane & 27) == 16) ? (short)0x3F80 : (short)0;
  bf16x8 va0, va1, vb0, vb1;
#pragma unroll
  for (int i = 0; i < 8; ++i) { va0[i] = one; va1[i] = one; vb0[i] = one; vb1[i] = one; }
  u32x4 p0 = {0, 0, 0, 0}, p1 = {0, 0, 0, 0};
  unsigned kaddr = lb + l16, vaddr = lb + 65536 + h * 256 + (lane & 15) * 16;
  const unsigned long long vmask = 0x0000FFFF0000FFFFull;

  {
    const u32x4 ones = {0xFFFFFFFFu, 0xFFFFFFFFu, 0xFFFFFFFFu, 0xFFFFFFFFu};
    WST16(rs, WS_O + (head * 64 + qt) * 1024 + l16, ones);
  }
  BAR();
  f32x16 Sa = MFMA(*(const bf16x8*)(kp), qf, negm);
  f32x16 Sb = MFMA(*(const bf16x8*)(kp + 1024), qf, negm);
  bf16x8 kf = *(const bf16x8*)(kp + 2048);
  if (vact) { va0 = *(const bf16x8*)(vp); va1 = *(const bf16x8*)(vp + 512); }
  {
    unsigned cnt = 16, tmp, fa_ = lb + L2_FLAG;
    asm volatile("s_nop 15\n\t" ATTN_LOOP_ASM
                 : "+{v[0:15]}"(O), "+{v[32:47]}"(Sa), "+{v[48:63]}"(Sb), "+{v[68:71]}"(kf),
                   "+{v[72:75]}"(va0), "+{v[76:79]}"(va1), "+{v[80:83]}"(vb0), "+{v[84:87]}"(vb1),
                   "+{v[88:91]}"(p0), "+{v[92:95]}"(p1), "+{v96}"(kaddr), "+{v97}"(vaddr), [cnt] "+s"(cnt),
                   [tmp] "=&s"(tmp), "+{v99}"(fa_)
                 : "{v[16:31]}"(negm), "{v[64:67]}"(qf), [mask] "s"(vmask), "{v100}"(lb + L2_FLAG + 24)
                 : "memory", "scc", "v98");
  }
  O = MFMA(vb1, __builtin_bit_cast(bf16x8, p1), O);

  float l = O[8];
  if (__any(!(l >= 8.271806125530277e-25f))) {
    float m = -INFINITY;
    f32x16 zero;
#pragma unroll
    for (int i = 0; i < 16; ++i) { zero[i] = 0.f; O[i] = 0.f; }
    const char* kg = ws + WS_K + (size_t)head * 65536 + l16;
    const char* vg = ws + WS_V + (size_t)head * 65536 + h * 256 + (lane & 15) * 16;
#pragma unroll 1
    for (int t = 0; t < 64; ++t) {
      f32x16 Sx = MFMA(*(const bf16x8*)(kg + t * 1024), qf, zero);
      float mx = Sx[0];
#pragma unroll
      for (int i = 1; i < 16; ++i) mx = fmaxf(mx, Sx[i]);
      mx = max_half(mx);
      const float mn = fmaxf(m, mx);
      const float alpha = __builtin_amdgcn_exp2f(m - mn);
      m = mn;
#pragma unroll
      for (int i = 0; i < 16; ++i) { O[i] *= alpha; Sx[i] -= mn; }
      if (vact) { va0 = *(const bf16x8*)(vg + t * 1024); va1 = *(const bf16x8*)(vg + t * 1024 + 512); }
#pragma unroll
      for (int e = 0; e < 4; ++e) {
        p0[e] = pk2(__builtin_amdgcn_exp2f(Sx[2 * e]), __builtin_amdgcn_exp2f(Sx[2 * e + 1]));
        p1[e] = pk2(__builtin_amdgcn_exp2f(Sx[8 + 2 * e]), __builtin_amdgcn_exp2f(Sx[8 + 2 * e + 1]));
      }
      O = MFMAU(va0, p0, O);
      O = MFMAU(va1, p1, O);
    }
    l = O[8];
  }
  {
    const float rl = gq / l;
    u32x4 o;
#pragma unroll
    for (int e = 0; e < 4; ++e) o[e] = pk2(O[2 * e] * rl, O[2 * e + 1] * rl);
    WST16(rs, WS_O + (head * 64 + qt) * 1024 + l16, o);
  }
  if (w < 4) {
    wait_lds(lds + L2_FLAG + 8, 8);
    const char* wsrc = ws + WS_WOP + cp * 2048 + (size_t)w * 8 * 16384;
    const unsigned wdst = lb + (w >> 1) * 65536 + (w & 1) * 16384;
#pragma unroll 1
    for (int i = 0; i < 8; ++i) {
      gldsa16(wsrc + i * 16384, l16, wdst + i * 2048);
      gldsa16(wsrc + i * 16384, l16 + 1024, wdst + i * 2048 + 1024);
    }
    WAIT_V(0);
  }
  BAR();
  {
    const int kq = w >> 1;
    u32x4 fo[8];
#pragma unroll
    for (int i = 0; i < 8; ++i) fo[i] = BLD(rs, l16, WS_O + ((kq * 8 + i) * 64 + T0 + (w & 1)) * 1024, HO_AUX);
    const char* wp = lds + (kq >> 1) * 65536 + (kq & 1) * 16384 + l16;
    char* pp = lds + (kq >> 1) * 65536 + 32768 + (kq & 1) * 16384 + (w & 1) * 8192 + l16;
    BAR();
    f32x16 a0, a1;
#pragma unroll
    for (int q = 0; q < 4; ++q) {
      const f32x4 v0 = *(const f32x4*)(pp + q * 1024), v1 = *(const f32x4*)(pp + 4096 + q * 1024);
#pragma unroll
      for (int e = 0; e < 4; ++e) { a0[4 * q + e] = v0[e]; a1[4 * q + e] = v1[e]; }
    }
#define INCOMPLETE(v) __any(max(max(v[0], v[1]), max(v[2], v[3])) == 0xFFFFFFFFu)
#pragma unroll
    for (int i = 0; i < 8; ++i) {
      if (INCOMPLETE(fo[i])) {
#pragma unroll 1
        for (unsigned spins = 0; spins < (1u << 18); ++spins) {
          asm volatile("" ::: "memory");
#pragma unroll
          for (int i2 = i; i2 < 8; ++i2)
            if (i2 == i || INCOMPLETE(fo[i2]))
              fo[i2] = BLD(rs, l16, WS_O + ((kq * 8 + i2) * 64 + T0 + (w & 1)) * 1024, HO_AUX);
          if (!INCOMPLETE(fo[i])) break;
        }
      }
      a0 = MFMAU(*(const u32x4*)(wp + i * 2048), fo[i], a0);
      a1 = MFMAU(*(const u32x4*)(wp + i * 2048 + 1024), fo[i], a1);
    }
#pragma unroll
    for (int q = 0; q < 4; ++q) {
      f32x4 v0 = {a0[4 * q], a0[4 * q + 1], a0[4 * q + 2], a0[4 * q + 3]};
      f32x4 v1 = {a1[4 * q], a1[4 * q + 1], a1[4 * q + 2], a1[4 * q + 3]};
      *(f32x4*)(pp + q * 1024) = v0;
      *(f32x4*)(pp + 4096 + q * 1024) = v1;
    }
  }
  WAIT_V(0);
  BAR();
  {
    const int t = w & 3, tt = t & 1, n = t >> 1;
    const int tok = (T0 + tt) * 32 + r;
#pragma unroll
    for (int e = 0; e < 2; ++e) {
      const int q = 2 * (w >> 2) + e;
      const char* sp = lds + 32768 + tt * 8192 + n * 4096 + q * 1024 + l16;
      const f32x4 s = (*(const f32x4*)(sp) + *(const f32x4*)(sp + 16384)) +
                      (*(const f32x4*)(sp + 65536) + *(const f32x4*)(sp + 65536 + 16384));
      u32x2 o = {pk2(s[0], s[1]), pk2(s[2], s[3])};
      *(u32x2*)(out + (size_t)tok * D_ + (2 * cp + n) * 32 + 8 * q + 4 * h) = o;
    }
  }
}

extern "C" void kernel_launch(void* const* d_in, const int* in_sizes, int n_in, void* d_out, int out_size,
                              void* d_ws, size_t ws_size, hipStream_t stream) {
  const u16* x = (const u16*)d_in[0];
  const u16* wqkv = (const u16*)d_in[1];
  const u16* wo = (const u16*)d_in[2];
  const u16* w1 = (const u16*)d_in[4];
  const u16* b1 = (const u16*)d_in[5];
  const u16* w2 = (const u16*)d_in[6];
  const u16* gw = (const u16*)d_in[7];
  char* ws = (char*)d_ws;
  k1<<<256, 768, 0, stream>>>(x, wqkv, wo, w1, gw, ws);
  k2<<<256, 768, 0, stream>>>(ws, b1, w2, (u16*)d_out);
}
```

```cpp
#include <hip/hip_runtime.h>
#include <hip/hip_bf16.h>
#include <stdint.h>

typedef short bf16x8 __attribute__((ext_vector_type(8)));
typedef short s16x4 __attribute__((ext_vector_type(4)));
typedef float f32x16 __attribute__((ext_vector_type(16)));
typedef float f32x4 __attribute__((ext_vector_type(4)));
typedef float f32x2 __attribute__((ext_vector_type(2)));
typedef __bf16 bf16x2_t __attribute__((ext_vector_type(2)));
typedef unsigned u32x4 __attribute__((ext_vector_type(4)));
typedef unsigned u32x2 __attribute__((ext_vector_type(2)));
typedef unsigned short u16;
#define DI __device__ __forceinline__
#define MFMA(a, b, c) __builtin_amdgcn_mfma_f32_32x32x16_bf16((a), (b), (c), 0, 0, 0)
#define MFMAU(a, b, c) MFMA(__builtin_bit_cast(bf16x8, a), __builtin_bit_cast(bf16x8, b), c)
#define LAS __attribute__((address_space(3)))
#define GAS __attribute__((address_space(1)))

#define KSTAG ((idx + (idx >> 3) * 2) & 7)
#define HO_AUX 16
#define FFN2_PRIO 0
#define SIDE_DELAY 8
#define SENT_AT 5
#define FFN1_PRIO 1
#define MISH_PRIO 1
#define FFN_PRIO 1
constexpr int S_ = 2048, D_ = 512, H_ = 32, F_ = 1376;
constexpr unsigned WS_Q = 0, WS_K = 2u << 20, WS_V = 4u << 20, WS_ACT = 6u << 20, WS_O = 12u << 20, WS_W2P = 14u << 20,
                   WS_WOP = 16u << 20, WS_GATE = 17u << 20, WS_KMAX = (17u << 20) + 16384, WS_CNT = (17u << 20) + 32768,
                   WS_W1P = 18u << 20, WS_XF = 20u << 20;

DI unsigned pk2(float a, float b) {
  f32x2 v = {a, b};
  bf16x2_t r = __builtin_convertvector(v, bf16x2_t);
  return __builtin_bit_cast(unsigned, r);
}
DI float bf_lo(unsigned u) { return __uint_as_float(u << 16); }
DI float bf_hi(unsigned u) { return __uint_as_float(u & 0xffff0000u); }
DI float rbf(float x) { return bf_lo(pk2(x, 0.f)); }
DI void gldsa16(const void* sbase, unsigned voff, unsigned dst) {
  unsigned keep;
  asm volatile("s_mov_b32 %0, m0\n\ts_mov_b32 m0, %3\n\ts_nop 0\n\tglobal_load_lds_dwordx4 %1, %2\n\ts_mov_b32 m0, %0"
               : "=&s"(keep) : "v"(voff), "s"(sbase), "s"(dst) : "memory");
}
DI s16x4 ldtr(const char* l) {
  typedef short v4i16_t __attribute__((ext_vector_type(4)));
  return __builtin_amdgcn_ds_read_tr16_b64_v4i16((LAS v4i16_t*)l);
}
#define WAIT_V(n) asm volatile("s_waitcnt vmcnt(" #n ") lgkmcnt(0)" ::: "memory")
#define BAR() do { asm volatile("" ::: "memory"); __builtin_amdgcn_s_barrier(); asm volatile("" ::: "memory"); } while (0)
template <int CTRL> DI float dppf(float v) {
  return __int_as_float(__builtin_amdgcn_update_dpp(0, __float_as_int(v), CTRL, 0xF, 0xF, true));
}
DI float rdl(float v, int l) { return __int_as_float(__builtin_amdgcn_readlane(__float_as_int(v), l)); }
DI float row16_max(float v) {
  v = fmaxf(v, dppf<0xB1>(v)); v = fmaxf(v, dppf<0x4E>(v)); v = fmaxf(v, dppf<0x141>(v)); v = fmaxf(v, dppf<0x140>(v));
  return v;
}
DI float row16_sum(float v) {
  v += dppf<0xB1>(v); v += dppf<0x4E>(v); v += dppf<0x141>(v); v += dppf<0x140>(v);
  return v;
}
DI float add_half(float v) {
  auto rr = __builtin_amdgcn_permlane32_swap(__float_as_uint(v), __float_as_uint(v), false, false);
  return __uint_as_float(rr[0]) + __uint_as_float(rr[1]);
}
DI float max_half(float v) {
  auto rr = __builtin_amdgcn_permlane32_swap(__float_as_uint(v), __float_as_uint(v), false, false);
  return fmaxf(__uint_as_float(rr[0]), __uint_as_float(rr[1]));
}
#define WS_RSRC(ws) __builtin_amdgcn_make_buffer_rsrc((void*)(ws), 0, 32u << 20, 0x00020000)
#define WST16(rs, off, v) __builtin_amdgcn_raw_buffer_store_b128(__builtin_bit_cast(u32x4, v), rs, (unsigned)(off), 0, 16)
#define BLD(rs, voff, soff, aux) __builtin_amdgcn_raw_buffer_load_b128(rs, voff, soff, aux)
#define RLX_AGENT __ATOMIC_RELAXED, __HIP_MEMORY_SCOPE_AGENT
#define RLX_WG __ATOMIC_RELAXED, __HIP_MEMORY_SCOPE_WORKGROUP

DI float mish_g(float h, float g) {
  const float w = __builtin_amdgcn_exp2f(h * 1.4426950408889634f);
  const float d = __builtin_fmaf(w, w + 2.f, 2.f);
  const float hg = h * g;
  return __builtin_fmaf(-2.f * hg, __builtin_amdgcn_rcpf(d), hg);
}
DI float mish_f(float h) {
  float t = fminf(h * 1.4426950408889634f, 60.f);
  float w = __builtin_amdgcn_exp2f(t);
  float a = w + 2.f;
  float u = w * a;
  float d = __builtin_fmaf(w, a, 2.f);
  return h * u * __builtin_amdgcn_rcpf(d);
}
DI void wait_global(char* p, unsigned want) {
  for (unsigned spins = 0; spins < (1u << 17); ++spins) {
    if ((unsigned)__builtin_amdgcn_readfirstlane(__hip_atomic_load((GAS unsigned*)p, RLX_AGENT)) >= want) break;
    __builtin_amdgcn_s_sleep(2);
  }
  asm volatile("" ::: "memory");
}
DI void wait_lds(char* p, unsigned want) {
  while ((unsigned)__builtin_amdgcn_readfirstlane(__hip_atomic_load((LAS unsigned*)p, RLX_WG)) < want)
    __builtin_amdgcn_s_sleep(1);
  asm volatile("" ::: "memory");
}
DI unsigned add_lds(char* p, int lane) {
  unsigned old = 0;
  if (lane == 0) old = __hip_atomic_fetch_add((LAS unsigned*)p, 1u, RLX_WG);
  return (unsigned)__builtin_amdgcn_readfirstlane(old);
}

#define K1_LOOP_ASM \
  "s_waitcnt vmcnt(0)\n\t" \
  "s_barrier\n\t" \
  "ds_read_b128 v[48:51], v80\n\t" \
  "ds_read_b64_tr_b16 v[52:53], v84 offset:0\n\t" \
  "ds_read_b64_tr_b16 v[54:55], v84 offset:256\n\t" \
  "ds_read_b64_tr_b16 v[56:57], v84 offset:4096\n\t" \
  "ds_read_b64_tr_b16 v[58:59], v84 offset:4352\n\t" \
  "ds_read_b64_tr_b16 v[60:61], v84 offset:8192\n\t" \
  "ds_read_b64_tr_b16 v[62:63], v84 offset:8448\n\t" \
  "1:\n\t" \
  "ds_read_b128 v[64:67], v81\n\t" \
  "ds_read_b64_tr_b16 v[68:69], v84 offset:1024\n\t" \
  "ds_read_b64_tr_b16 v[70:71], v84 offset:1280\n\t" \
  "ds_read_b64_tr_b16 v[72:73], v84 offset:5120\n\t" \
  "ds_read_b64_tr_b16 v[74:75], v84 offset:5376\n\t" \
  "ds_read_b64_tr_b16 v[76:77], v84 offset:9216\n\t" \
  "ds_read_b64_tr_b16 v[78:79], v84 offset:9472\n\t" \
  "s_waitcnt lgkmcnt(7)\n\t" \
  "v_mfma_f32_32x32x16_bf16 v[0:15], v[52:55], v[48:51], v[0:15]\n\t" \
  "v_mfma_f32_32x32x16_bf16 v[16:31], v[56:59], v[48:51], v[16:31]\n\t" \
  "v_mfma_f32_32x32x16_bf16 v[32:47], v[48:51], v[60:63], v[32:47]\n\t" \
  "ds_read_b128 v[48:51], v82\n\t" \
  "ds_read_b64_tr_b16 v[52:53], v84 offset:2048\n\t" \
  "ds_read_b64_tr_b16 v[54:55], v84 offset:2304\n\t" \
  "ds_read_b64_tr_b16 v[56:57], v84 offset:6144\n\t" \
  "ds_read_b64_tr_b16 v[58:59], v84 offset:6400\n\t" \
  "ds_read_b64_tr_b16 v[60:61], v84 offset:10240\n\t" \
  "ds_read_b64_tr_b16 v[62:63], v84 offset:10496\n\t" \
  "s_waitcnt lgkmcnt(7)\n\t" \
  "v_mfma_f32_32x32x16_bf16 v[0:15], v[68:71], v[64:67], v[0:15]\n\t" \
  "v_mfma_f32_32x32x16_bf16 v[16:31], v[72:75], v[64:67], v[16:31]\n\t" \
  "v_mfma_f32_32x32x16_bf16 v[32:47], v[64:67], v[76:79], v[32:47]\n\t" \
  "ds_read_b128 v[64:67], v83\n\t" \
  "ds_read_b64_tr_b16 v[68:69], v84 offset:3072\n\t" \
  "ds_read_b64_tr_b16 v[70:71], v84 offset:3328\n\t" \
  "ds_read_b64_tr_b16 v[72:73], v84 offset:7168\n\t" \
  "ds_read_b64_tr_b16 v[74:75], v84 offset:7424\n\t" \
  "ds_read_b64_tr_b16 v[76:77], v84 offset:11264\n\t" \
  "ds_read_b64_tr_b16 v[78:79], v84 offset:11520\n\t" \
  "s_waitcnt lgkmcnt(7)\n\t" \
  "v_mfma_f32_32x32x16_bf16 v[0:15], v[52:55], v[48:51], v[0:15]\n\t" \
  "v_mfma_f32_32x32x16_bf16 v[16:31], v[56:59], v[48:51], v[16:31]\n\t" \
  "v_mfma_f32_32x32x16_bf16 v[32:47], v[48:51], v[60:63], v[32:47]\n\t" \
  "s_cmp_eq_u32 %[kt], 7\n\t" \
  "s_cbranch_scc1 9f\n\t" \
  "s_add_u32 %[kt], %[kt], 1\n\t" \
  "s_and_b32 %[t0], %[kt], 3\n\t" \
  "s_cmp_eq_u32 %[t0], 0\n\t" \
  "s_cselect_b32 %[t0], %[cmw], %[cst]\n\t" \
  "v_add_u32 v80, %[t0], v80\n\t" \
  "v_add_u32 v81, %[t0], v81\n\t" \
  "v_add_u32 v82, %[t0], v82\n\t" \
  "v_add_u32 v83, %[t0], v83\n\t" \
  "v_add_u32 v84, %[t0], v84\n\t" \
  "s_waitcnt lgkmcnt(0)\n\t" \
  "s_barrier\n\t" \
  "ds_read_b128 v[48:51], v80\n\t" \
  "ds_read_b64_tr_b16 v[52:53], v84 offset:0\n\t" \
  "ds_read_b64_tr_b16 v[54:55], v84 offset:256\n\t" \
  "ds_read_b64_tr_b16 v[56:57], v84 offset:4096\n\t" \
  "ds_read_b64_tr_b16 v[58:59], v84 offset:4352\n\t" \
  "ds_read_b64_tr_b16 v[60:61], v84 offset:8192\n\t" \
  "ds_read_b64_tr_b16 v[62:63], v84 offset:8448\n\t" \
  "v_mfma_f32_32x32x16_bf16 v[0:15], v[68:71], v[64:67], v[0:15]\n\t" \
  "v_mfma_f32_32x32x16_bf16 v[16:31], v[72:75], v[64:67], v[16:31]\n\t" \
  "v_mfma_f32_32x32x16_bf16 v[32:47], v[64:67], v[76:79], v[32:47]\n\t" \
  "s_branch 1b\n\t" \
  "9:\n\t" \
  "s_waitcnt lgkmcnt(0)\n\t" \
  "v_mfma_f32_32x32x16_bf16 v[0:15], v[68:71], v[64:67], v[0:15]\n\t" \
  "v_mfma_f32_32x32x16_bf16 v[16:31], v[72:75], v[64:67], v[16:31]\n\t" \
  "v_mfma_f32_32x32x16_bf16 v[32:47], v[64:67], v[76:79], v[32:47]\n\t" \
  "s_nop 15\n\t" \
  ""

constexpr int K1_ST = 28672, K1_SIDE = 4 * K1_ST;
__global__ __launch_bounds__(768) void k1(const u16* __restrict__ x, const u16* __restrict__ wqkv,
                                          const u16* __restrict__ wo, const u16* __restrict__ w1,
                                          const u16* __restrict__ gw,
                                          char* __restrict__ ws) {
  __shared__ __attribute__((aligned(16))) char lds[K1_SIDE + 4 * 6144 + 4096];
  const int bid = blockIdx.x, xcd = bid & 7, idx = bid >> 3;
  const int rb = (xcd & 1) * 8 + (idx & 7), j = (xcd >> 1) * 4 + (idx >> 3);
  const int tid = threadIdx.x, lane = tid & 63;
  const int w = __builtin_amdgcn_readfirstlane(tid >> 6);
  const int r = lane & 31, h = lane >> 5, m = w & 3;
  const unsigned lb = (unsigned)(size_t)(LAS char*)lds;
  const int tile = rb * 4 + m;
  const int q4 = (lane & 15) >> 2, p4 = lane & 3, grp = (lane >> 4) & 1;
  const int sw = (r >> 1) & 7;
  const auto rs = WS_RSRC(ws);
  const int u0 = bid * 4 + m;
  const int tok = rb * 128 + j * 8 + 2 * m;
  const int kq0 = KSTAG;
  const unsigned xo0 = (unsigned)((lane >> 3) * D_ + (((lane & 7) ^ ((lane >> 4) & 7)) * 8)) * 2u;
  const unsigned xo1 = (unsigned)((8 + (lane >> 3)) * D_ + (((lane & 7) ^ ((4 + (lane >> 4)) & 7)) * 8)) * 2u;
  const unsigned wqo = (unsigned)((lane >> 2) * 1536 + (lane & 3) * 8) * 2u;
  const unsigned vo_ = wqo + 2048 - 64 * j;
  const char* xb = (const char*)(x + (size_t)(rb * 128 + 32 * m) * D_ + 64 * kq0);
  const char* wqb = (const char*)(wqkv + (size_t)(16 * m + 64 * kq0) * 1536 + 64 * j);
  {
    const int g = w >> 2;
    const char* b_ = g == 0 ? wqb : (g == 1 ? xb : xb + 16 * D_ * 2);
    const unsigned d_ = g == 0 ? lb + 16384 + m * 1024 : lb + m * 4096 + (g - 1) * 2048;
    gldsa16(b_, g == 0 ? wqo : xo0, d_);
    gldsa16(b_, g == 0 ? wqo + 64 : xo1, d_ + (g == 0 ? 4096 : 1024));
    if (g == 0) gldsa16(wqb, vo_, d_ + 8192);
  }
  if (w >= 8) {
    __builtin_amdgcn_s_sleep(SIDE_DELAY);
    const unsigned sl = lb + K1_SIDE + m * 6144;
    {
      const int u = min(u0, 959);
      const bool is1 = u < 704;
      const int uu = is1 ? u : u - 704;
      const int kb = is1 ? uu / 22 : uu >> 3, pr = is1 ? uu % 22 : uu & 7;
      const int ld = is1 ? F_ : D_;
      const u16* src = is1 ? w1 + (size_t)kb * 16 * F_ : wo + (size_t)kb * 16 * D_;
      const unsigned po = (unsigned)((lane >> 3) * ld + min(64 * pr + (lane & 7) * 8, ld - 8)) * 2u;
      gldsa16(src, po, sl);
      gldsa16(src + 8 * ld, po, sl + 1024);
    }
    gldsa16(x + (size_t)tok * D_, lane * 16, sl + 4096);
    gldsa16(x + (size_t)tok * D_, lane * 16 + 1024, sl + 5120);
    gldsa16(gw, lane * 16, lb + K1_SIDE + 24576 + m * 1024);
    if (bid == 0 && w == 8 && lane < 40) ((unsigned*)(ws + WS_CNT))[lane * 32] = 0;
    const int sstar = ((j >> 1) - kq0) & 7;
    const char* xsrc = lds + (sstar & 3) * K1_ST + m * 4096 + r * 128;
    const char* sp = lds + K1_SIDE + m * 6144;
    WAIT_V(5);
    int job = -1;
#pragma unroll 1
    for (int s = 0; s < 8 || job < 2; ++s) {
      if (s < 8) {
        asm volatile("s_waitcnt lgkmcnt(0)" ::: "memory");
        BAR();
      } else if (job < 0) {
        WAIT_V(0);
        job = 0;
      }
      if (job < 0) {
        const unsigned ib = __builtin_amdgcn_s_getreg(7 | (31 << 11));
        if (((ib & 15u) | ((ib >> 18) & 48u)) == 0) { job = 0; asm volatile("" ::: "memory"); }
      }
      if (s == sstar) {
        const u32x4 xc0 = *(const u32x4*)(xsrc + (((4 * (j & 1) + h) ^ sw) * 16));
        const u32x4 xc1 = *(const u32x4*)(xsrc + (((4 * (j & 1) + 2 + h) ^ sw) * 16));
        WST16(rs, WS_XF + ((4 * (j >> 1) + 2 * (j & 1)) * 64 + tile) * 1024 + lane * 16, xc0);
        WST16(rs, WS_XF + ((4 * (j >> 1) + 2 * (j & 1) + 1) * 64 + tile) * 1024 + lane * 16, xc1);
      }
      if (job == 0) {
        const int c = 0, u = u0;
        if (u < 960) {
          const bool is1 = u < 704;
          const int uu = is1 ? u : u - 704;
          const int kb = is1 ? uu / 22 : uu >> 3, pr = is1 ? uu % 22 : uu & 7;
          const char* pp = sp + c * 2048 + ((is1 ? 8 : 4) * h + q4) * 128 + grp * 32 + p4 * 8;
          const int hi_off = is1 ? 512 : 1024;
#pragma unroll
          for (int n = 0; n < 2; ++n) {
            const s16x4 lo = ldtr(pp + n * 64), hi = ldtr(pp + n * 64 + hi_off);
            const bf16x8 f = __builtin_shufflevector(lo, hi, 0, 1, 2, 3, 4, 5, 6, 7);
            const int blk = 2 * pr + n;
            const unsigned dst = is1 ? WS_W1P + (kb * 43 + blk) * 1024 : WS_WOP + (kb * 16 + blk) * 1024;
            if (!is1 || blk < 43) WST16(rs, dst + lane * 16, f);
          }
        }
      }
      if (job == 1) {
        const u32x4 gg = *(const u32x4*)(lds + K1_SIDE + 24576 + m * 1024 + lane * 16);
#pragma unroll
        for (int t = 0; t < 2; ++t) {
          const u32x4 gx = *(const u32x4*)(sp + 4096 + t * 1024 + lane * 16);
          float sm = 0.f;
#pragma unroll
          for (int e = 0; e < 4; ++e) {
            sm += rbf(bf_lo(gx[e]) * bf_lo(gg[e]));
            sm += rbf(bf_hi(gx[e]) * bf_hi(gg[e]));
          }
          sm = row16_sum(sm);
          sm = (rdl(sm, 0) + rdl(sm, 16)) + (rdl(sm, 32) + rdl(sm, 48));
          sm = rbf(sm);
          const float g = rbf(__builtin_amdgcn_rcpf(1.f + __builtin_amdgcn_exp2f(-sm * 1.4426950408889634f)));
          if (lane == 0) ((float*)(ws + WS_GATE))[tok + t] = g;
        }
      }
      if (job >= 0) ++job;
    }
    return;
  }
  if (w >= 4) {
    int kq = (kq0 + 1) & 7;
    { const int dk_ = kq ? 128 : -896; xb += dk_; wqb += dk_ * 1536; }
    unsigned xl = lb + K1_ST + m * 4096, wl = lb + K1_ST + 16384 + m * 1024;
#define STAGE()                                                                                   \
  do {                                                                                            \
    gldsa16(wqb, wqo, wl);                                                                        \
    gldsa16(wqb, wqo + 64, wl + 4096);                                                            \
    gldsa16(wqb, vo_, wl + 8192);                                                                 \
    gldsa16(xb, xo0, xl);                                                                         \
    gldsa16(xb, xo1, xl + 1024);                                                                  \
    gldsa16(xb + 16 * D_ * 2, xo0, xl + 2048);                                                    \
    gldsa16(xb + 16 * D_ * 2, xo1, xl + 3072);                                                    \
    kq = (kq + 1) & 7;                                                                            \
    { const int dk_ = kq ? 128 : -896; xb += dk_; wqb += dk_ * 1536; }                            \
    const bool wrap_ = xl + K1_ST >= lb + 4 * K1_ST;                                              \
    xl += wrap_ ? 0u - 3u * K1_ST : K1_ST; wl += wrap_ ? 0u - 3u * K1_ST : K1_ST;                 \
  } while (0)
    STAGE();
    WAIT_V(7); BAR();
    STAGE();
    WAIT_V(7); BAR();
    STAGE();
#pragma unroll 1
    for (int kt = 2; kt < 6; ++kt) {
      STAGE();
      WAIT_V(14); BAR();
    }
#undef STAGE
    WAIT_V(7); BAR();
    WAIT_V(0); BAR();
    return;
  }

  f32x16 acc[3];
#pragma unroll
  for (int b = 0; b < 3; ++b)
#pragma unroll
    for (int i = 0; i < 16; ++i) acc[b][i] = 0.f;
  {
    const unsigned xr0 = lb + m * 4096 + r * 128;
    unsigned xa0 = xr0 + ((0 + h) ^ sw) * 16, xa1 = xr0 + ((2 + h) ^ sw) * 16, xa2 = xr0 + ((4 + h) ^ sw) * 16,
             xa3 = xr0 + ((6 + h) ^ sw) * 16;
    unsigned wa = lb + 16384 + (8 * h + q4) * 64 + grp * 32 + p4 * 8;
    unsigned kt = 0, t0;
    asm volatile(K1_LOOP_ASM
                 : "+{v[0:15]}"(acc[0]), "+{v[16:31]}"(acc[1]), "+{v[32:47]}"(acc[2]), "+{v80}"(xa0),
                   "+{v81}"(xa1), "+{v82}"(xa2), "+{v83}"(xa3), "+{v84}"(wa), [kt] "+s"(kt), [t0] "=&s"(t0)
                 : [cst] "s"((unsigned)K1_ST), [cmw] "s"(0u - 3u * K1_ST)
                 : "memory", "scc", "v48", "v49", "v50", "v51", "v52", "v53", "v54", "v55", "v56", "v57",
                   "v58", "v59", "v60", "v61", "v62", "v63", "v64", "v65", "v66", "v67", "v68", "v69", "v70",
                   "v71", "v72", "v73", "v74", "v75", "v76", "v77", "v78", "v79");
  }
#pragma unroll
  for (int b = 0; b < 2; ++b) {
    const int c0 = 64 * j + 32 * b;
    const bool isq = c0 < 512;
    const float sc = isq ? 0.36067376022224085f : 1.0f;
    const int head0 = (c0 & 511) >> 4;
    const unsigned dst = isq ? WS_Q : WS_K;
#pragma unroll
    for (int hh = 0; hh < 2; ++hh) {
      u32x4 o;
#pragma unroll
      for (int e = 0; e < 4; ++e) o[e] = pk2(acc[b][8 * hh + 2 * e] * sc, acc[b][8 * hh + 2 * e + 1] * sc);
      WST16(rs, dst + (((head0 + hh) * 64 + tile) * 64 + lane) * 16, o);
      if (!isq) {
        float ss = 0.f;
#pragma unroll
        for (int e = 0; e < 4; ++e) {
          const float a = bf_lo(o[e]), c = bf_hi(o[e]);
          ss = __builtin_fmaf(a, a, ss);
          ss = __builtin_fmaf(c, c, ss);
        }
        ss = row16_max(add_half(ss));
        ss = fmaxf(rdl(ss, 0), rdl(ss, 16));
        if (lane == 0) ((float*)(ws + WS_KMAX))[(head0 + hh) * 64 + tile] = ss;
      }
    }
  }
  {
    const int head = 2 * j + (r >> 4), d = r & 15;
#pragma unroll
    for (int s = 0; s < 2; ++s) {
      u32x4 o;
#pragma unroll
      for (int e = 0; e < 4; ++e) o[e] = pk2(acc[2][8 * s + 2 * e], acc[2][8 * s + 2 * e + 1]);
      WST16(rs, WS_V + (head * 64 + tile) * 1024 + s * 512 + h * 256 + d * 16, o);
    }
  }
}

#define ATTN_LOOP_ASM \
  "1:\n\t" \
  "v_exp_f32 v32, v32\n\t" \
  "v_exp_f32 v33, v33\n\t" \
  "v_exp_f32 v34, v34\n\t" \
  "v_cvt_pk_bf16_f32 v88, v32, v33\n\t" \
  "v_mfma_f32_32x32x16_bf16 v[0:15], v[84:87], v[92:95], v[0:15]\n\t" \
  "s_mov_b64 exec, %[mask]\n\t" \
  "ds_read_b128 v[80:83], v97 offset:1024\n\t" \
  "ds_read_b128 v[84:87], v97 offset:1536\n\t" \
  "s_mov_b64 exec, -1\n\t" \
  "v_exp_f32 v35, v35\n\t" \
  "v_exp_f32 v36, v36\n\t" \
  "v_cvt_pk_bf16_f32 v89, v34, v35\n\t" \
  "v_exp_f32 v37, v37\n\t" \
  "v_exp_f32 v38, v38\n\t" \
  "v_cvt_pk_bf16_f32 v90, v36, v37\n\t" \
  "v_exp_f32 v39, v39\n\t" \
  "v_exp_f32 v40, v40\n\t" \
  "v_cvt_pk_bf16_f32 v91, v38, v39\n\t" \
  "v_exp_f32 v41, v41\n\t" \
  "v_exp_f32 v42, v42\n\t" \
  "v_mfma_f32_32x32x16_bf16 v[0:15], v[72:75], v[88:91], v[0:15]\n\t" \
  "v_cvt_pk_bf16_f32 v92, v40, v41\n\t" \
  "v_exp_f32 v43, v43\n\t" \
  "v_exp_f32 v44, v44\n\t" \
  "v_cvt_pk_bf16_f32 v93, v42, v43\n\t" \
  "v_exp_f32 v45, v45\n\t" \
  "v_exp_f32 v46, v46\n\t" \
  "v_cvt_pk_bf16_f32 v94, v44, v45\n\t" \
  "v_exp_f32 v47, v47\n\t" \
  "s_waitcnt lgkmcnt(0)\n\t" \
  "v_cvt_pk_bf16_f32 v95, v46, v47\n\t" \
  "v_mfma_f32_32x32x16_bf16 v[32:47], v[68:71], v[64:67], v[16:31]\n\t" \
  "ds_read_b128 v[68:71], v96 offset:3072\n\t" \
  "v_exp_f32 v48, v48\n\t" \
  "v_exp_f32 v49, v49\n\t" \
  "v_exp_f32 v50, v50\n\t" \
  "v_cvt_pk_bf16_f32 v88, v48, v49\n\t" \
  "v_mfma_f32_32x32x16_bf16 v[0:15], v[76:79], v[92:95], v[0:15]\n\t" \
  "s_mov_b64 exec, %[mask]\n\t" \
  "ds_read_b128 v[72:75], v97 offset:2048\n\t" \
  "ds_read_b128 v[76:79], v97 offset:2560\n\t" \
  "s_mov_b64 exec, -1\n\t" \
  "v_exp_f32 v51, v51\n\t" \
  "v_exp_f32 v52, v52\n\t" \
  "v_cvt_pk_bf16_f32 v89, v50, v51\n\t" \
  "v_exp_f32 v53, v53\n\t" \
  "v_exp_f32 v54, v54\n\t" \
  "v_cvt_pk_bf16_f32 v90, v52, v53\n\t" \
  "v_exp_f32 v55, v55\n\t" \
  "v_exp_f32 v56, v56\n\t" \
  "v_cvt_pk_bf16_f32 v91, v54, v55\n\t" \
  "v_exp_f32 v57, v57\n\t" \
  "v_exp_f32 v58, v58\n\t" \
  "v_mfma_f32_32x32x16_bf16 v[0:15], v[80:83], v[88:91], v[0:15]\n\t" \
  "v_cvt_pk_bf16_f32 v92, v56, v57\n\t" \
  "v_exp_f32 v59, v59\n\t" \
  "v_exp_f32 v60, v60\n\t" \
  "v_cvt_pk_bf16_f32 v93, v58, v59\n\t" \
  "v_exp_f32 v61, v61\n\t" \
  "v_exp_f32 v62, v62\n\t" \
  "v_cvt_pk_bf16_f32 v94, v60, v61\n\t" \
  "v_exp_f32 v63, v63\n\t" \
  "s_waitcnt lgkmcnt(0)\n\t" \
  "v_cvt_pk_bf16_f32 v95, v62, v63\n\t" \
  "v_mfma_f32_32x32x16_bf16 v[48:63], v[68:71], v[64:67], v[16:31]\n\t" \
  "ds_read_b128 v[68:71], v96 offset:4096\n\t" \
  "v_exp_f32 v32, v32\n\t" \
  "v_exp_f32 v33, v33\n\t" \
  "v_exp_f32 v34, v34\n\t" \
  "v_cvt_pk_bf16_f32 v88, v32, v33\n\t" \
  "v_mfma_f32_32x32x16_bf16 v[0:15], v[84:87], v[92:95], v[0:15]\n\t" \
  "s_mov_b64 exec, %[mask]\n\t" \
  "ds_read_b128 v[80:83], v97 offset:3072\n\t" \
  "ds_read_b128 v[84:87], v97 offset:3584\n\t" \
  "s_mov_b64 exec, -1\n\t" \
  "v_exp_f32 v35, v35\n\t" \
  "v_exp_f32 v36, v36\n\t" \
  "v_cvt_pk_bf16_f32 v89, v34, v35\n\t" \
  "v_exp_f32 v37, v37\n\t" \
  "v_exp_f32 v38, v38\n\t" \
  "v_cvt_pk_bf16_f32 v90, v36, v37\n\t" \
  "v_exp_f32 v39, v39\n\t" \
  "v_exp_f32 v40, v40\n\t" \
  "v_cvt_pk_bf16_f32 v91, v38, v39\n\t" \
  "v_exp_f32 v41, v41\n\t" \
  "v_exp_f32 v42, v42\n\t" \
  "v_mfma_f32_32x32x16_bf16 v[0:15], v[72:75], v[88:91], v[0:15]\n\t" \
  "v_cvt_pk_bf16_f32 v92, v40, v41\n\t" \
  "v_exp_f32 v43, v43\n\t" \
  "v_exp_f32 v44, v44\n\t" \
  "v_cvt_pk_bf16_f32 v93, v42, v43\n\t" \
  "v_exp_f32 v45, v45\n\t" \
  "v_exp_f32 v46, v46\n\t" \
  "v_cvt_pk_bf16_f32 v94, v44, v45\n\t" \
  "v_exp_f32 v47, v47\n\t" \
  "s_waitcnt lgkmcnt(0)\n\t" \
  "v_cvt_pk_bf16_f32 v95, v46, v47\n\t" \
  "v_mfma_f32_32x32x16_bf16 v[32:47], v[68:71], v[64:67], v[16:31]\n\t" \
  "ds_read_b128 v[68:71], v96 offset:5120\n\t" \
  "v_exp_f32 v48, v48\n\t" \
  "v_exp_f32 v49, v49\n\t" \
  "v_exp_f32 v50, v50\n\t" \
  "v_cvt_pk_bf16_f32 v88, v48, v49\n\t" \
  "v_mfma_f32_32x32x16_bf16 v[0:15], v[76:79], v[92:95], v[0:15]\n\t" \
  "s_mov_b64 exec, %[mask]\n\t" \
  "ds_read_b128 v[72:75], v97 offset:4096\n\t" \
  "ds_read_b128 v[76:79], v97 offset:4608\n\t" \
  "s_mov_b64 exec, -1\n\t" \
  "v_exp_f32 v51, v51\n\t" \
  "v_exp_f32 v52, v52\n\t" \
  "v_cvt_pk_bf16_f32 v89, v50, v51\n\t" \
  "v_exp_f32 v53, v53\n\t" \
  "v_exp_f32 v54, v54\n\t" \
  "v_cvt_pk_bf16_f32 v90, v52, v53\n\t" \
  "v_exp_f32 v55, v55\n\t" \
  "v_exp_f32 v56, v56\n\t" \
  "v_cvt_pk_bf16_f32 v91, v54, v55\n\t" \
  "v_exp_f32 v57, v57\n\t" \
  "v_exp_f32 v58, v58\n\t" \
  "v_mfma_f32_32x32x16_bf16 v[0:15], v[80:83], v[88:91], v[0:15]\n\t" \
  "v_cvt_pk_bf16_f32 v92, v56, v57\n\t" \
  "v_exp_f32 v59, v59\n\t" \
  "v_exp_f32 v60, v60\n\t" \
  "v_cvt_pk_bf16_f32 v93, v58, v59\n\t" \
  "v_exp_f32 v61, v61\n\t" \
  "v_exp_f32 v62, v62\n\t" \
  "v_cvt_pk_bf16_f32 v94, v60, v61\n\t" \
  "v_exp_f32 v63, v63\n\t" \
  "s_waitcnt lgkmcnt(0)\n\t" \
  "v_cvt_pk_bf16_f32 v95, v62, v63\n\t" \
  "v_mfma_f32_32x32x16_bf16 v[48:63], v[68:71], v[64:67], v[16:31]\n\t" \
  "ds_read_b128 v[68:71], v96 offset:6144\n\t" \
  "v_add_u32 v96, 0x1000, v96\n\t" \
  "v_add_u32 v97, 0x1000, v97\n\t" \
  "s_sub_u32 %[cnt], %[cnt], 1\n\t" \
  "s_and_b32 %[tmp], %[cnt], 13\n\t" \
  "s_cmp_lg_u32 %[tmp], 12\n\t" \
  "s_cbranch_scc1 2f\n\t" \
  "3:\n\t" \
  "ds_read_b32 v98, v99\n\t" \
  "s_waitcnt lgkmcnt(0)\n\t" \
  "v_readfirstlane_b32 %[tmp], v98\n\t" \
  "s_cmp_ge_u32 %[tmp], 4\n\t" \
  "s_cbranch_scc1 4f\n\t" \
  "s_sleep 1\n\t" \
  "s_branch 3b\n\t" \
  "4:\n\t" \
  "v_add_u32 v99, 4, v99\n\t" \
  "2:\n\t" \
  "s_cmp_lg_u32 %[cnt], 7\n\t" \
  "s_cbranch_scc1 5f\n\t" \
  "v_mov_b32 v98, 1\n\t" \
  "s_mov_b64 exec, 1\n\t" \
  "ds_add_u32 v99, v98\n\t" \
  "s_mov_b64 exec, -1\n\t" \
  "5:\n\t" \
  "s_cmp_lg_u32 %[cnt], 15\n\t" \
  "s_cbranch_scc1 6f\n\t" \
  "v_mov_b32 v98, 1\n\t" \
  "s_waitcnt vmcnt(0)\n\t" \
  "s_mov_b64 exec, 1\n\t" \
  "ds_add_u32 v100, v98\n\t" \
  "s_mov_b64 exec, -1\n\t" \
  "6:\n\t" \
  "s_cmp_lg_u32 %[cnt], 0\n\t" \
  "s_cbranch_scc1 1b\n\t" \
  "s_waitcnt lgkmcnt(0)\n\t" \
  "s_nop 15\n\t" \
  ""

constexpr int L2_FLAG = 131072;
__global__ __launch_bounds__(768) void k2(char* __restrict__ ws, const u16* __restrict__ b1,
                                          const u16* __restrict__ w2, u16* __restrict__ out) {
  __shared__ __attribute__((aligned(16))) char lds[L2_FLAG + 256 + 8192];
  const int bid = blockIdx.x, xcd = bid & 7, idx = bid >> 3;
  const int head = xcd * 4 + (idx & 3), qb = idx >> 2;
  const int tp = xcd >> 1, cp = (xcd & 1) * 4 + (idx & 3);
  const int T0 = qb * 8 + 2 * tp;
  const int tid = threadIdx.x, lane = tid & 63;
  const int w = __builtin_amdgcn_readfirstlane(tid >> 6);
  const int r = lane & 31, h = lane >> 5;
  const unsigned lb = (unsigned)(size_t)(LAS char*)lds;
  const unsigned l16 = lane * 16;
  const auto rs = WS_RSRC(ws);
  char* cnt = ws + WS_CNT;
  char* cntF = cnt + (8 + qb * 4 + tp) * 128;
  {
    const int p = 2 * w, tl = (p >> 3) * 4 + (p & 3);
    const char* src = ws + ((p & 4) ? WS_V : WS_K) + (size_t)(head * 64 + tl) * 1024;
    const unsigned dst = lb + ((p & 4) ? 65536 : 0) + tl * 1024;
    gldsa16(src, l16, dst);
    gldsa16(src + 1024, l16, dst + 1024);
  }
  if (w >= 8) {
    const int L = w - 8;
    {
      const char* ksrc = ws + WS_K + (size_t)(head * 64 + L) * 1024;
      const char* vsrc = ws + WS_V + (size_t)(head * 64 + L) * 1024;
      const unsigned kdst = lb + L * 1024, vdst = lb + 65536 + L * 1024;
      if (tid >= 512 && tid < 520) ((volatile LAS unsigned*)(lds + L2_FLAG))[tid - 512] = 0;
      WAIT_V(0); BAR();
#pragma unroll 1
      for (int c = 3; c < 16; ++c) {
        gldsa16(ksrc + c * 4096, l16, kdst + c * 4096); gldsa16(vsrc + c * 4096, l16, vdst + c * 4096);
      }
    }
    const int pu = bid * 4 + L, puc = min(pu, 687);
    {
      const u16* src = w2 + (size_t)(puc >> 3) * 16 * D_ + (puc & 7) * 64;
      const unsigned po = (unsigned)((lane >> 3) * D_ + (lane & 7) * 8) * 2u;
      gldsa16(src, po, lb + L2_FLAG + 256 + L * 2048);
      gldsa16(src + 8 * D_, po, lb + L2_FLAG + 256 + L * 2048 + 1024);
    }
    {
      const int T = T0 + (L & 1);
      const int f0 = cp + 24 * (L >> 1);
      const int fbc2 = min(f0 + 16, 42);
      const auto rx = __builtin_amdgcn_make_buffer_rsrc((void*)(ws + WS_XF + T * 1024), 0, 31 * 65536 + 1024, 0x00020000);
      const auto rw = __builtin_amdgcn_make_buffer_rsrc((void*)(ws + WS_W1P), 0, 32 * 43 * 1024, 0x00020000);
      const unsigned wv0 = l16 + f0 * 1024, wv1 = l16 + (f0 + 8) * 1024, wv2 = f0 + 16 < 43 ? l16 + (f0 + 16) * 1024 : 0x40000000u;
      constexpr int PD = 6;
      u32x4 fx[PD], fw[PD][3];
#pragma unroll
      for (int i = 0; i < PD; ++i) {
        fx[i] = BLD(rx, l16, i * 65536, 0);
        fw[i][0] = BLD(rw, wv0, i * 44032, 0); fw[i][1] = BLD(rw, wv1, i * 44032, 0); fw[i][2] = BLD(rw, wv2, i * 44032, 0);
      }
      WAIT_V(40);
      add_lds(lds + L2_FLAG, lane);
      WAIT_V(24);
      add_lds(lds + L2_FLAG + 4, lane);
      {
        const char* pp = lds + L2_FLAG + 256 + L * 2048 + (4 * h + ((lane & 15) >> 2)) * 128 + ((lane >> 4) & 1) * 32 + (lane & 3) * 8;
#pragma unroll
        for (int n = 0; n < 2; ++n) {
          const s16x4 lo = ldtr(pp + n * 64), hi = ldtr(pp + n * 64 + 1024);
          const bf16x8 f = __builtin_shufflevector(lo, hi, 0, 1, 2, 3, 4, 5, 6, 7);
          if (pu < 688) WST16(rs, WS_W2P + (pu * 2 + n) * 1024 + l16, f);
        }
      }
      __builtin_amdgcn_s_setprio(FFN1_PRIO);
      f32x16 a[3];
#pragma unroll
      for (int g = 0; g < 4; ++g) {
        const u32x2 b0 = *(const u32x2*)(b1 + 32 * f0 + 8 * g + 4 * h);
        const u32x2 b1_ = *(const u32x2*)(b1 + 32 * (f0 + 8) + 8 * g + 4 * h);
        const u32x2 b2 = *(const u32x2*)(b1 + 32 * fbc2 + 8 * g + 4 * h);
        a[0][4 * g] = bf_lo(b0[0]); a[0][4 * g + 1] = bf_hi(b0[0]); a[0][4 * g + 2] = bf_lo(b0[1]); a[0][4 * g + 3] = bf_hi(b0[1]);
        a[1][4 * g] = bf_lo(b1_[0]); a[1][4 * g + 1] = bf_hi(b1_[0]); a[1][4 * g + 2] = bf_lo(b1_[1]); a[1][4 * g + 3] = bf_hi(b1_[1]);
        a[2][4 * g] = bf_lo(b2[0]); a[2][4 * g + 1] = bf_hi(b2[0]); a[2][4 * g + 2] = bf_lo(b2[1]); a[2][4 * g + 3] = bf_hi(b2[1]);
      }
      __builtin_amdgcn_sched_barrier(0);
      WAIT_V(0);
#pragma unroll 1
      for (int k0 = PD; k0 < 12 + PD; k0 += PD) {
#pragma unroll
        for (int i = 0; i < PD; ++i) {
          a[0] = MFMAU(fw[i][0], fx[i], a[0]);
          a[1] = MFMAU(fw[i][1], fx[i], a[1]);
          a[2] = MFMAU(fw[i][2], fx[i], a[2]);
          fx[i] = BLD(rx, l16, (k0 + i) * 65536, 0);
          fw[i][0] = BLD(rw, wv0, (k0 + i) * 44032, 0); fw[i][1] = BLD(rw, wv1, (k0 + i) * 44032, 0);
          fw[i][2] = BLD(rw, wv2, (k0 + i) * 44032, 0);
          __builtin_amdgcn_sched_barrier(0);
        }
      }
      wait_lds(lds + L2_FLAG + 24, 8);
      if (add_lds(lds + L2_FLAG + 16, lane) == 3 && lane == 0) __hip_atomic_fetch_add((GAS unsigned*)(cnt + xcd * 128), 1u, RLX_AGENT);
      __builtin_amdgcn_sched_barrier(0);
#pragma unroll 1
      for (int k0 = 12 + PD; k0 < 30 + PD; k0 += PD) {
#pragma unroll
        for (int i = 0; i < PD; ++i) {
          a[0] = MFMAU(fw[i][0], fx[i], a[0]);
          a[1] = MFMAU(fw[i][1], fx[i], a[1]);
          a[2] = MFMAU(fw[i][2], fx[i], a[2]);
          fx[i] = BLD(rx, l16, (k0 + i) * 65536, 0);
          fw[i][0] = BLD(rw, wv0, (k0 + i) * 44032, 0); fw[i][1] = BLD(rw, wv1, (k0 + i) * 44032, 0);
          fw[i][2] = BLD(rw, wv2, (k0 + i) * 44032, 0);
          __builtin_amdgcn_sched_barrier(0);
        }
      }
#pragma unroll
      for (int i = 0; i < 2; ++i) {
        a[0] = MFMAU(fw[i][0], fx[i], a[0]);
        a[1] = MFMAU(fw[i][1], fx[i], a[1]);
        a[2] = MFMAU(fw[i][2], fx[i], a[2]);
      }
      const float g1 = rbf(1.f - ((const float*)(ws + WS_GATE))[T * 32 + r]);
      __builtin_amdgcn_s_setprio(MISH_PRIO);
#pragma unroll
      for (int b = 0; b < 3; ++b) {
        if (f0 + 8 * b < 43) {
#pragma unroll
          for (int hh = 0; hh < 2; ++hh) {
            u32x4 o;
#pragma unroll
            for (int gq = 0; gq < 2; ++gq) {
              const int g = 2 * hh + gq;
              const float h0 = a[b][4 * g + 0], h1 = a[b][4 * g + 1], h2 = a[b][4 * g + 2], h3 = a[b][4 * g + 3];
              o[2 * gq] = pk2(mish_g(h0, g1), mish_g(h1, g1));
              o[2 * gq + 1] = pk2(mish_g(h2, g1), mish_g(h3, g1));
            }
            WST16(rs, WS_ACT + ((2 * (f0 + 8 * b) + hh) * 64 + T) * 1024 + l16, o);
          }
        }
      }
      WAIT_V(0);
      if (add_lds(lds + L2_FLAG + 12, lane) == 3 && lane == 0) __hip_atomic_fetch_add((GAS unsigned*)cntF, 1u, RLX_AGENT);
    }
    {
      const int klast = min(22 * L + 21, 85);
      const auto ra = __builtin_amdgcn_make_buffer_rsrc((void*)(ws + WS_ACT + T0 * 1024), 0, klast * 65536 + 2048, 0x00020000);
      const auto rw = __builtin_amdgcn_make_buffer_rsrc((void*)(ws + WS_W2P + cp * 2048), 0, klast * 16384 + 2048, 0x00020000);
      constexpr int PD = 5;
      u32x4 fa[PD][2], fw[PD][2];
      const int kb0 = 22 * L;
      if (L == 0) {
        char* pw = lane < 8 ? cnt + lane * 128 : cntF;
        const unsigned want = lane < 8 ? 32u : 8u;
        wait_global(cntF, 8);
        for (unsigned spins = 0; spins < (1u << 17); ++spins) {
          const unsigned v = __hip_atomic_load((GAS unsigned*)pw, RLX_AGENT);
          if (__all(v >= want)) break;
          __builtin_amdgcn_s_sleep(2);
        }
        asm volatile("" ::: "memory");
        add_lds(lds + L2_FLAG + 20, lane);
      } else {
        wait_lds(lds + L2_FLAG + 20, 1);
      }
      __builtin_amdgcn_s_setprio(FFN2_PRIO);
#pragma unroll
      for (int i = 0; i < PD; ++i) {
        fa[i][0] = BLD(ra, l16, (kb0 + i) * 65536, HO_AUX); fa[i][1] = BLD(ra, l16 + 1024, (kb0 + i) * 65536, HO_AUX);
        fw[i][0] = BLD(rw, l16, (kb0 + i) * 16384, HO_AUX); fw[i][1] = BLD(rw, l16 + 1024, (kb0 + i) * 16384, HO_AUX);
        __builtin_amdgcn_sched_barrier(0);
      }
      f32x16 b[4];
#pragma unroll
      for (int t = 0; t < 4; ++t)
#pragma unroll
        for (int i = 0; i < 16; ++i) b[t][i] = 0.f;
      __builtin_amdgcn_sched_barrier(0);
#pragma unroll 1
      for (int k0 = PD; k0 < 20 + PD; k0 += PD) {
#pragma unroll
        for (int i = 0; i < PD; ++i) {
#pragma unroll
          for (int t = 0; t < 4; ++t) b[t] = MFMAU(fw[i][t >> 1], fa[i][t & 1], b[t]);
          const int kn = kb0 + k0 + i;
          fa[i][0] = BLD(ra, l16, kn * 65536, HO_AUX); fa[i][1] = BLD(ra, l16 + 1024, kn * 65536, HO_AUX);
          fw[i][0] = BLD(rw, l16, kn * 16384, HO_AUX); fw[i][1] = BLD(rw, l16 + 1024, kn * 16384, HO_AUX);
          __builtin_amdgcn_sched_barrier(0);
        }
      }
#pragma unroll
      for (int i = 0; i < 2; ++i)
#pragma unroll
        for (int t = 0; t < 4; ++t) b[t] = MFMAU(fw[i][t >> 1], fa[i][t & 1], b[t]);
      WAIT_V(0);
      BAR();
      char* bs = lds + (L >> 1) * 65536 + 32768 + (L & 1) * 16384 + l16;
#pragma unroll
      for (int t = 0; t < 4; ++t)
#pragma unroll
        for (int q = 0; q < 4; ++q) {
          f32x4 v = {b[t][4 * q], b[t][4 * q + 1], b[t][4 * q + 2], b[t][4 * q + 3]};
          *(f32x4*)(bs + (t & 1) * 8192 + (t >> 1) * 4096 + q * 1024) = v;
        }
      WAIT_V(0);
      BAR();
      BAR();
    }
    return;
  }

  const int qt = qb * 8 + w;
  bf16x8 qf = *(const bf16x8*)(ws + WS_Q + (size_t)(head * 64 + qt) * 1024 + l16);
  float km = ((const float*)(ws + WS_KMAX))[head * 64 + lane];
  const float gq = ((const float*)(ws + WS_GATE))[qt * 32 + r];
  km = row16_max(km);
  km = fmaxf(fmaxf(rdl(km, 0), rdl(km, 16)), fmaxf(rdl(km, 32), rdl(km, 48)));
  float qs = 0.f;
  {
    const u32x4 qu = __builtin_bit_cast(u32x4, qf);
#pragma unroll
    for (int e = 0; e < 4; ++e) {
      const float a = bf_lo(qu[e]), c = bf_hi(qu[e]);
      qs = __builtin_fmaf(a, a, qs);
      qs = __builtin_fmaf(c, c, qs);
    }
    qs = add_half(qs);
  }
  float mb = __builtin_amdgcn_sqrtf(qs * km) * 1.0001f;
  asm volatile("s_waitcnt vmcnt(0)" : "+v"(mb) : "v"(gq) : "memory");
  f32x16 negm, O;
#pragma unroll
  for (int i = 0; i < 16; ++i) { negm[i] = -mb; O[i] = 0.f; }

  const char* kp = lds + lane * 16;
  const char* vp = lds + 65536 + h * 256 + (lane & 15) * 16;
  const bool vact = (lane & 16) == 0;
  const short one = ((lane & 27) == 16) ? (short)0x3F80 : (short)0;
  bf16x8 va0, va1, vb0, vb1;
#pragma unroll
  for (int i = 0; i < 8; ++i) { va0[i] = one; va1[i] = one; vb0[i] = one; vb1[i] = one; }
  u32x4 p0 = {0, 0, 0, 0}, p1 = {0, 0, 0, 0};
  unsigned kaddr = lb + l16, vaddr = lb + 65536 + h * 256 + (lane & 15) * 16;
  const unsigned long long vmask = 0x0000FFFF0000FFFFull;

  {
    const u32x4 ones = {0xFFFFFFFFu, 0xFFFFFFFFu, 0xFFFFFFFFu, 0xFFFFFFFFu};
    WST16(rs, WS_O + (head * 64 + qt) * 1024 + l16, ones);
  }
  BAR();
  f32x16 Sa = MFMA(*(const bf16x8*)(kp), qf, negm);
  f32x16 Sb = MFMA(*(const bf16x8*)(kp + 1024), qf, negm);
  bf16x8 kf = *(const bf16x8*)(kp + 2048);
  if (vact) { va0 = *(const bf16x8*)(vp); va1 = *(const bf16x8*)(vp + 512); }
  {
    unsigned cnt = 16, tmp, fa_ = lb + L2_FLAG;
    asm volatile("s_nop 15\n\t" ATTN_LOOP_ASM
                 : "+{v[0:15]}"(O), "+{v[32:47]}"(Sa), "+{v[48:63]}"(Sb), "+{v[68:71]}"(kf),
                   "+{v[72:75]}"(va0), "+{v[76:79]}"(va1), "+{v[80:83]}"(vb0), "+{v[84:87]}"(vb1),
                   "+{v[88:91]}"(p0), "+{v[92:95]}"(p1), "+{v96}"(kaddr), "+{v97}"(vaddr), [cnt] "+s"(cnt),
                   [tmp] "=&s"(tmp), "+{v99}"(fa_)
                 : "{v[16:31]}"(negm), "{v[64:67]}"(qf), [mask] "s"(vmask), "{v100}"(lb + L2_FLAG + 24)
                 : "memory", "scc", "v98");
  }
  O = MFMA(vb1, __builtin_bit_cast(bf16x8, p1), O);

  float l = O[8];
  if (__any(!(l >= 8.271806125530277e-25f))) {
    float m = -INFINITY;
    f32x16 zero;
#pragma unroll
    for (int i = 0; i < 16; ++i) { zero[i] = 0.f; O[i] = 0.f; }
    const char* kg = ws + WS_K + (size_t)head * 65536 + l16;
    const char* vg = ws + WS_V + (size_t)head * 65536 + h * 256 + (lane & 15) * 16;
#pragma unroll 1
    for (int t = 0; t < 64; ++t) {
      f32x16 Sx = MFMA(*(const bf16x8*)(kg + t * 1024), qf, zero);
      float mx = Sx[0];
#pragma unroll
      for (int i = 1; i < 16; ++i) mx = fmaxf(mx, Sx[i]);
      mx = max_half(mx);
      const float mn = fmaxf(m, mx);
      const float alpha = __builtin_amdgcn_exp2f(m - mn);
      m = mn;
#pragma unroll
      for (int i = 0; i < 16; ++i) { O[i] *= alpha; Sx[i] -= mn; }
      if (vact) { va0 = *(const bf16x8*)(vg + t * 1024); va1 = *(const bf16x8*)(vg + t * 1024 + 512); }
#pragma unroll
      for (int e = 0; e < 4; ++e) {
        p0[e] = pk2(__builtin_amdgcn_exp2f(Sx[2 * e]), __builtin_amdgcn_exp2f(Sx[2 * e + 1]));
        p1[e] = pk2(__builtin_amdgcn_exp2f(Sx[8 + 2 * e]), __builtin_amdgcn_exp2f(Sx[8 + 2 * e + 1]));
      }
      O = MFMAU(va0, p0, O);
      O = MFMAU(va1, p1, O);
    }
    l = O[8];
  }
  {
    const float rl = gq / l;
    u32x4 o;
#pragma unroll
    for (int e = 0; e < 4; ++e) o[e] = pk2(O[2 * e] * rl, O[2 * e + 1] * rl);
    WST16(rs, WS_O + (head * 64 + qt) * 1024 + l16, o);
  }
  if (w < 4) {
    wait_lds(lds + L2_FLAG + 8, 8);
    const char* wsrc = ws + WS_WOP + cp * 2048 + (size_t)w * 8 * 16384;
    const unsigned wdst = lb + (w >> 1) * 65536 + (w & 1) * 16384;
#pragma unroll 1
    for (int i = 0; i < 8; ++i) {
      gldsa16(wsrc + i * 16384, l16, wdst + i * 2048);
      gldsa16(wsrc + i * 16384, l16 + 1024, wdst + i * 2048 + 1024);
    }
    WAIT_V(0);
  }
  BAR();
  {
    const int kq = w >> 1;
    u32x4 fo[8];
#pragma unroll
    for (int i = 0; i < 8; ++i) fo[i] = BLD(rs, l16, WS_O + ((kq * 8 + i) * 64 + T0 + (w & 1)) * 1024, HO_AUX);
    const char* wp = lds + (kq >> 1) * 65536 + (kq & 1) * 16384 + l16;
    char* pp = lds + (kq >> 1) * 65536 + 32768 + (kq & 1) * 16384 + (w & 1) * 8192 + l16;
    BAR();
    f32x16 a0, a1;
#pragma unroll
    for (int q = 0; q < 4; ++q) {
      const f32x4 v0 = *(const f32x4*)(pp + q * 1024), v1 = *(const f32x4*)(pp + 4096 + q * 1024);
#pragma unroll
      for (int e = 0; e < 4; ++e) { a0[4 * q + e] = v0[e]; a1[4 * q + e] = v1[e]; }
    }
#define INCOMPLETE(v) __any(max(max(v[0], v[1]), max(v[2], v[3])) == 0xFFFFFFFFu)
#pragma unroll
    for (int i = 0; i < 8; ++i) {
      if (INCOMPLETE(fo[i])) {
#pragma unroll 1
        for (unsigned spins = 0; spins < (1u << 18); ++spins) {
          asm volatile("" ::: "memory");
#pragma unroll
          for (int i2 = i; i2 < 8; ++i2)
            if (i2 == i || INCOMPLETE(fo[i2]))
              fo[i2] = BLD(rs, l16, WS_O + ((kq * 8 + i2) * 64 + T0 + (w & 1)) * 1024, HO_AUX);
          if (!INCOMPLETE(fo[i])) break;
        }
      }
      a0 = MFMAU(*(const u32x4*)(wp + i * 2048), fo[i], a0);
      a1 = MFMAU(*(const u32x4*)(wp + i * 2048 + 1024), fo[i], a1);
    }
#pragma unroll
    for (int q = 0; q < 4; ++q) {
      f32x4 v0 = {a0[4 * q], a0[4 * q + 1], a0[4 * q + 2], a0[4 * q + 3]};
      f32x4 v1 = {a1[4 * q], a1[4 * q + 1], a1[4 * q + 2], a1[4 * q + 3]};
      *(f32x4*)(pp + q * 1024) = v0;
      *(f32x4*)(pp + 4096 + q * 1024) = v1;
    }
  }
  WAIT_V(0);
  BAR();
  {
    const int t = w & 3, tt = t & 1, n = t >> 1;
    const int tok = (T0 + tt) * 32 + r;
#pragma unroll
    for (int e = 0; e < 2; ++e) {
      const int q = 2 * (w >> 2) + e;
      const char* sp = lds + 32768 + tt * 8192 + n * 4096 + q * 1024 + l16;
      const f32x4 s = (*(const f32x4*)(sp) + *(const f32x4*)(sp + 16384)) +
                      (*(const f32x4*)(sp + 65536) + *(const f32x4*)(sp + 65536 + 16384));
      u32x2 o = {pk2(s[0], s[1]), pk2(s[2], s[3])};
      *(u32x2*)(out + (size_t)tok * D_ + (2 * cp + n) * 32 + 8 * q + 4 * h) = o;
    }
  }
}

extern "C" void kernel_launch(void* const* d_in, const int* in_sizes, int n_in, void* d_out, int out_size,
                              void* d_ws, size_t ws_size, hipStream_t stream) {
  const u16* x = (const u16*)d_in[0];
  const u16* wqkv = (const u16*)d_in[1];
  const u16* wo = (const u16*)d_in[2];
  const u16* w1 = (const u16*)d_in[4];
  const u16* b1 = (const u16*)d_in[5];
  const u16* w2 = (const u16*)d_in[6];
  const u16* gw = (const u16*)d_in[7];
  char* ws = (char*)d_ws;
  k1<<<256, 768, 0, stream>>>(x, wqkv, wo, w1, gw, ws);
  k2<<<256, 768, 0, stream>>>(ws, b1, w2, (u16*)d_out);
}
```

```cpp
#include <hip/hip_runtime.h>
#include <hip/hip_bf16.h>
#include <stdint.h>

typedef short bf16x8 __attribute__((ext_vector_type(8)));
typedef short s16x4 __attribute__((ext_vector_type(4)));
typedef float f32x16 __attribute__((ext_vector_type(16)));
typedef float f32x4 __attribute__((ext_vector_type(4)));
typedef float f32x2 __attribute__((ext_vector_type(2)));
typedef __bf16 bf16x2_t __attribute__((ext_vector_type(2)));
typedef unsigned u32x4 __attribute__((ext_vector_type(4)));
typedef unsigned u32x2 __attribute__((ext_vector_type(2)));
typedef unsigned short u16;
#define DI __device__ __forceinline__
#define MFMA(a, b, c) __builtin_amdgcn_mfma_f32_32x32x16_bf16((a), (b), (c), 0, 0, 0)
#define MFMAU(a, b, c) MFMA(__builtin_bit_cast(bf16x8, a), __builtin_bit_cast(bf16x8, b), c)
#define LAS __attribute__((address_space(3)))
#define GAS __attribute__((address_space(1)))

#define KSTAG ((idx + (idx >> 3) * 2) & 7)
#define HO_AUX 16
#define FFN1_PRIO 1
#define MISH_PRIO 1
#define FFN2_PRIO 0
#define SIDE_DELAY 8
constexpr int D_ = 512, F_ = 1376;
constexpr unsigned WS_Q = 0, WS_K = 2u << 20, WS_V = 4u << 20, WS_ACT = 6u << 20, WS_O = 12u << 20, WS_W2P = 14u << 20,
                   WS_WOP = 16u << 20, WS_GATE = 17u << 20, WS_KMAX = (17u << 20) + 16384, WS_CNT = (17u << 20) + 32768,
                   WS_W1P = 18u << 20, WS_XF = 20u << 20;

DI unsigned pk2(float a, float b) {
  f32x2 v = {a, b};
  bf16x2_t r = __builtin_convertvector(v, bf16x2_t);
  return __builtin_bit_cast(unsigned, r);
}
DI float bf_lo(unsigned u) { return __uint_as_float(u << 16); }
DI float bf_hi(unsigned u) { return __uint_as_float(u & 0xffff0000u); }
DI float rbf(float x) { return bf_lo(pk2(x, 0.f)); }
DI void gldsa16(const void* sbase, unsigned voff, unsigned dst) {
  unsigned keep;
  asm volatile("s_mov_b32 %0, m0\n\ts_mov_b32 m0, %3\n\ts_nop 0\n\tglobal_load_lds_dwordx4 %1, %2\n\ts_mov_b32 m0, %0"
               : "=&s"(keep) : "v"(voff), "s"(sbase), "s"(dst) : "memory");
}
DI s16x4 ldtr(const char* l) {
  typedef short v4i16_t __attribute__((ext_vector_type(4)));
  return __builtin_amdgcn_ds_read_tr16_b64_v4i16((LAS v4i16_t*)l);
}
#define WAIT_V(n) asm volatile("s_waitcnt vmcnt(" #n ") lgkmcnt(0)" ::: "memory")
#define BAR() do { asm volatile("" ::: "memory"); __builtin_amdgcn_s_barrier(); asm volatile("" ::: "memory"); } while (0)
template <int CTRL> DI float dppf(float v) {
  return __int_as_float(__builtin_amdgcn_update_dpp(0, __float_as_int(v), CTRL, 0xF, 0xF, true));
}
DI float rdl(float v, int l) { return __int_as_float(__builtin_amdgcn_readlane(__float_as_int(v), l)); }
DI float row16_max(float v) {
  v = fmaxf(v, dppf<0xB1>(v)); v = fmaxf(v, dppf<0x4E>(v)); v = fmaxf(v, dppf<0x141>(v)); v = fmaxf(v, dppf<0x140>(v));
  return v;
}
DI float row16_sum(float v) {
  v += dppf<0xB1>(v); v += dppf<0x4E>(v); v += dppf<0x141>(v); v += dppf<0x140>(v);
  return v;
}
DI float add_half(float v) {
  auto rr = __builtin_amdgcn_permlane32_swap(__float_as_uint(v), __float_as_uint(v), false, false);
  return __uint_as_float(rr[0]) + __uint_as_float(rr[1]);
}
DI float max_half(float v) {
  auto rr = __builtin_amdgcn_permlane32_swap(__float_as_uint(v), __float_as_uint(v), false, false);
  return fmaxf(__uint_as_float(rr[0]), __uint_as_float(rr[1]));
}
#define WS_RSRC(ws) __builtin_amdgcn_make_buffer_rsrc((void*)(ws), 0, 32u << 20, 0x00020000)
#define WST16(rs, off, v) __builtin_amdgcn_raw_buffer_store_b128(__builtin_bit_cast(u32x4, v), rs, (unsigned)(off), 0, 16)
#define BLD(rs, voff, soff, aux) __builtin_amdgcn_raw_buffer_load_b128(rs, voff, soff, aux)
#define RLX_AGENT __ATOMIC_RELAXED, __HIP_MEMORY_SCOPE_AGENT
#define RLX_WG __ATOMIC_RELAXED, __HIP_MEMORY_SCOPE_WORKGROUP

DI float mish_g(float h, float g) {
  const float w = __builtin_amdgcn_exp2f(h * 1.4426950408889634f);
  const float d = __builtin_fmaf(w, w + 2.f, 2.f);
  const float hg = h * g;
  return __builtin_fmaf(-2.f * hg, __builtin_amdgcn_rcpf(d), hg);
}
DI void wait_global(char* p, unsigned want) {
  for (unsigned spins = 0; spins < (1u << 17); ++spins) {
    if ((unsigned)__builtin_amdgcn_readfirstlane(__hip_atomic_load((GAS unsigned*)p, RLX_AGENT)) >= want) break;
    __builtin_amdgcn_s_sleep(2);
  }
  asm volatile("" ::: "memory");
}
DI void wait_lds(char* p, unsigned want) {
  while ((unsigned)__builtin_amdgcn_readfirstlane(__hip_atomic_load((LAS unsigned*)p, RLX_WG)) < want)
    __builtin_amdgcn_s_sleep(1);
  asm volatile("" ::: "memory");
}
DI unsigned add_lds(char* p, int lane) {
  unsigned old = 0;
  if (lane == 0) old = __hip_atomic_fetch_add((LAS unsigned*)p, 1u, RLX_WG);
  return (unsigned)__builtin_amdgcn_readfirstlane(old);
}

#define K1_LOOP_ASM \
  "s_waitcnt vmcnt(0)\n\t" \
  "s_barrier\n\t" \
  "ds_read_b128 v[48:51], v80\n\t" \
  "ds_read_b64_tr_b16 v[52:53], v84 offset:0\n\t" \
  "ds_read_b64_tr_b16 v[54:55], v84 offset:256\n\t" \
  "ds_read_b64_tr_b16 v[56:57], v84 offset:4096\n\t" \
  "ds_read_b64_tr_b16 v[58:59], v84 offset:4352\n\t" \
  "ds_read_b64_tr_b16 v[60:61], v84 offset:8192\n\t" \
  "ds_read_b64_tr_b16 v[62:63], v84 offset:8448\n\t" \
  "1:\n\t" \
  "ds_read_b128 v[64:67], v81\n\t" \
  "ds_read_b64_tr_b16 v[68:69], v84 offset:1024\n\t" \
  "ds_read_b64_tr_b16 v[70:71], v84 offset:1280\n\t" \
  "ds_read_b64_tr_b16 v[72:73], v84 offset:5120\n\t" \
  "ds_read_b64_tr_b16 v[74:75], v84 offset:5376\n\t" \
  "ds_read_b64_tr_b16 v[76:77], v84 offset:9216\n\t" \
  "ds_read_b64_tr_b16 v[78:79], v84 offset:9472\n\t" \
  "s_waitcnt lgkmcnt(7)\n\t" \
  "v_mfma_f32_32x32x16_bf16 v[0:15], v[52:55], v[48:51], v[0:15]\n\t" \
  "v_mfma_f32_32x32x16_bf16 v[16:31], v[56:59], v[48:51], v[16:31]\n\t" \
  "v_mfma_f32_32x32x16_bf16 v[32:47], v[48:51], v[60:63], v[32:47]\n\t" \
  "ds_read_b128 v[48:51], v82\n\t" \
  "ds_read_b64_tr_b16 v[52:53], v84 offset:2048\n\t" \
  "ds_read_b64_tr_b16 v[54:55], v84 offset:2304\n\t" \
  "ds_read_b64_tr_b16 v[56:57], v84 offset:6144\n\t" \
  "ds_read_b64_tr_b16 v[58:59], v84 offset:6400\n\t" \
  "ds_read_b64_tr_b16 v[60:61], v84 offset:10240\n\t" \
  "ds_read_b64_tr_b16 v[62:63], v84 offset:10496\n\t" \
  "s_waitcnt lgkmcnt(7)\n\t" \
  "v_mfma_f32_32x32x16_bf16 v[0:15], v[68:71], v[64:67], v[0:15]\n\t" \
  "v_mfma_f32_32x32x16_bf16 v[16:31], v[72:75], v[64:67], v[16:31]\n\t" \
  "v_mfma_f32_32x32x16_bf16 v[32:47], v[64:67], v[76:79], v[32:47]\n\t" \
  "ds_read_b128 v[64:67], v83\n\t" \
  "ds_read_b64_tr_b16 v[68:69], v84 offset:3072\n\t" \
  "ds_read_b64_tr_b16 v[70:71], v84 offset:3328\n\t" \
  "ds_read_b64_tr_b16 v[72:73], v84 offset:7168\n\t" \
  "ds_read_b64_tr_b16 v[74:75], v84 offset:7424\n\t" \
  "ds_read_b64_tr_b16 v[76:77], v84 offset:11264\n\t" \
  "ds_read_b64_tr_b16 v[78:79], v84 offset:11520\n\t" \
  "s_waitcnt lgkmcnt(7)\n\t" \
  "v_mfma_f32_32x32x16_bf16 v[0:15], v[52:55], v[48:51], v[0:15]\n\t" \
  "v_mfma_f32_32x32x16_bf16 v[16:31], v[56:59], v[48:51], v[16:31]\n\t" \
  "v_mfma_f32_32x32x16_bf16 v[32:47], v[48:51], v[60:63], v[32:47]\n\t" \
  "s_cmp_eq_u32 %[kt], 7\n\t" \
  "s_cbranch_scc1 9f\n\t" \
  "s_add_u32 %[kt], %[kt], 1\n\t" \
  "s_and_b32 %[t0], %[kt], 3\n\t" \
  "s_cmp_eq_u32 %[t0], 0\n\t" \
  "s_cselect_b32 %[t0], %[cmw], %[cst]\n\t" \
  "v_add_u32 v80, %[t0], v80\n\t" \
  "v_add_u32 v81, %[t0], v81\n\t" \
  "v_add_u32 v82, %[t0], v82\n\t" \
  "v_add_u32 v83, %[t0], v83\n\t" \
  "v_add_u32 v84, %[t0], v84\n\t" \
  "s_waitcnt lgkmcnt(0)\n\t" \
  "s_barrier\n\t" \
  "ds_read_b128 v[48:51], v80\n\t" \
  "ds_read_b64_tr_b16 v[52:53], v84 offset:0\n\t" \
  "ds_read_b64_tr_b16 v[54:55], v84 offset:256\n\t" \
  "ds_read_b64_tr_b16 v[56:57], v84 offset:4096\n\t" \
  "ds_read_b64_tr_b16 v[58:59], v84 offset:4352\n\t" \
  "ds_read_b64_tr_b16 v[60:61], v84 offset:8192\n\t" \
  "ds_read_b64_tr_b16 v[62:63], v84 offset:8448\n\t" \
  "v_mfma_f32_32x32x16_bf16 v[0:15], v[68:71], v[64:67], v[0:15]\n\t" \
  "v_mfma_f32_32x32x16_bf16 v[16:31], v[72:75], v[64:67], v[16:31]\n\t" \
  "v_mfma_f32_32x32x16_bf16 v[32:47], v[64:67], v[76:79], v[32:47]\n\t" \
  "s_branch 1b\n\t" \
  "9:\n\t" \
  "s_waitcnt lgkmcnt(0)\n\t" \
  "v_mfma_f32_32x32x16_bf16 v[0:15], v[68:71], v[64:67], v[0:15]\n\t" \
  "v_mfma_f32_32x32x16_bf16 v[16:31], v[72:75], v[64:67], v[16:31]\n\t" \
  "v_mfma_f32_32x32x16_bf16 v[32:47], v[64:67], v[76:79], v[32:47]\n\t" \
  "s_nop 15\n\t" \
  ""

constexpr int K1_ST = 28672, K1_SIDE = 4 * K1_ST;
__global__ __launch_bounds__(768) void k1(const u16* __restrict__ x, const u16* __restrict__ wqkv,
                                          const u16* __restrict__ wo, const u16* __restrict__ w1,
                                          const u16* __restrict__ gw,
                                          char* __restrict__ ws) {
  __shared__ __attribute__((aligned(16))) char lds[K1_SIDE + 4 * 6144 + 4096];
  const int bid = blockIdx.x, xcd = bid & 7, idx = bid >> 3;
  const int rb = (xcd & 1) * 8 + (idx & 7), j = (xcd >> 1) * 4 + (idx >> 3);
  const int tid = threadIdx.x, lane = tid & 63;
  const int w = __builtin_amdgcn_readfirstlane(tid >> 6);
  const int r = lane & 31, h = lane >> 5, m = w & 3;
  const unsigned lb = (unsigned)(size_t)(LAS char*)lds;
  const int tile = rb * 4 + m;
  const int q4 = (lane & 15) >> 2, p4 = lane & 3, grp = (lane >> 4) & 1;
  const int sw = (r >> 1) & 7;
  const auto rs = WS_RSRC(ws);
  const int u0 = bid * 4 + m;
  const int tok = rb * 128 + j * 8 + 2 * m;
  const int kq0 = KSTAG;
  const unsigned xo0 = (unsigned)((lane >> 3) * D_ + (((lane & 7) ^ ((lane >> 4) & 7)) * 8)) * 2u;
  const unsigned xo1 = (unsigned)((8 + (lane >> 3)) * D_ + (((lane & 7) ^ ((4 + (lane >> 4)) & 7)) * 8)) * 2u;
  const unsigned wqo = (unsigned)((lane >> 2) * 1536 + (lane & 3) * 8) * 2u;
  const unsigned vo_ = wqo + 2048 - 64 * j;
  const char* xb = (const char*)(x + (size_t)(rb * 128 + 32 * m) * D_ + 64 * kq0);
  const char* wqb = (const char*)(wqkv + (size_t)(16 * m + 64 * kq0) * 1536 + 64 * j);
  {
    const int g = w >> 2;
    const char* b_ = g == 0 ? wqb : (g == 1 ? xb : xb + 16 * D_ * 2);
    const unsigned d_ = g == 0 ? lb + 16384 + m * 1024 : lb + m * 4096 + (g - 1) * 2048;
    gldsa16(b_, g == 0 ? wqo : xo0, d_);
    gldsa16(b_, g == 0 ? wqo + 64 : xo1, d_ + (g == 0 ? 4096 : 1024));
    if (g == 0) gldsa16(wqb, vo_, d_ + 8192);
  }
  if (w >= 8) {
    __builtin_amdgcn_s_sleep(SIDE_DELAY);
    const unsigned sl = lb + K1_SIDE + m * 6144;
    {
      const int u = min(u0, 959);
      const bool is1 = u < 704;
      const int uu = is1 ? u : u - 704;
      const int kb = is1 ? uu / 22 : uu >> 3, pr = is1 ? uu % 22 : uu & 7;
      const int ld = is1 ? F_ : D_;
      const u16* src = is1 ? w1 + (size_t)kb * 16 * F_ : wo + (size_t)kb * 16 * D_;
      const unsigned po = (unsigned)((lane >> 3) * ld + min(64 * pr + (lane & 7) * 8, ld - 8)) * 2u;
      gldsa16(src, po, sl);
      gldsa16(src + 8 * ld, po, sl + 1024);
    }
    gldsa16(x + (size_t)tok * D_, lane * 16, sl + 4096);
    gldsa16(x + (size_t)tok * D_, lane * 16 + 1024, sl + 5120);
    gldsa16(gw, lane * 16, lb + K1_SIDE + 24576 + m * 1024);
    if (bid == 0 && w == 8 && lane < 40) ((unsigned*)(ws + WS_CNT))[lane * 32] = 0;
    const int sstar = ((j >> 1) - kq0) & 7;
    const char* xsrc = lds + (sstar & 3) * K1_ST + m * 4096 + r * 128;
    const char* sp = lds + K1_SIDE + m * 6144;
    WAIT_V(5);
    int job = -1;
#pragma unroll 1
    for (int s = 0; s < 8 || job < 2; ++s) {
      if (s < 8) {
        asm volatile("s_waitcnt lgkmcnt(0)" ::: "memory");
        BAR();
      } else if (job < 0) {
        WAIT_V(0);
        job = 0;
      }
      if (job < 0) {
        const unsigned ib = __builtin_amdgcn_s_getreg(7 | (31 << 11));
        if (((ib & 15u) | ((ib >> 18) & 48u)) == 0) { WAIT_V(0); job = 0; }
      }
      if (s == sstar) {
        const u32x4 xc0 = *(const u32x4*)(xsrc + (((4 * (j & 1) + h) ^ sw) * 16));
        const u32x4 xc1 = *(const u32x4*)(xsrc + (((4 * (j & 1) + 2 + h) ^ sw) * 16));
        WST16(rs, WS_XF + ((4 * (j >> 1) + 2 * (j & 1)) * 64 + tile) * 1024 + lane * 16, xc0);
        WST16(rs, WS_XF + ((4 * (j >> 1) + 2 * (j & 1) + 1) * 64 + tile) * 1024 + lane * 16, xc1);
      }
      if (job == 0) {
        const int c = 0, u = u0;
        if (u < 960) {
          const bool is1 = u < 704;
          const int uu = is1 ? u : u - 704;
          const int kb = is1 ? uu / 22 : uu >> 3, pr = is1 ? uu % 22 : uu & 7;
          const char* pp = sp + c * 2048 + ((is1 ? 8 : 4) * h + q4) * 128 + grp * 32 + p4 * 8;
          const int hi_off = is1 ? 512 : 1024;
#pragma unroll
          for (int n = 0; n < 2; ++n) {
            const s16x4 lo = ldtr(pp + n * 64), hi = ldtr(pp + n * 64 + hi_off);
            const bf16x8 f = __builtin_shufflevector(lo, hi, 0, 1, 2, 3, 4, 5, 6, 7);
            const int blk = 2 * pr + n;
            const unsigned dst = is1 ? WS_W1P + (kb * 43 + blk) * 1024 : WS_WOP + (kb * 16 + blk) * 1024;
            if (!is1 || blk < 43) WST16(rs, dst + lane * 16, f);
          }
        }
      }
      if (job == 1) {
        const u32x4 gg = *(const u32x4*)(lds + K1_SIDE + 24576 + m * 1024 + lane * 16);
#pragma unroll
        for (int t = 0; t < 2; ++t) {
          const u32x4 gx = *(const u32x4*)(sp + 4096 + t * 1024 + lane * 16);
          float sm = 0.f;
#pragma unroll
          for (int e = 0; e < 4; ++e) {
            sm += rbf(bf_lo(gx[e]) * bf_lo(gg[e]));
            sm += rbf(bf_hi(gx[e]) * bf_hi(gg[e]));
          }
          sm = row16_sum(sm);
          sm = (rdl(sm, 0) + rdl(sm, 16)) + (rdl(sm, 32) + rdl(sm, 48));
          sm = rbf(sm);
          const float g = rbf(__builtin_amdgcn_rcpf(1.f + __builtin_amdgcn_exp2f(-sm * 1.4426950408889634f)));
          if (lane == 0) ((float*)(ws + WS_GATE))[tok + t] = g;
        }
      }
      if (job >= 0) ++job;
    }
    return;
  }
  if (w >= 4) {
    int kq = (kq0 + 1) & 7;
    { const int dk_ = kq ? 128 : -896; xb += dk_; wqb += dk_ * 1536; }
    unsigned xl = lb + K1_ST + m * 4096, wl = lb + K1_ST + 16384 + m * 1024;
#define STAGE()                                                                                   \
  do {                                                                                            \
    gldsa16(wqb, wqo, wl);                                                                        \
    gldsa16(wqb, wqo + 64, wl + 4096);                                                            \
    gldsa16(wqb, vo_, wl + 8192);                                                                 \
    gldsa16(xb, xo0, xl);                                                                         \
    gldsa16(xb, xo1, xl + 1024);                                                                  \
    gldsa16(xb + 16 * D_ * 2, xo0, xl + 2048);                                                    \
    gldsa16(xb + 16 * D_ * 2, xo1, xl + 3072);                                                    \
    kq = (kq + 1) & 7;                                                                            \
    { const int dk_ = kq ? 128 : -896; xb += dk_; wqb += dk_ * 1536; }                            \
    const bool wrap_ = xl + K1_ST >= lb + 4 * K1_ST;                                              \
    xl += wrap_ ? 0u - 3u * K1_ST : K1_ST; wl += wrap_ ? 0u - 3u * K1_ST : K1_ST;                 \
  } while (0)
    STAGE();
    WAIT_V(7); BAR();
    STAGE();
    WAIT_V(7); BAR();
    STAGE();
#pragma unroll 1
    for (int kt = 2; kt < 6; ++kt) {
      STAGE();
      WAIT_V(14); BAR();
    }
#undef STAGE
    WAIT_V(7); BAR();
    WAIT_V(0); BAR();
    return;
  }

  f32x16 acc[3];
#pragma unroll
  for (int b = 0; b < 3; ++b)
#pragma unroll
    for (int i = 0; i < 16; ++i) acc[b][i] = 0.f;
  {
    const unsigned xr0 = lb + m * 4096 + r * 128;
    unsigned xa0 = xr0 + ((0 + h) ^ sw) * 16, xa1 = xr0 + ((2 + h) ^ sw) * 16, xa2 = xr0 + ((4 + h) ^ sw) * 16,
             xa3 = xr0 + ((6 + h) ^ sw) * 16;
    unsigned wa = lb + 16384 + (8 * h + q4) * 64 + grp * 32 + p4 * 8;
    unsigned kt = 0, t0;
    asm volatile(K1_LOOP_ASM
                 : "+{v[0:15]}"(acc[0]), "+{v[16:31]}"(acc[1]), "+{v[32:47]}"(acc[2]), "+{v80}"(xa0),
                   "+{v81}"(xa1), "+{v82}"(xa2), "+{v83}"(xa3), "+{v84}"(wa), [kt] "+s"(kt), [t0] "=&s"(t0)
                 : [cst] "s"((unsigned)K1_ST), [cmw] "s"(0u - 3u * K1_ST)
                 : "memory", "scc", "v48", "v49", "v50", "v51", "v52", "v53", "v54", "v55", "v56", "v57",
                   "v58", "v59", "v60", "v61", "v62", "v63", "v64", "v65", "v66", "v67", "v68", "v69", "v70",
                   "v71", "v72", "v73", "v74", "v75", "v76", "v77", "v78", "v79");
  }
#pragma unroll
  for (int b = 0; b < 2; ++b) {
    const int c0 = 64 * j + 32 * b;
    const bool isq = c0 < 512;
    const float sc = isq ? 0.36067376022224085f : 1.0f;
    const int head0 = (c0 & 511) >> 4;
    const unsigned dst = isq ? WS_Q : WS_K;
#pragma unroll
    for (int hh = 0; hh < 2; ++hh) {
      u32x4 o;
#pragma unroll
      for (int e = 0; e < 4; ++e) o[e] = pk2(acc[b][8 * hh + 2 * e] * sc, acc[b][8 * hh + 2 * e + 1] * sc);
      WST16(rs, dst + (((head0 + hh) * 64 + tile) * 64 + lane) * 16, o);
      if (!isq) {
        float ss = 0.f;
#pragma unroll
        for (int e = 0; e < 4; ++e) {
          const float a = bf_lo(o[e]), c = bf_hi(o[e]);
          ss = __builtin_fmaf(a, a, ss);
          ss = __builtin_fmaf(c, c, ss);
        }
        ss = row16_max(add_half(ss));
        ss = fmaxf(rdl(ss, 0), rdl(ss, 16));
        if (lane == 0) ((float*)(ws + WS_KMAX))[(head0 + hh) * 64 + tile] = ss;
      }
    }
  }
  {
    const int head = 2 * j + (r >> 4), d = r & 15;
#pragma unroll
    for (int s = 0; s < 2; ++s) {
      u32x4 o;
#pragma unroll
      for (int e = 0; e < 4; ++e) o[e] = pk2(acc[2][8 * s + 2 * e], acc[2][8 * s + 2 * e + 1]);
      WST16(rs, WS_V + (head * 64 + tile) * 1024 + s * 512 + h * 256 + d * 16, o);
    }
  }
}

#define ATTN_LOOP_ASM \
  "1:\n\t" \
  "v_exp_f32 v32, v32\n\t" \
  "v_exp_f32 v33, v33\n\t" \
  "v_exp_f32 v34, v34\n\t" \
  "v_cvt_pk_bf16_f32 v88, v32, v33\n\t" \
  "v_mfma_f32_32x32x16_bf16 v[0:15], v[84:87], v[92:95], v[0:15]\n\t" \
  "s_mov_b64 exec, %[mask]\n\t" \
  "ds_read_b128 v[80:83], v97 offset:1024\n\t" \
  "ds_read_b128 v[84:87], v97 offset:1536\n\t" \
  "s_mov_b64 exec, -1\n\t" \
  "v_exp_f32 v35, v35\n\t" \
  "v_exp_f32 v36, v36\n\t" \
  "v_cvt_pk_bf16_f32 v89, v34, v35\n\t" \
  "v_exp_f32 v37, v37\n\t" \
  "v_exp_f32 v38, v38\n\t" \
  "v_cvt_pk_bf16_f32 v90, v36, v37\n\t" \
  "v_exp_f32 v39, v39\n\t" \
  "v_exp_f32 v40, v40\n\t" \
  "v_cvt_pk_bf16_f32 v91, v38, v39\n\t" \
  "v_exp_f32 v41, v41\n\t" \
  "v_exp_f32 v42, v42\n\t" \
  "v_mfma_f32_32x32x16_bf16 v[0:15], v[72:75], v[88:91], v[0:15]\n\t" \
  "v_cvt_pk_bf16_f32 v92, v40, v41\n\t" \
  "v_exp_f32 v43, v43\n\t" \
  "v_exp_f32 v44, v44\n\t" \
  "v_cvt_pk_bf16_f32 v93, v42, v43\n\t" \
  "v_exp_f32 v45, v45\n\t" \
  "v_exp_f32 v46, v46\n\t" \
  "v_cvt_pk_bf16_f32 v94, v44, v45\n\t" \
  "v_exp_f32 v47, v47\n\t" \
  "s_waitcnt lgkmcnt(0)\n\t" \
  "v_cvt_pk_bf16_f32 v95, v46, v47\n\t" \
  "v_mfma_f32_32x32x16_bf16 v[32:47], v[68:71], v[64:67], v[16:31]\n\t" \
  "ds_read_b128 v[68:71], v96 offset:3072\n\t" \
  "v_exp_f32 v48, v48\n\t" \
  "v_exp_f32 v49, v49\n\t" \
  "v_exp_f32 v50, v50\n\t" \
  "v_cvt_pk_bf16_f32 v88, v48, v49\n\t" \
  "v_mfma_f32_32x32x16_bf16 v[0:15], v[76:79], v[92:95], v[0:15]\n\t" \
  "s_mov_b64 exec, %[mask]\n\t" \
  "ds_read_b128 v[72:75], v97 offset:2048\n\t" \
  "ds_read_b128 v[76:79], v97 offset:2560\n\t" \
  "s_mov_b64 exec, -1\n\t" \
  "v_exp_f32 v51, v51\n\t" \
  "v_exp_f32 v52, v52\n\t" \
  "v_cvt_pk_bf16_f32 v89, v50, v51\n\t" \
  "v_exp_f32 v53, v53\n\t" \
  "v_exp_f32 v54, v54\n\t" \
  "v_cvt_pk_bf16_f32 v90, v52, v53\n\t" \
  "v_exp_f32 v55, v55\n\t" \
  "v_exp_f32 v56, v56\n\t" \
  "v_cvt_pk_bf16_f32 v91, v54, v55\n\t" \
  "v_exp_f32 v57, v57\n\t" \
  "v_exp_f32 v58, v58\n\t" \
  "v_mfma_f32_32x32x16_bf16 v[0:15], v[80:83], v[88:91], v[0:15]\n\t" \
  "v_cvt_pk_bf16_f32 v92, v56, v57\n\t" \
  "v_exp_f32 v59, v59\n\t" \
  "v_exp_f32 v60, v60\n\t" \
  "v_cvt_pk_bf16_f32 v93, v58, v59\n\t" \
  "v_exp_f32 v61, v61\n\t" \
  "v_exp_f32 v62, v62\n\t" \
  "v_cvt_pk_bf16_f32 v94, v60, v61\n\t" \
  "v_exp_f32 v63, v63\n\t" \
  "s_waitcnt lgkmcnt(0)\n\t" \
  "v_cvt_pk_bf16_f32 v95, v62, v63\n\t" \
  "v_mfma_f32_32x32x16_bf16 v[48:63], v[68:71], v[64:67], v[16:31]\n\t" \
  "ds_read_b128 v[68:71], v96 offset:4096\n\t" \
  "v_exp_f32 v32, v32\n\t" \
  "v_exp_f32 v33, v33\n\t" \
  "v_exp_f32 v34, v34\n\t" \
  "v_cvt_pk_bf16_f32 v88, v32, v33\n\t" \
  "v_mfma_f32_32x32x16_bf16 v[0:15], v[84:87], v[92:95], v[0:15]\n\t" \
  "s_mov_b64 exec, %[mask]\n\t" \
  "ds_read_b128 v[80:83], v97 offset:3072\n\t" \
  "ds_read_b128 v[84:87], v97 offset:3584\n\t" \
  "s_mov_b64 exec, -1\n\t" \
  "v_exp_f32 v35, v35\n\t" \
  "v_exp_f32 v36, v36\n\t" \
  "v_cvt_pk_bf16_f32 v89, v34, v35\n\t" \
  "v_exp_f32 v37, v37\n\t" \
  "v_exp_f32 v38, v38\n\t" \
  "v_cvt_pk_bf16_f32 v90, v36, v37\n\t" \
  "v_exp_f32 v39, v39\n\t" \
  "v_exp_f32 v40, v40\n\t" \
  "v_cvt_pk_bf16_f32 v91, v38, v39\n\t" \
  "v_exp_f32 v41, v41\n\t" \
  "v_exp_f32 v42, v42\n\t" \
  "v_mfma_f32_32x32x16_bf16 v[0:15], v[72:75], v[88:91], v[0:15]\n\t" \
  "v_cvt_pk_bf16_f32 v92, v40, v41\n\t" \
  "v_exp_f32 v43, v43\n\t" \
  "v_exp_f32 v44, v44\n\t" \
  "v_cvt_pk_bf16_f32 v93, v42, v43\n\t" \
  "v_exp_f32 v45, v45\n\t" \
  "v_exp_f32 v46, v46\n\t" \
  "v_cvt_pk_bf16_f32 v94, v44, v45\n\t" \
  "v_exp_f32 v47, v47\n\t" \
  "s_waitcnt lgkmcnt(0)\n\t" \
  "v_cvt_pk_bf16_f32 v95, v46, v47\n\t" \
  "v_mfma_f32_32x32x16_bf16 v[32:47], v[68:71], v[64:67], v[16:31]\n\t" \
  "ds_read_b128 v[68:71], v96 offset:5120\n\t" \
  "v_exp_f32 v48, v48\n\t" \
  "v_exp_f32 v49, v49\n\t" \
  "v_exp_f32 v50, v50\n\t" \
  "v_cvt_pk_bf16_f32 v88, v48, v49\n\t" \
  "v_mfma_f32_32x32x16_bf16 v[0:15], v[76:79], v[92:95], v[0:15]\n\t" \
  "s_mov_b64 exec, %[mask]\n\t" \
  "ds_read_b128 v[72:75], v97 offset:4096\n\t" \
  "ds_read_b128 v[76:79], v97 offset:4608\n\t" \
  "s_mov_b64 exec, -1\n\t" \
  "v_exp_f32 v51, v51\n\t" \
  "v_exp_f32 v52, v52\n\t" \
  "v_cvt_pk_bf16_f32 v89, v50, v51\n\t" \
  "v_exp_f32 v53, v53\n\t" \
  "v_exp_f32 v54, v54\n\t" \
  "v_cvt_pk_bf16_f32 v90, v52, v53\n\t" \
  "v_exp_f32 v55, v55\n\t" \
  "v_exp_f32 v56, v56\n\t" \
  "v_cvt_pk_bf16_f32 v91, v54, v55\n\t" \
  "v_exp_f32 v57, v57\n\t" \
  "v_exp_f32 v58, v58\n\t" \
  "v_mfma_f32_32x32x16_bf16 v[0:15], v[80:83], v[88:91], v[0:15]\n\t" \
  "v_cvt_pk_bf16_f32 v92, v56, v57\n\t" \
  "v_exp_f32 v59, v59\n\t" \
  "v_exp_f32 v60, v60\n\t" \
  "v_cvt_pk_bf16_f32 v93, v58, v59\n\t" \
  "v_exp_f32 v61, v61\n\t" \
  "v_exp_f32 v62, v62\n\t" \
  "v_cvt_pk_bf16_f32 v94, v60, v61\n\t" \
  "v_exp_f32 v63, v63\n\t" \
  "s_waitcnt lgkmcnt(0)\n\t" \
  "v_cvt_pk_bf16_f32 v95, v62, v63\n\t" \
  "v_mfma_f32_32x32x16_bf16 v[48:63], v[68:71], v[64:67], v[16:31]\n\t" \
  "ds_read_b128 v[68:71], v96 offset:6144\n\t" \
  "v_add_u32 v96, 0x1000, v96\n\t" \
  "v_add_u32 v97, 0x1000, v97\n\t" \
  "s_sub_u32 %[cnt], %[cnt], 1\n\t" \
  "s_and_b32 %[tmp], %[cnt], 13\n\t" \
  "s_cmp_lg_u32 %[tmp], 12\n\t" \
  "s_cbranch_scc1 2f\n\t" \
  "3:\n\t" \
  "ds_read_b32 v98, v99\n\t" \
  "s_waitcnt lgkmcnt(0)\n\t" \
  "v_readfirstlane_b32 %[tmp], v98\n\t" \
  "s_cmp_ge_u32 %[tmp], 4\n\t" \
  "s_cbranch_scc1 4f\n\t" \
  "s_sleep 1\n\t" \
  "s_branch 3b\n\t" \
  "4:\n\t" \
  "v_add_u32 v99, 4, v99\n\t" \
  "2:\n\t" \
  "s_cmp_lg_u32 %[cnt], 7\n\t" \
  "s_cbranch_scc1 5f\n\t" \
  "v_mov_b32 v98, 1\n\t" \
  "s_mov_b64 exec, 1\n\t" \
  "ds_add_u32 v99, v98\n\t" \
  "s_mov_b64 exec, -1\n\t" \
  "5:\n\t" \
  "s_cmp_lg_u32 %[cnt], 15\n\t" \
  "s_cbranch_scc1 6f\n\t" \
  "v_mov_b32 v98, 1\n\t" \
  "s_waitcnt vmcnt(0)\n\t" \
  "s_mov_b64 exec, 1\n\t" \
  "ds_add_u32 v100, v98\n\t" \
  "s_mov_b64 exec, -1\n\t" \
  "6:\n\t" \
  "s_cmp_lg_u32 %[cnt], 0\n\t" \
  "s_cbranch_scc1 1b\n\t" \
  "s_waitcnt lgkmcnt(0)\n\t" \
  "s_nop 15\n\t" \
  ""

constexpr int L2_FLAG = 131072;
__global__ __launch_bounds__(768) void k2(char* __restrict__ ws, const u16* __restrict__ b1,
                                          const u16* __restrict__ w2, u16* __restrict__ out) {
  __shared__ __attribute__((aligned(16))) char lds[L2_FLAG + 256 + 8192];
  const int bid = blockIdx.x, xcd = bid & 7, idx = bid >> 3;
  const int head = xcd * 4 + (idx & 3), qb = idx >> 2;
  const int tp = xcd >> 1, cp = (xcd & 1) * 4 + (idx & 3);
  const int T0 = qb * 8 + 2 * tp;
  const int tid = threadIdx.x, lane = tid & 63;
  const int w = __builtin_amdgcn_readfirstlane(tid >> 6);
  const int r = lane & 31, h = lane >> 5;
  const unsigned lb = (unsigned)(size_t)(LAS char*)lds;
  const unsigned l16 = lane * 16;
  const auto rs = WS_RSRC(ws);
  char* cnt = ws + WS_CNT;
  char* cntF = cnt + (8 + qb * 4 + tp) * 128;
  {
    const int p = 2 * w, tl = (p >> 3) * 4 + (p & 3);
    const char* src = ws + ((p & 4) ? WS_V : WS_K) + (size_t)(head * 64 + tl) * 1024;
    const unsigned dst = lb + ((p & 4) ? 65536 : 0) + tl * 1024;
    gldsa16(src, l16, dst);
    gldsa16(src + 1024, l16, dst + 1024);
  }
  if (w >= 8) {
    const int L = w - 8;
    {
      const char* ksrc = ws + WS_K + (size_t)(head * 64 + L) * 1024;
      const char* vsrc = ws + WS_V + (size_t)(head * 64 + L) * 1024;
      const unsigned kdst = lb + L * 1024, vdst = lb + 65536 + L * 1024;
      if (tid >= 512 && tid < 520) ((volatile LAS unsigned*)(lds + L2_FLAG))[tid - 512] = 0;
      WAIT_V(0); BAR();
#pragma unroll 1
      for (int c = 3; c < 16; ++c) {
        gldsa16(ksrc + c * 4096, l16, kdst + c * 4096); gldsa16(vsrc + c * 4096, l16, vdst + c * 4096);
      }
    }
    const int pu = bid * 4 + L, puc = min(pu, 687);
    {
      const u16* src = w2 + (size_t)(puc >> 3) * 16 * D_ + (puc & 7) * 64;
      const unsigned po = (unsigned)((lane >> 3) * D_ + (lane & 7) * 8) * 2u;
      gldsa16(src, po, lb + L2_FLAG + 256 + L * 2048);
      gldsa16(src + 8 * D_, po, lb + L2_FLAG + 256 + L * 2048 + 1024);
    }
    {
      const int T = T0 + (L & 1);
      const int f0 = cp + 24 * (L >> 1);
      const int fbc2 = min(f0 + 16, 42);
      const auto rx = __builtin_amdgcn_make_buffer_rsrc((void*)(ws + WS_XF + T * 1024), 0, 31 * 65536 + 1024, 0x00020000);
      const auto rw = __builtin_amdgcn_make_buffer_rsrc((void*)(ws + WS_W1P), 0, 32 * 43 * 1024, 0x00020000);
      const unsigned wv0 = l16 + f0 * 1024, wv1 = l16 + (f0 + 8) * 1024, wv2 = f0 + 16 < 43 ? l16 + (f0 + 16) * 1024 : 0x40000000u;
      constexpr int PD = 6;
      u32x4 fx[PD], fw[PD][3];
#pragma unroll
      for (int i = 0; i < PD; ++i) {
        fx[i] = BLD(rx, l16, i * 65536, 0);
        fw[i][0] = BLD(rw, wv0, i * 44032, 0); fw[i][1] = BLD(rw, wv1, i * 44032, 0); fw[i][2] = BLD(rw, wv2, i * 44032, 0);
      }
      WAIT_V(40);
      add_lds(lds + L2_FLAG, lane);
      WAIT_V(24);
      add_lds(lds + L2_FLAG + 4, lane);
      {
        const char* pp = lds + L2_FLAG + 256 + L * 2048 + (4 * h + ((lane & 15) >> 2)) * 128 + ((lane >> 4) & 1) * 32 + (lane & 3) * 8;
#pragma unroll
        for (int n = 0; n < 2; ++n) {
          const s16x4 lo = ldtr(pp + n * 64), hi = ldtr(pp + n * 64 + 1024);
          const bf16x8 f = __builtin_shufflevector(lo, hi, 0, 1, 2, 3, 4, 5, 6, 7);
          if (pu < 688) WST16(rs, WS_W2P + (pu * 2 + n) * 1024 + l16, f);
        }
      }
      __builtin_amdgcn_s_setprio(FFN1_PRIO);
      f32x16 a[3];
#pragma unroll
      for (int g = 0; g < 4; ++g) {
        const u32x2 b0 = *(const u32x2*)(b1 + 32 * f0 + 8 * g + 4 * h);
        const u32x2 b1_ = *(const u32x2*)(b1 + 32 * (f0 + 8) + 8 * g + 4 * h);
        const u32x2 b2 = *(const u32x2*)(b1 + 32 * fbc2 + 8 * g + 4 * h);
        a[0][4 * g] = bf_lo(b0[0]); a[0][4 * g + 1] = bf_hi(b0[0]); a[0][4 * g + 2] = bf_lo(b0[1]); a[0][4 * g + 3] = bf_hi(b0[1]);
        a[1][4 * g] = bf_lo(b1_[0]); a[1][4 * g + 1] = bf_hi(b1_[0]); a[1][4 * g + 2] = bf_lo(b1_[1]); a[1][4 * g + 3] = bf_hi(b1_[1]);
        a[2][4 * g] = bf_lo(b2[0]); a[2][4 * g + 1] = bf_hi(b2[0]); a[2][4 * g + 2] = bf_lo(b2[1]); a[2][4 * g + 3] = bf_hi(b2[1]);
      }
      __builtin_amdgcn_sched_barrier(0);
      WAIT_V(0);
#pragma unroll 1
      for (int k0 = PD; k0 < 12 + PD; k0 += PD) {
#pragma unroll
        for (int i = 0; i < PD; ++i) {
          a[0] = MFMAU(fw[i][0], fx[i], a[0]);
          a[1] = MFMAU(fw[i][1], fx[i], a[1]);
          a[2] = MFMAU(fw[i][2], fx[i], a[2]);
          fx[i] = BLD(rx, l16, (k0 + i) * 65536, 0);
          fw[i][0] = BLD(rw, wv0, (k0 + i) * 44032, 0); fw[i][1] = BLD(rw, wv1, (k0 + i) * 44032, 0);
          fw[i][2] = BLD(rw, wv2, (k0 + i) * 44032, 0);
          __builtin_amdgcn_sched_barrier(0);
        }
      }
      wait_lds(lds + L2_FLAG + 24, 8);
      if (add_lds(lds + L2_FLAG + 16, lane) == 3 && lane == 0) __hip_atomic_fetch_add((GAS unsigned*)(cnt + xcd * 128), 1u, RLX_AGENT);
      __builtin_amdgcn_sched_barrier(0);
#pragma unroll 1
      for (int k0 = 12 + PD; k0 < 30 + PD; k0 += PD) {
#pragma unroll
        for (int i = 0; i < PD; ++i) {
          a[0] = MFMAU(fw[i][0], fx[i], a[0]);
          a[1] = MFMAU(fw[i][1], fx[i], a[1]);
          a[2] = MFMAU(fw[i][2], fx[i], a[2]);
          fx[i] = BLD(rx, l16, (k0 + i) * 65536, 0);
          fw[i][0] = BLD(rw, wv0, (k0 + i) * 44032, 0); fw[i][1] = BLD(rw, wv1, (k0 + i) * 44032, 0);
          fw[i][2] = BLD(rw, wv2, (k0 + i) * 44032, 0);
          __builtin_amdgcn_sched_barrier(0);
        }
      }
#pragma unroll
      for (int i = 0; i < 2; ++i) {
        a[0] = MFMAU(fw[i][0], fx[i], a[0]);
        a[1] = MFMAU(fw[i][1], fx[i], a[1]);
        a[2] = MFMAU(fw[i][2], fx[i], a[2]);
      }
      const float g1 = rbf(1.f - ((const float*)(ws + WS_GATE))[T * 32 + r]);
      __builtin_amdgcn_s_setprio(MISH_PRIO);
#pragma unroll
      for (int b = 0; b < 3; ++b) {
        if (f0 + 8 * b < 43) {
#pragma unroll
          for (int hh = 0; hh < 2; ++hh) {
            u32x4 o;
#pragma unroll
            for (int gq = 0; gq < 2; ++gq) {
              const int g = 2 * hh + gq;
              const float h0 = a[b][4 * g + 0], h1 = a[b][4 * g + 1], h2 = a[b][4 * g + 2], h3 = a[b][4 * g + 3];
              o[2 * gq] = pk2(mish_g(h0, g1), mish_g(h1, g1));
              o[2 * gq + 1] = pk2(mish_g(h2, g1), mish_g(h3, g1));
            }
            WST16(rs, WS_ACT + ((2 * (f0 + 8 * b) + hh) * 64 + T) * 1024 + l16, o);
          }
        }
      }
      WAIT_V(0);
      if (add_lds(lds + L2_FLAG + 12, lane) == 3 && lane == 0) __hip_atomic_fetch_add((GAS unsigned*)cntF, 1u, RLX_AGENT);
    }
    {
      const int klast = min(22 * L + 21, 85);
      const auto ra = __builtin_amdgcn_make_buffer_rsrc((void*)(ws + WS_ACT + T0 * 1024), 0, klast * 65536 + 2048, 0x00020000);
      const auto rw = __builtin_amdgcn_make_buffer_rsrc((void*)(ws + WS_W2P + cp * 2048), 0, klast * 16384 + 2048, 0x00020000);
      constexpr int PD = 5;
      u32x4 fa[PD][2], fw[PD][2];
      const int kb0 = 22 * L;
      if (L == 0) {
        char* pw = lane < 8 ? cnt + lane * 128 : cntF;
        const unsigned want = lane < 8 ? 32u : 8u;
        wait_global(cntF, 8);
        for (unsigned spins = 0; spins < (1u << 17); ++spins) {
          const unsigned v = __hip_atomic_load((GAS unsigned*)pw, RLX_AGENT);
          if (__all(v >= want)) break;
          __builtin_amdgcn_s_sleep(2);
        }
        asm volatile("" ::: "memory");
        add_lds(lds + L2_FLAG + 20, lane);
      } else {
        wait_lds(lds + L2_FLAG + 20, 1);
      }
      __builtin_amdgcn_s_setprio(FFN2_PRIO);
#pragma unroll
      for (int i = 0; i < PD; ++i) {
        fa[i][0] = BLD(ra, l16, (kb0 + i) * 65536, HO_AUX); fa[i][1] = BLD(ra, l16 + 1024, (kb0 + i) * 65536, HO_AUX);
        fw[i][0] = BLD(rw, l16, (kb0 + i) * 16384, HO_AUX); fw[i][1] = BLD(rw, l16 + 1024, (kb0 + i) * 16384, HO_AUX);
        __builtin_amdgcn_sched_barrier(0);
      }
      f32x16 b[4];
#pragma unroll
      for (int t = 0; t < 4; ++t)
#pragma unroll
        for (int i = 0; i < 16; ++i) b[t][i] = 0.f;
      __builtin_amdgcn_sched_barrier(0);
#pragma unroll 1
      for (int k0 = PD; k0 < 20 + PD; k0 += PD) {
#pragma unroll
        for (int i = 0; i < PD; ++i) {
#pragma unroll
          for (int t = 0; t < 4; ++t) b[t] = MFMAU(fw[i][t >> 1], fa[i][t & 1], b[t]);
          const int kn = kb0 + k0 + i;
          fa[i][0] = BLD(ra, l16, kn * 65536, HO_AUX); fa[i][1] = BLD(ra, l16 + 1024, kn * 65536, HO_AUX);
          fw[i][0] = BLD(rw, l16, kn * 16384, HO_AUX); fw[i][1] = BLD(rw, l16 + 1024, kn * 16384, HO_AUX);
          __builtin_amdgcn_sched_barrier(0);
        }
      }
#pragma unroll
      for (int i = 0; i < 2; ++i)
#pragma unroll
        for (int t = 0; t < 4; ++t) b[t] = MFMAU(fw[i][t >> 1], fa[i][t & 1], b[t]);
      WAIT_V(0);
      BAR();
      char* bs = lds + (L >> 1) * 65536 + 32768 + (L & 1) * 16384 + l16;
#pragma unroll
      for (int t = 0; t < 4; ++t)
#pragma unroll
        for (int q = 0; q < 4; ++q) {
          f32x4 v = {b[t][4 * q], b[t][4 * q + 1], b[t][4 * q + 2], b[t][4 * q + 3]};
          *(f32x4*)(bs + (t & 1) * 8192 + (t >> 1) * 4096 + q * 1024) = v;
        }
      WAIT_V(0);
      BAR();
      BAR();
    }
    return;
  }

  const int qt = qb * 8 + w;
  bf16x8 qf = *(const bf16x8*)(ws + WS_Q + (size_t)(head * 64 + qt) * 1024 + l16);
  float km = ((const float*)(ws + WS_KMAX))[head * 64 + lane];
  const float gq = ((const float*)(ws + WS_GATE))[qt * 32 + r];
  km = row16_max(km);
  km = fmaxf(fmaxf(rdl(km, 0), rdl(km, 16)), fmaxf(rdl(km, 32), rdl(km, 48)));
  float qs = 0.f;
  {
    const u32x4 qu = __builtin_bit_cast(u32x4, qf);
#pragma unroll
    for (int e = 0; e < 4; ++e) {
      const float a = bf_lo(qu[e]), c = bf_hi(qu[e]);
      qs = __builtin_fmaf(a, a, qs);
      qs = __builtin_fmaf(c, c, qs);
    }
    qs = add_half(qs);
  }
  float mb = __builtin_amdgcn_sqrtf(qs * km) * 1.0001f;
  asm volatile("s_waitcnt vmcnt(0)" : "+v"(mb) : "v"(gq) : "memory");
  f32x16 negm, O;
#pragma unroll
  for (int i = 0; i < 16; ++i) { negm[i] = -mb; O[i] = 0.f; }

  const char* kp = lds + lane * 16;
  const char* vp = lds + 65536 + h * 256 + (lane & 15) * 16;
  const bool vact = (lane & 16) == 0;
  const short one = ((lane & 27) == 16) ? (short)0x3F80 : (short)0;
  bf16x8 va0, va1, vb0, vb1;
#pragma unroll
  for (int i = 0; i < 8; ++i) { va0[i] = one; va1[i] = one; vb0[i] = one; vb1[i] = one; }
  u32x4 p0 = {0, 0, 0, 0}, p1 = {0, 0, 0, 0};
  unsigned kaddr = lb + l16, vaddr = lb + 65536 + h * 256 + (lane & 15) * 16;
  const unsigned long long vmask = 0x0000FFFF0000FFFFull;

  {
    const u32x4 ones = {0xFFFFFFFFu, 0xFFFFFFFFu, 0xFFFFFFFFu, 0xFFFFFFFFu};
    WST16(rs, WS_O + (head * 64 + qt) * 1024 + l16, ones);
  }
  BAR();
  f32x16 Sa = MFMA(*(const bf16x8*)(kp), qf, negm);
  f32x16 Sb = MFMA(*(const bf16x8*)(kp + 1024), qf, negm);
  bf16x8 kf = *(const bf16x8*)(kp + 2048);
  if (vact) { va0 = *(const bf16x8*)(vp); va1 = *(const bf16x8*)(vp + 512); }
  {
    unsigned cnt = 16, tmp, fa_ = lb + L2_FLAG;
    asm volatile("s_nop 15\n\t" ATTN_LOOP_ASM
                 : "+{v[0:15]}"(O), "+{v[32:47]}"(Sa), "+{v[48:63]}"(Sb), "+{v[68:71]}"(kf),
                   "+{v[72:75]}"(va0), "+{v[76:79]}"(va1), "+{v[80:83]}"(vb0), "+{v[84:87]}"(vb1),
                   "+{v[88:91]}"(p0), "+{v[92:95]}"(p1), "+{v96}"(kaddr), "+{v97}"(vaddr), [cnt] "+s"(cnt),
                   [tmp] "=&s"(tmp), "+{v99}"(fa_)
                 : "{v[16:31]}"(negm), "{v[64:67]}"(qf), [mask] "s"(vmask), "{v100}"(lb + L2_FLAG + 24)
                 : "memory", "scc", "v98");
  }
  O = MFMA(vb1, __builtin_bit_cast(bf16x8, p1), O);

  float l = O[8];
  if (__any(!(l >= 8.271806125530277e-25f))) {
    float m = -INFINITY;
    f32x16 zero;
#pragma unroll
    for (int i = 0; i < 16; ++i) { zero[i] = 0.f; O[i] = 0.f; }
    const char* kg = ws + WS_K + (size_t)head * 65536 + l16;
    const char* vg = ws + WS_V + (size_t)head * 65536 + h * 256 + (lane & 15) * 16;
#pragma unroll 1
    for (int t = 0; t < 64; ++t) {
      f32x16 Sx = MFMA(*(const bf16x8*)(kg + t * 1024), qf, zero);
      float mx = Sx[0];
#pragma unroll
      for (int i = 1; i < 16; ++i) mx = fmaxf(mx, Sx[i]);
      mx = max_half(mx);
      const float mn = fmaxf(m, mx);
      const float alpha = __builtin_amdgcn_exp2f(m - mn);
      m = mn;
#pragma unroll
      for (int i = 0; i < 16; ++i) { O[i] *= alpha; Sx[i] -= mn; }
      if (vact) { va0 = *(const bf16x8*)(vg + t * 1024); va1 = *(const bf16x8*)(vg + t * 1024 + 512); }
#pragma unroll
      for (int e = 0; e < 4; ++e) {
        p0[e] = pk2(__builtin_amdgcn_exp2f(Sx[2 * e]), __builtin_amdgcn_exp2f(Sx[2 * e + 1]));
        p1[e] = pk2(__builtin_amdgcn_exp2f(Sx[8 + 2 * e]), __builtin_amdgcn_exp2f(Sx[8 + 2 * e + 1]));
      }
      O = MFMAU(va0, p0, O);
      O = MFMAU(va1, p1, O);
    }
    l = O[8];
  }
  {
    const float rl = gq / l;
    u32x4 o;
#pragma unroll
    for (int e = 0; e < 4; ++e) o[e] = pk2(O[2 * e] * rl, O[2 * e + 1] * rl);
    WST16(rs, WS_O + (head * 64 + qt) * 1024 + l16, o);
  }
  if (w < 4) {
    wait_lds(lds + L2_FLAG + 8, 8);
    const char* wsrc = ws + WS_WOP + cp * 2048 + (size_t)w * 8 * 16384;
    const unsigned wdst = lb + (w >> 1) * 65536 + (w & 1) * 16384;
#pragma unroll 1
    for (int i = 0; i < 8; ++i) {
      gldsa16(wsrc + i * 16384, l16, wdst + i * 2048);
      gldsa16(wsrc + i * 16384, l16 + 1024, wdst + i * 2048 + 1024);
    }
    WAIT_V(0);
  }
  BAR();
  {
    const int kq = w >> 1;
    u32x4 fo[8];
#pragma unroll
    for (int i = 0; i < 8; ++i) fo[i] = BLD(rs, l16, WS_O + ((kq * 8 + i) * 64 + T0 + (w & 1)) * 1024, HO_AUX);
    const char* wp = lds + (kq >> 1) * 65536 + (kq & 1) * 16384 + l16;
    char* pp = lds + (kq >> 1) * 65536 + 32768 + (kq & 1) * 16384 + (w & 1) * 8192 + l16;
    BAR();
    f32x16 a0, a1;
#pragma unroll
    for (int q = 0; q < 4; ++q) {
      const f32x4 v0 = *(const f32x4*)(pp + q * 1024), v1 = *(const f32x4*)(pp + 4096 + q * 1024);
#pragma unroll
      for (int e = 0; e < 4; ++e) { a0[4 * q + e] = v0[e]; a1[4 * q + e] = v1[e]; }
    }
#define INCOMPLETE(v) __any(max(max(v[0], v[1]), max(v[2], v[3])) == 0xFFFFFFFFu)
#pragma unroll
    for (int i = 0; i < 8; ++i) {
      if (INCOMPLETE(fo[i])) {
#pragma unroll 1
        for (unsigned spins = 0; spins < (1u << 18); ++spins) {
          asm volatile("" ::: "memory");
#pragma unroll
          for (int i2 = i; i2 < 8; ++i2)
            if (i2 == i || INCOMPLETE(fo[i2]))
              fo[i2] = BLD(rs, l16, WS_O + ((kq * 8 + i2) * 64 + T0 + (w & 1)) * 1024, HO_AUX);
          if (!INCOMPLETE(fo[i])) break;
        }
      }
      a0 = MFMAU(*(const u32x4*)(wp + i * 2048), fo[i], a0);
      a1 = MFMAU(*(const u32x4*)(wp + i * 2048 + 1024), fo[i], a1);
    }
#pragma unroll
    for (int q = 0; q < 4; ++q) {
      f32x4 v0 = {a0[4 * q], a0[4 * q + 1], a0[4 * q + 2], a0[4 * q + 3]};
      f32x4 v1 = {a1[4 * q], a1[4 * q + 1], a1[4 * q + 2], a1[4 * q + 3]};
      *(f32x4*)(pp + q * 1024) = v0;
      *(f32x4*)(pp + 4096 + q * 1024) = v1;
    }
  }
  WAIT_V(0);
  BAR();
  {
    const int t = w & 3, tt = t & 1, n = t >> 1;
    const int tok = (T0 + tt) * 32 + r;
#pragma unroll
    for (int e = 0; e < 2; ++e) {
      const int q = 2 * (w >> 2) + e;
      const char* sp = lds + 32768 + tt * 8192 + n * 4096 + q * 1024 + l16;
      const f32x4 s = (*(const f32x4*)(sp) + *(const f32x4*)(sp + 16384)) +
                      (*(const f32x4*)(sp + 65536) + *(const f32x4*)(sp + 65536 + 16384));
      u32x2 o = {pk2(s[0], s[1]), pk2(s[2], s[3])};
      *(u32x2*)(out + (size_t)tok * D_ + (2 * cp + n) * 32 + 8 * q + 4 * h) = o;
    }
  }
}

extern "C" void kernel_launch(void* const* d_in, const int* in_sizes, int n_in, void* d_out, int out_size,
                              void* d_ws, size_t ws_size, hipStream_t stream) {
  const u16* x = (const u16*)d_in[0];
  const u16* wqkv = (const u16*)d_in[1];
  const u16* wo = (const u16*)d_in[2];
  const u16* w1 = (const u16*)d_in[4];
  const u16* b1 = (const u16*)d_in[5];
  const u16* w2 = (const u16*)d_in[6];
  const u16* gw = (const u16*)d_in[7];
  char* ws = (char*)d_ws;
  k1<<<256, 768, 0, stream>>>(x, wqkv, wo, w1, gw, ws);
  k2<<<256, 768, 0, stream>>>(ws, b1, w2, (u16*)d_out);
}
```
